```python
import torch

def _softmax(x, axis=-1):
    return torch.softmax(x, dim=axis)
def _gelu(x):
    return torch.nn.functional.gelu(x, approximate="tanh")
def _silu(x):
    return torch.nn.functional.silu(x)
def _sigmoid(x):
    return torch.sigmoid(x)
def _layernorm(x, axis=-1, eps=1e-5):
    m = x.mean(dim=axis, keepdim=True)
    v = x.var(dim=axis, keepdim=True, unbiased=False)
    return (x - m) / torch.sqrt(v + eps)
def _scatter_add(tgt, idx, src, axis=0):
    return tgt.clone().index_add_(axis, idx, src)
def _segment_sum(src, seg, n):
    out = torch.zeros((n,) + src.shape[1:], dtype=src.dtype)
    return out.index_add_(0, seg, src)
def _pad(x, pw):
    flat = tuple(v for pair in reversed(pw) for v in pair)
    return torch.nn.functional.pad(x, flat)

kernel_name = 'standalone__mha_block__prompt'

def setup_inputs():

    _w = (lambda a: (lambda t: t.to(torch.bfloat16) if t.is_floating_point() else t)(torch.as_tensor(a)))
    B, S, D, H = 1, 2048, 448, 16
    return {
        "x": _w((lambda *s: torch.randn(*s, dtype=torch.bfloat16) * 0.2)(B, S, D)),
        "wqkv": _w((lambda *s: torch.randn(*s, dtype=torch.bfloat16) * 0.2)(D, 3 * D)),
        "wo": _w((lambda *s: torch.randn(*s, dtype=torch.bfloat16) * 0.05)(D, D)),
    }

def reference(x, wqkv, wo):
    B, S, D = x.shape
    H = 16
    Dh = D // H
    qkv = (lambda x, *d: x.permute(*d))((x @ wqkv).reshape(B, S, 3, H, Dh), 2, 0, 3, 1, 4)
    q, k, v = qkv[0], qkv[1], qkv[2]
    scores = torch.einsum("bhqd,bhkd->bhqk", q, k) * (Dh ** -0.5)
    p = _softmax(scores, axis=-1)
    o = torch.einsum("bhqk,bhkd->bhqd", p, v)
    return (lambda x, *d: x.permute(*d))(o, 0, 2, 1, 3).reshape(B, S, D) @ wo
```

```cpp
#include <hip/hip_runtime.h>
#include <hip/hip_bf16.h>
#include <stdint.h>

typedef short bf16x8 __attribute__((ext_vector_type(8)));
typedef short s16x4 __attribute__((ext_vector_type(4)));
typedef float f32x16 __attribute__((ext_vector_type(16)));
typedef float f32x4 __attribute__((ext_vector_type(4)));
typedef float f32x2 __attribute__((ext_vector_type(2)));
typedef unsigned u32x4 __attribute__((ext_vector_type(4)));
typedef __bf16 bf16x2_t __attribute__((ext_vector_type(2)));
typedef short v4i16_t __attribute__((ext_vector_type(4)));
#define LAS __attribute__((address_space(3)))
#define DI __device__ __forceinline__
#define MFMA(a, b, c) __builtin_amdgcn_mfma_f32_32x32x16_bf16((a), (b), (c), 0, 0, 0)
#define SBAR() __builtin_amdgcn_sched_barrier(0)
#define PIN(x) asm volatile("" : "+v"(x))

constexpr int SEQ = 2048, DM = 448, NH = 16, DH = 28, N3 = 1344;
constexpr float QSCALE = 0.18898223650461362f * 1.4426950408889634f;
constexpr size_t WS_QF = 0;
constexpr size_t WS_KV = 2u << 20;
constexpr size_t WS_OF = 6u << 20;
constexpr size_t WS_KM = 8u << 20;

DI unsigned rfl(unsigned x) { return __builtin_amdgcn_readfirstlane(x); }
DI unsigned cvtpk(float lo, float hi) { f32x2 v = {lo, hi}; bf16x2_t b = __builtin_convertvector(v, bf16x2_t); return __builtin_bit_cast(unsigned, b); }
DI bf16x8 pack8(const f32x16& x, int s) {
    u32x4 p = {cvtpk(x[8 * s], x[8 * s + 1]), cvtpk(x[8 * s + 2], x[8 * s + 3]), cvtpk(x[8 * s + 4], x[8 * s + 5]), cvtpk(x[8 * s + 6], x[8 * s + 7])};
    return __builtin_bit_cast(bf16x8, p);
}
DI s16x4 vtr(LAS const char* p) { return __builtin_bit_cast(s16x4, __builtin_amdgcn_ds_read_tr16_b64_v4i16((LAS v4i16_t*)p)); }
DI bf16x8 cat(s16x4 lo, s16x4 hi) { return __builtin_shufflevector(lo, hi, 0, 1, 2, 3, 4, 5, 6, 7); }
DI float half_lo(float x) { auto r = __builtin_amdgcn_permlane32_swap(__float_as_uint(x), __float_as_uint(x), false, false); return __uint_as_float(r[0]); }
DI float half_hi(float x) { auto r = __builtin_amdgcn_permlane32_swap(__float_as_uint(x), __float_as_uint(x), false, false); return __uint_as_float(r[1]); }
DI void dma16(unsigned lds_dst, unsigned voff, __amdgpu_buffer_rsrc_t rs, unsigned soff) {
    asm volatile("s_mov_b32 m0, %0\n\ts_nop 0\n\tbuffer_load_dwordx4 %1, %2, %3 offen lds" ::"s"(lds_dst), "v"(voff), "s"(rs), "s"(soff) : "m0", "memory");
}
DI u32x4 ld16_asm(unsigned voff, __amdgpu_buffer_rsrc_t rs, unsigned soff) {
    u32x4 r;
    asm volatile("buffer_load_dwordx4 %0, %1, %2, %3 offen" : "=v"(r) : "v"(voff), "s"(rs), "s"(soff) : "memory");
    return r;
}

constexpr int K1_WIMG = 448 * 64;
constexpr int K1_XOFF = 3 * K1_WIMG;
constexpr int K1_LDS = K1_XOFF + 4 * 4 * 4096;

__global__ __launch_bounds__(256, 1) void qkv_kernel(const char* __restrict__ x, const char* __restrict__ wqkv, char* __restrict__ ws) {
    extern __shared__ __attribute__((aligned(16))) char lds[];
    const int tid = threadIdx.x, lane = tid & 63;
    const unsigned w = rfl(tid >> 6);
    const int r = lane & 31, h = lane >> 5;
    const unsigned b = blockIdx.x, xcd = b & 7, idx = b >> 3;
    const unsigned hd = (xcd & 1) * 8 + (idx >> 2), tt = (xcd >> 1) * 4 + (idx & 3);
    const unsigned lds0 = (unsigned)(uintptr_t)lds;
    const __amdgpu_buffer_rsrc_t rsW = __builtin_amdgcn_make_buffer_rsrc((void*)wqkv, (short)0, DM * N3 * 2, 0x00020000);
    const __amdgpu_buffer_rsrc_t rsX = __builtin_amdgcn_make_buffer_rsrc((void*)x, (short)0, SEQ * DM * 2, 0x00020000);
    const unsigned wvo = (16 * w + (lane >> 2)) * (N3 * 2) + (lane & 3) * 16 + hd * 56;
    const unsigned xrow = tt * 128 + w * 32 + (lane >> 3);
    const unsigned xvo0 = xrow * (DM * 2) + (((lane & 7) ^ (lane >> 4)) * 16);
    const unsigned xvo1 = xrow * (DM * 2) + (((lane & 7) ^ (4 + (lane >> 4))) * 16);
    const unsigned xl = lds0 + K1_XOFF + w * 16384;
    u32x4 xr[3][4];
    asm volatile("s_nop 4" ::: "memory");
#pragma unroll
    for (int kb = 0; kb < 7; ++kb) {
#pragma unroll
        for (int ty = 0; ty < 3; ++ty) dma16(lds0 + ty * K1_WIMG + (kb * 64 + 16 * w) * 64, wvo, rsW, kb * 64 * (N3 * 2) + ty * 896);
#pragma unroll
        for (int m = 0; m < 4; ++m) {
            if (kb < 4) dma16(xl + kb * 4096 + m * 1024, (m & 1) ? xvo1 : xvo0, rsX, kb * 128 + m * 8 * (DM * 2));
            else xr[kb - 4][m] = ld16_asm((m & 1) ? xvo1 : xvo0, rsX, kb * 128 + m * 8 * (DM * 2));
        }
    }
    f32x16 aq = {}, ak = {}, av = {};
    const int i16 = lane & 15;
    LAS const char* wb = (LAS const char*)lds + (8 * h + (i16 >> 2)) * 64 + ((lane >> 4) & 1) * 32 + (i16 & 3) * 8;
    LAS const char* xb = (LAS const char*)lds + K1_XOFF + w * 16384 + r * 128;
    LAS char* xw = (LAS char*)lds + K1_XOFF + w * 16384 + lane * 16;
    const int xs = (r >> 1) & 7;
#define K1_STEP(kb, NV)                                                                                                   \
    do {                                                                                                                  \
        if (kb >= 4) {                                                                                                    \
            asm volatile("s_waitcnt vmcnt(" #NV ")" : "+v"(xr[kb >= 4 ? kb - 4 : 0][0]), "+v"(xr[kb >= 4 ? kb - 4 : 0][1]), \
                         "+v"(xr[kb >= 4 ? kb - 4 : 0][2]), "+v"(xr[kb >= 4 ? kb - 4 : 0][3])::"memory");                 \
            _Pragma("unroll") for (int m = 0; m < 4; ++m) *(LAS u32x4*)(xw + (kb & 3) * 4096 + m * 1024) = xr[kb >= 4 ? kb - 4 : 0][m]; \
            asm volatile("s_waitcnt lgkmcnt(0)\n\ts_barrier" ::: "memory");                                             \
        } else {                                                                                                          \
            asm volatile("s_waitcnt vmcnt(" #NV ")\n\ts_barrier" ::: "memory");                                         \
        }                                                                                                                 \
        _Pragma("unroll") for (int ks = 0; ks < 4; ++ks) {                                                                \
            const bf16x8 xf = *(LAS const bf16x8*)(xb + (kb & 3) * 4096 + (((2 * ks + h) ^ xs) * 16));                    \
            const int ro = (kb * 64 + ks * 16) * 64;                                                                      \
            const bf16x8 wq = cat(vtr(wb + ro), vtr(wb + ro + 256));                                                      \
            const bf16x8 wk = cat(vtr(wb + K1_WIMG + ro), vtr(wb + K1_WIMG + ro + 256));                                  \
            const bf16x8 wv = cat(vtr(wb + 2 * K1_WIMG + ro), vtr(wb + 2 * K1_WIMG + ro + 256));                          \
            aq = MFMA(wq, xf, aq);                                                                                        \
            ak = MFMA(wk, xf, ak);                                                                                        \
            av = MFMA(xf, wv, av);                                                                                        \
        }                                                                                                                 \
    } while (0)
    K1_STEP(0, 42); K1_STEP(1, 35); K1_STEP(2, 28); K1_STEP(3, 21); K1_STEP(4, 14); K1_STEP(5, 7); K1_STEP(6, 0);
#undef K1_STEP
    float q2 = 0.f, k2 = 0.f;
#pragma unroll
    for (int i = 0; i < 16; ++i) {
        aq[i] *= QSCALE;
        if (i >= 12) { aq[i] = h ? 0.f : aq[i]; ak[i] = h ? 0.f : ak[i]; }
        q2 += aq[i] * aq[i]; k2 += ak[i] * ak[i];
    }
    q2 = half_lo(q2) + half_hi(q2);
    k2 = half_lo(k2) + half_hi(k2);
    if (h) { aq[12] = __builtin_sqrtf(q2); ak[12] = 1.f; ak[13] = 1.f; ak[14] = 1.f; }
    if (r >= DH) {
#pragma unroll
        for (int i = 0; i < 16; ++i) av[i] = (r == DH) ? 1.f : 0.f;
    }
#pragma unroll
    for (int o = 1; o < 32; o <<= 1) k2 = __builtin_fmaxf(k2, __shfl_xor(k2, o));
    const unsigned t32 = tt * 4 + w;
    char* qf = ws + WS_QF + (size_t)(hd * 64 + t32) * 2048 + lane * 16;
    char* kv = ws + WS_KV + (size_t)(hd * 64 + t32) * 4096 + lane * 16;
    *(bf16x8*)(kv) = pack8(ak, 0);
    *(bf16x8*)(kv + 1024) = pack8(ak, 1);
    *(bf16x8*)(kv + 2048) = pack8(av, 0);
    *(bf16x8*)(kv + 3072) = pack8(av, 1);
    *(bf16x8*)(qf) = pack8(aq, 0);
    *(bf16x8*)(qf + 1024) = pack8(aq, 1);
    if (lane == 0) ((float*)(ws + WS_KM))[hd * 64 + t32] = k2;
}

constexpr float L_MIN = 9.094947017729282e-13f;
DI unsigned bf16_bits(float x) { return cvtpk(x, 0.f) & 0xffffu; }
DI float bf16_val(unsigned b) { return __uint_as_float(b << 16); }

__global__ __launch_bounds__(256, 2) void attn_kernel(char* __restrict__ ws) {
    __shared__ __attribute__((aligned(16))) char lds[4 * 4 * 4096];
    const int tid = threadIdx.x, lane = tid & 63;
    const unsigned w = rfl(tid >> 6);
    const int h = lane >> 5;
    const unsigned b = blockIdx.x;
    const unsigned hd = (b & 7) * 2 + ((b >> 3) & 1), qt = b >> 4;
    const char* kvh = ws + WS_KV + (size_t)hd * (64 * 4096);
    const bf16x8* kvp = (const bf16x8*)(kvh + (size_t)w * (16 * 4096)) + lane;
    const bf16x8* qp = (const bf16x8*)(ws + WS_QF + (size_t)(hd * 64 + qt * 4) * 2048) + lane;

    bf16x8 kA0 = kvp[0], kA1 = kvp[64], vA0 = kvp[128], vA1 = kvp[192];
    bf16x8 q[4][2];
#pragma unroll
    for (int i = 0; i < 4; ++i) { q[i][0] = qp[i * 128]; q[i][1] = qp[i * 128 + 64]; }
    float km = ((const float*)(ws + WS_KM))[hd * 64 + lane];
    bf16x8 kB0 = kvp[256], kB1 = kvp[256 + 64], vB0 = kvp[256 + 128], vB1 = kvp[256 + 192];
#pragma unroll
    for (int o = 1; o < 64; o <<= 1) km = __builtin_fmaxf(km, __shfl_xor(km, o));
    km = -1.03f * __builtin_sqrtf(km);
#pragma unroll
    for (int i = 0; i < 4; ++i) {
        const float ref = bf16_val((unsigned short)q[i][1][4]) * km;
        q[i][1][4] = h ? (short)bf16_bits(ref) : q[i][1][4];
    }
    const f32x16 zero = {};
    f32x16 o[4] = {zero, zero, zero, zero};
    f32x16 sA, sB;
    u32x4 pA0, pA1, pB0, pB1;
#define EX4(SC, i) do { SC[i] = __builtin_amdgcn_exp2f(SC[i]); SC[i + 1] = __builtin_amdgcn_exp2f(SC[i + 1]); SC[i + 2] = __builtin_amdgcn_exp2f(SC[i + 2]); SC[i + 3] = __builtin_amdgcn_exp2f(SC[i + 3]); } while (0)
#define SLOT(SC, SN, PC0, PC1, PP0, PP1, K0, K1, QN, V0, V1, OP, DOQK, DOPV)                             \
    do {                                                                                                  \
        if (DOQK) SN = MFMA(K0, QN[0], zero);                                                             \
        EX4(SC, 0); PIN(SC); SBAR();                                                                      \
        if (DOPV) OP = MFMA(V0, __builtin_bit_cast(bf16x8, PP0), OP);                                     \
        EX4(SC, 4); PC0[0] = cvtpk(SC[0], SC[1]); PC0[1] = cvtpk(SC[2], SC[3]); PIN(SC); PIN(PC0); SBAR(); \
        if (DOQK) SN = MFMA(K1, QN[1], SN);                                                               \
        EX4(SC, 8); PC0[2] = cvtpk(SC[4], SC[5]); PC0[3] = cvtpk(SC[6], SC[7]); PIN(SC); PIN(PC0); SBAR(); \
        if (DOPV) OP = MFMA(V1, __builtin_bit_cast(bf16x8, PP1), OP);                                     \
        EX4(SC, 12); PC1[0] = cvtpk(SC[8], SC[9]); PC1[1] = cvtpk(SC[10], SC[11]); PIN(SC); PIN(PC1); SBAR(); \
        PC1[2] = cvtpk(SC[12], SC[13]); PC1[3] = cvtpk(SC[14], SC[15]); PIN(PC1); SBAR();                 \
    } while (0)
    sA = MFMA(kA0, q[0][0], zero); sA = MFMA(kA1, q[0][1], sA);
    pB0 = (u32x4){0, 0, 0, 0}; pB1 = pB0;
#pragma unroll 1
    for (int t = 0; t < 16; t += 2) {
        const bf16x8* nx = kvp + (size_t)((t + 2) & 15) * 256;
        SLOT(sA, sB, pA0, pA1, pB0, pB1, kA0, kA1, q[1], vB0, vB1, o[3], 1, 1);
        if (t) { vB0 = kvp[(size_t)(t + 1) * 256 + 128]; vB1 = kvp[(size_t)(t + 1) * 256 + 192]; } SBAR();
        SLOT(sB, sA, pB0, pB1, pA0, pA1, kA0, kA1, q[2], vA0, vA1, o[0], 1, 1);
        SLOT(sA, sB, pA0, pA1, pB0, pB1, kA0, kA1, q[3], vA0, vA1, o[1], 1, 1);
        kA0 = nx[0]; kA1 = nx[64]; SBAR();
        SLOT(sB, sA, pB0, pB1, pA0, pA1, kB0, kB1, q[0], vA0, vA1, o[2], 1, 1);
        SLOT(sA, sB, pA0, pA1, pB0, pB1, kB0, kB1, q[1], vA0, vA1, o[3], 1, 1);
        vA0 = nx[128]; vA1 = nx[192]; SBAR();
        SLOT(sB, sA, pB0, pB1, pA0, pA1, kB0, kB1, q[2], vB0, vB1, o[0], 1, 1);
        SLOT(sA, sB, pA0, pA1, pB0, pB1, kB0, kB1, q[3], vB0, vB1, o[1], 1, 1);
        kB0 = nx[256]; kB1 = nx[256 + 64]; SBAR();
        SLOT(sB, sA, pB0, pB1, pA0, pA1, kA0, kA1, q[0], vB0, vB1, o[2], 1, 1);
    }
    o[3] = MFMA(vB0, __builtin_bit_cast(bf16x8, pB0), o[3]);
    o[3] = MFMA(vB1, __builtin_bit_cast(bf16x8, pB1), o[3]);
#undef SLOT
#pragma unroll
    for (int i = 0; i < 4; ++i) {
        if (i != (int)w) {
#pragma unroll
            for (int g = 0; g < 4; ++g)
                *(f32x4*)(lds + (w * 4 + i) * 4096 + g * 1024 + lane * 16) = (f32x4){o[i][4 * g], o[i][4 * g + 1], o[i][4 * g + 2], o[i][4 * g + 3]};
        }
    }
    f32x16 acc = (w == 0) ? o[0] : (w == 1) ? o[1] : (w == 2) ? o[2] : o[3];
    __syncthreads();
#pragma unroll
    for (int s = 1; s < 4; ++s) {
        const unsigned src = (w + s) & 3;
#pragma unroll
        for (int g = 0; g < 4; ++g) {
            const f32x4 v = *(const f32x4*)(lds + (src * 4 + w) * 4096 + g * 1024 + lane * 16);
            acc[4 * g] += v[0]; acc[4 * g + 1] += v[1]; acc[4 * g + 2] += v[2]; acc[4 * g + 3] += v[3];
        }
    }
    float l = half_hi(acc[12]);
    if (__builtin_expect(__any(!(l >= L_MIN)), 0)) {
        const bf16x8* kall = (const bf16x8*)kvh + lane;
        bf16x8 q0 = qp[w * 128], q1 = qp[w * 128 + 64];
        if (h) q1[4] = 0;
        float m = -__builtin_inff();
#pragma unroll 1
        for (int t = 0; t < 64; ++t) {
            f32x16 s = MFMA(kall[t * 256], q0, zero);
            s = MFMA(kall[t * 256 + 64], q1, s);
#pragma unroll
            for (int i = 0; i < 16; ++i) m = __builtin_fmaxf(m, s[i]);
        }
        m = __builtin_fmaxf(half_lo(m), half_hi(m));
        const unsigned m0 = bf16_bits(-m); const float r1 = -m - bf16_val(m0);
        const unsigned m1 = bf16_bits(r1); const float r2 = r1 - bf16_val(m1);
        const unsigned m2 = bf16_bits(r2);
        if (h) { q1[4] = (short)m0; q1[5] = (short)m1; q1[6] = (short)m2; }
        acc = zero;
#pragma unroll 1
        for (int t = 0; t < 64; ++t) {
            f32x16 s = MFMA(kall[t * 256], q0, zero);
            s = MFMA(kall[t * 256 + 64], q1, s);
#pragma unroll
            for (int i = 0; i < 16; ++i) s[i] = __builtin_amdgcn_exp2f(s[i]);
            acc = MFMA(kall[t * 256 + 128], pack8(s, 0), acc);
            acc = MFMA(kall[t * 256 + 192], pack8(s, 1), acc);
        }
        l = half_hi(acc[12]);
    }
    const float rl = 1.0f / l;
#pragma unroll
    for (int i = 0; i < 16; ++i) acc[i] = (i >= 12 && h) ? 0.f : acc[i] * rl;
    char* of = ws + WS_OF + (size_t)((qt * 4 + w) * NH + hd) * 2048 + lane * 16;
    *(bf16x8*)(of) = pack8(acc, 0);
    *(bf16x8*)(of + 1024) = pack8(acc, 1);
}

constexpr int K3_LDS = 480 * 128;
__global__ __launch_bounds__(256, 1) void proj_kernel(const char* __restrict__ ws, const char* __restrict__ wo, char* __restrict__ out) {
    __shared__ __attribute__((aligned(16))) char lds[K3_LDS];
    const int tid = threadIdx.x, lane = tid & 63;
    const unsigned w = rfl(tid >> 6);
    const int r = lane & 31, h = lane >> 5;
    const unsigned b = blockIdx.x, rb = b / 7, cb = b % 7;
    const unsigned lds0 = (unsigned)(uintptr_t)lds;
    const __amdgpu_buffer_rsrc_t rsW = __builtin_amdgcn_make_buffer_rsrc((void*)wo, (short)0, DM * DM * 2, 0x00020000);
    const unsigned wvo = (8 * w + (lane >> 3)) * (DM * 2) + cb * 128 + (((lane & 7) ^ (((lane >> 4) & 1) << 2)) * 16);
    asm volatile("s_nop 4" ::: "memory");
#pragma unroll
    for (int m = 0; m < 15; ++m) dma16(lds0 + (m * 32 + 8 * w) * 128, wvo, rsW, m * 32 * (DM * 2));
    const unsigned qt = rb * 2 + (w >> 1), cw = w & 1;
    const bf16x8* op = (const bf16x8*)(ws + WS_OF + (size_t)qt * (NH * 2048)) + lane;
    bf16x8 of[32];
#pragma unroll
    for (int i = 0; i < 32; ++i) of[i] = op[i * 64];
    asm volatile("s_waitcnt vmcnt(32)\n\ts_barrier" ::: "memory");
    const int i16 = lane & 15, q4 = i16 >> 2;
    LAS const char* wb = (LAS const char*)lds + (4 * h + q4) * 128 + ((cw ^ (q4 >> 1)) * 64) + ((lane >> 4) & 1) * 32 + (i16 & 3) * 8;
    f32x16 acc = {};
#pragma unroll
    for (int i = 0; i < 32; ++i) {
        const int ro = ((i >> 1) * DH + (i & 1) * 16) * 128;
        const bf16x8 wf = cat(vtr(wb + ro), vtr(wb + ro + 8 * 128));
        acc = MFMA(wf, of[i], acc);
    }
    char* orow = out + (size_t)(qt * 32 + r) * (DM * 2) + cb * 128 + cw * 64 + h * 16;
#pragma unroll
    for (int g = 0; g < 4; g += 2) {
        unsigned a0 = cvtpk(acc[4 * g], acc[4 * g + 1]), a1 = cvtpk(acc[4 * g + 2], acc[4 * g + 3]);
        unsigned b0 = cvtpk(acc[4 * g + 4], acc[4 * g + 5]), b1 = cvtpk(acc[4 * g + 6], acc[4 * g + 7]);
        auto s0 = __builtin_amdgcn_permlane32_swap(a0, b0, false, false);
        auto s1 = __builtin_amdgcn_permlane32_swap(a1, b1, false, false);
        *(u32x4*)(orow + g * 16) = (u32x4){s0[0], s1[0], s0[1], s1[1]};
    }
}

extern "C" void kernel_launch(void* const* d_in, const int* in_sizes, int n_in, void* d_out, int out_size, void* d_ws, size_t ws_size,
                              hipStream_t stream) {
    const char* x = (const char*)d_in[0];
    const char* wqkv = (const char*)d_in[1];
    const char* wo = (const char*)d_in[2];
    char* ws = (char*)d_ws;
    (void)hipFuncSetAttribute((const void*)qkv_kernel, hipFuncAttributeMaxDynamicSharedMemorySize, K1_LDS);
    qkv_kernel<<<256, 256, K1_LDS, stream>>>(x, wqkv, ws);
    attn_kernel<<<256, 256, 0, stream>>>(ws);
    proj_kernel<<<224, 256, 0, stream>>>(ws, wo, (char*)d_out);
}
```

```cpp
#include <hip/hip_runtime.h>
#include <hip/hip_bf16.h>
#include <stdint.h>

typedef short bf16x8 __attribute__((ext_vector_type(8)));
typedef short s16x4 __attribute__((ext_vector_type(4)));
typedef float f32x16 __attribute__((ext_vector_type(16)));
typedef float f32x4 __attribute__((ext_vector_type(4)));
typedef float f32x2 __attribute__((ext_vector_type(2)));
typedef unsigned u32x4 __attribute__((ext_vector_type(4)));
typedef __bf16 bf16x2_t __attribute__((ext_vector_type(2)));
typedef short v4i16_t __attribute__((ext_vector_type(4)));
#define LAS __attribute__((address_space(3)))
#define DI __device__ __forceinline__
#define MFMA(a, b, c) __builtin_amdgcn_mfma_f32_32x32x16_bf16((a), (b), (c), 0, 0, 0)
#define SBAR() __builtin_amdgcn_sched_barrier(0)
#define PIN(x) asm volatile("" : "+v"(x))

constexpr int SEQ = 2048, DM = 448, NH = 16, DH = 28, N3 = 1344;
constexpr float QSCALE = 0.18898223650461362f * 1.4426950408889634f;
constexpr size_t WS_QF = 0;
constexpr size_t WS_KV = 2u << 20;
constexpr size_t WS_OF = 6u << 20;
constexpr size_t WS_KM = 8u << 20;

#define STAMP_DECL
#define STAMP(i)
#define STAMP_FLUSH(kid)
DI unsigned rfl(unsigned x) { return __builtin_amdgcn_readfirstlane(x); }
DI unsigned cvtpk(float lo, float hi) { f32x2 v = {lo, hi}; bf16x2_t b = __builtin_convertvector(v, bf16x2_t); return __builtin_bit_cast(unsigned, b); }
DI bf16x8 pack8(const f32x16& x, int s) {
    u32x4 p = {cvtpk(x[8 * s], x[8 * s + 1]), cvtpk(x[8 * s + 2], x[8 * s + 3]), cvtpk(x[8 * s + 4], x[8 * s + 5]), cvtpk(x[8 * s + 6], x[8 * s + 7])};
    return __builtin_bit_cast(bf16x8, p);
}
DI s16x4 vtr(LAS const char* p) { return __builtin_bit_cast(s16x4, __builtin_amdgcn_ds_read_tr16_b64_v4i16((LAS v4i16_t*)p)); }
DI bf16x8 cat(s16x4 lo, s16x4 hi) { return __builtin_shufflevector(lo, hi, 0, 1, 2, 3, 4, 5, 6, 7); }
DI float half_lo(float x) { auto r = __builtin_amdgcn_permlane32_swap(__float_as_uint(x), __float_as_uint(x), false, false); return __uint_as_float(r[0]); }
DI float half_hi(float x) { auto r = __builtin_amdgcn_permlane32_swap(__float_as_uint(x), __float_as_uint(x), false, false); return __uint_as_float(r[1]); }
DI void dma16(unsigned lds_dst, unsigned voff, __amdgpu_buffer_rsrc_t rs, unsigned soff) {
    asm volatile("s_mov_b32 m0, %0\n\ts_nop 0\n\tbuffer_load_dwordx4 %1, %2, %3 offen lds" ::"s"(lds_dst), "v"(voff), "s"(rs), "s"(soff) : "m0", "memory");
}
DI u32x4 ld16_asm(unsigned voff, __amdgpu_buffer_rsrc_t rs, unsigned soff) {
    u32x4 r;
    asm volatile("buffer_load_dwordx4 %0, %1, %2, %3 offen" : "=v"(r) : "v"(voff), "s"(rs), "s"(soff) : "memory");
    return r;
}

constexpr int K1_WIMG = 448 * 64;
constexpr int K1_XOFF = 3 * K1_WIMG;
constexpr int K1_LDS = K1_XOFF + 4 * 4 * 4096;

__global__ __launch_bounds__(256, 1) void qkv_kernel(const char* __restrict__ x, const char* __restrict__ wqkv, char* __restrict__ ws) {
    extern __shared__ __attribute__((aligned(16))) char lds[];
    STAMP_DECL STAMP(0);
    const int tid = threadIdx.x, lane = tid & 63;
    const unsigned w = rfl(tid >> 6);
    const int r = lane & 31, h = lane >> 5;
    const unsigned b = blockIdx.x, xcd = b & 7, idx = b >> 3;
    const unsigned hd = (xcd & 1) * 8 + (idx >> 2), tt = (xcd >> 1) * 4 + (idx & 3);
    const unsigned lds0 = (unsigned)(uintptr_t)lds;
    const __amdgpu_buffer_rsrc_t rsW = __builtin_amdgcn_make_buffer_rsrc((void*)wqkv, (short)0, DM * N3 * 2, 0x00020000);
    const __amdgpu_buffer_rsrc_t rsX = __builtin_amdgcn_make_buffer_rsrc((void*)x, (short)0, SEQ * DM * 2, 0x00020000);
    const unsigned wvo = (16 * w + (lane >> 2)) * (N3 * 2) + (lane & 3) * 16 + hd * 56;
    const unsigned xrow = tt * 128 + w * 32 + (lane >> 3);
    const unsigned xvo0 = xrow * (DM * 2) + (((lane & 7) ^ (lane >> 4)) * 16);
    const unsigned xvo1 = xrow * (DM * 2) + (((lane & 7) ^ (4 + (lane >> 4))) * 16);
    const unsigned xl = lds0 + K1_XOFF + w * 16384;
    u32x4 xr[3][4];
    asm volatile("s_nop 4" ::: "memory");
#pragma unroll
    for (int kb = 0; kb < 7; ++kb) {
#pragma unroll
        for (int ty = 0; ty < 3; ++ty) dma16(lds0 + ty * K1_WIMG + (kb * 64 + 16 * w) * 64, wvo, rsW, kb * 64 * (N3 * 2) + ty * 896);
#pragma unroll
        for (int m = 0; m < 4; ++m) {
            if (kb < 4) dma16(xl + kb * 4096 + m * 1024, (m & 1) ? xvo1 : xvo0, rsX, kb * 128 + m * 8 * (DM * 2));
            else xr[kb - 4][m] = ld16_asm((m & 1) ? xvo1 : xvo0, rsX, kb * 128 + m * 8 * (DM * 2));
        }
    }
    STAMP(1);
    f32x16 aq = {}, ak = {}, av = {};
    const int i16 = lane & 15;
    LAS const char* wb = (LAS const char*)lds + (8 * h + (i16 >> 2)) * 64 + ((lane >> 4) & 1) * 32 + (i16 & 3) * 8;
    LAS const char* xb = (LAS const char*)lds + K1_XOFF + w * 16384 + r * 128;
    LAS char* xw = (LAS char*)lds + K1_XOFF + w * 16384 + lane * 16;
    const int xs = (r >> 1) & 7;
#define K1_STEP(kb, NV)                                                                                                   \
    do {                                                                                                                  \
        if (kb >= 4) {                                                                                                    \
            asm volatile("s_waitcnt vmcnt(" #NV ")" : "+v"(xr[kb >= 4 ? kb - 4 : 0][0]), "+v"(xr[kb >= 4 ? kb - 4 : 0][1]), \
                         "+v"(xr[kb >= 4 ? kb - 4 : 0][2]), "+v"(xr[kb >= 4 ? kb - 4 : 0][3])::"memory");                 \
            _Pragma("unroll") for (int m = 0; m < 4; ++m) *(LAS u32x4*)(xw + (kb & 3) * 4096 + m * 1024) = xr[kb >= 4 ? kb - 4 : 0][m]; \
            asm volatile("s_waitcnt lgkmcnt(0)\n\ts_barrier" ::: "memory");                                             \
        } else {                                                                                                          \
            asm volatile("s_waitcnt vmcnt(" #NV ")\n\ts_barrier" ::: "memory");                                         \
        }                                                                                                                 \
        _Pragma("unroll") for (int ks = 0; ks < 4; ++ks) {                                                                \
            const bf16x8 xf = *(LAS const bf16x8*)(xb + (kb & 3) * 4096 + (((2 * ks + h) ^ xs) * 16));                    \
            const int ro = (kb * 64 + ks * 16) * 64;                                                                      \
            const bf16x8 wq = cat(vtr(wb + ro), vtr(wb + ro + 256));                                                      \
            const bf16x8 wk = cat(vtr(wb + K1_WIMG + ro), vtr(wb + K1_WIMG + ro + 256));                                  \
            const bf16x8 wv = cat(vtr(wb + 2 * K1_WIMG + ro), vtr(wb + 2 * K1_WIMG + ro + 256));                          \
            aq = MFMA(wq, xf, aq);                                                                                        \
            ak = MFMA(wk, xf, ak);                                                                                        \
            av = MFMA(xf, wv, av);                                                                                        \
        }                                                                                                                 \
    } while (0)
    K1_STEP(0, 42); STAMP(2); K1_STEP(1, 35); K1_STEP(2, 28); K1_STEP(3, 21); STAMP(3); K1_STEP(4, 14); K1_STEP(5, 7); K1_STEP(6, 0); STAMP(4);
#undef K1_STEP
    float q2 = 0.f, k2 = 0.f;
#pragma unroll
    for (int i = 0; i < 16; ++i) {
        aq[i] *= QSCALE;
        if (i >= 12) { aq[i] = h ? 0.f : aq[i]; ak[i] = h ? 0.f : ak[i]; }
        q2 += aq[i] * aq[i]; k2 += ak[i] * ak[i];
    }
    q2 = half_lo(q2) + half_hi(q2);
    k2 = half_lo(k2) + half_hi(k2);
    if (h) { aq[12] = __builtin_sqrtf(q2); ak[12] = 1.f; ak[13] = 1.f; ak[14] = 1.f; }
    if (r >= DH) {
#pragma unroll
        for (int i = 0; i < 16; ++i) av[i] = (r == DH) ? 1.f : 0.f;
    }
#pragma unroll
    for (int o = 1; o < 32; o <<= 1) k2 = __builtin_fmaxf(k2, __shfl_xor(k2, o));
    const unsigned t32 = tt * 4 + w;
    char* qf = ws + WS_QF + (size_t)(hd * 64 + t32) * 2048 + lane * 16;
    char* kv = ws + WS_KV + (size_t)(hd * 64 + t32) * 4096 + lane * 16;
    *(bf16x8*)(kv) = pack8(ak, 0);
    *(bf16x8*)(kv + 1024) = pack8(ak, 1);
    *(bf16x8*)(kv + 2048) = pack8(av, 0);
    *(bf16x8*)(kv + 3072) = pack8(av, 1);
    *(bf16x8*)(qf) = pack8(aq, 0);
    *(bf16x8*)(qf + 1024) = pack8(aq, 1);
    if (lane == 0) ((float*)(ws + WS_KM))[hd * 64 + t32] = k2;
    STAMP(5); asm volatile("s_waitcnt vmcnt(0)" ::: "memory"); STAMP(6);
    STAMP_FLUSH(0);
}

constexpr float L_MIN = 9.094947017729282e-13f;
DI unsigned bf16_bits(float x) { return cvtpk(x, 0.f) & 0xffffu; }
DI float bf16_val(unsigned b) { return __uint_as_float(b << 16); }

__global__ __launch_bounds__(256, 2) void attn_kernel(char* __restrict__ ws) {
    __shared__ __attribute__((aligned(16))) char lds[4 * 4 * 4096];
    STAMP_DECL STAMP(0);
    const int tid = threadIdx.x, lane = tid & 63;
    const unsigned w = rfl(tid >> 6);
    const int h = lane >> 5;
    const unsigned b = blockIdx.x;
    const unsigned hd = (b & 7) * 2 + ((b >> 3) & 1), qt = b >> 4;
    const char* kvh = ws + WS_KV + (size_t)hd * (64 * 4096);
    const bf16x8* kvp = (const bf16x8*)(kvh + (size_t)w * (16 * 4096)) + lane;
    const bf16x8* qp = (const bf16x8*)(ws + WS_QF + (size_t)(hd * 64 + qt * 4) * 2048) + lane;

    bf16x8 kA0 = kvp[0], kA1 = kvp[64], vA0 = kvp[128], vA1 = kvp[192];
    bf16x8 q[4][2];
#pragma unroll
    for (int i = 0; i < 4; ++i) { q[i][0] = qp[i * 128]; q[i][1] = qp[i * 128 + 64]; }
    float km = ((const float*)(ws + WS_KM))[hd * 64 + lane];
    bf16x8 kB0 = kvp[256], kB1 = kvp[256 + 64], vB0 = kvp[256 + 128], vB1 = kvp[256 + 192];
#pragma unroll
    for (int o = 1; o < 64; o <<= 1) km = __builtin_fmaxf(km, __shfl_xor(km, o));
    km = -1.03f * __builtin_sqrtf(km);
#pragma unroll
    for (int i = 0; i < 4; ++i) {
        const float ref = bf16_val((unsigned short)q[i][1][4]) * km;
        q[i][1][4] = h ? (short)bf16_bits(ref) : q[i][1][4];
    }
    const f32x16 zero = {};
    f32x16 o[4] = {zero, zero, zero, zero};
    f32x16 sA, sB;
    u32x4 pA0, pA1, pB0, pB1;
#define EX4(SC, i) do { SC[i] = __builtin_amdgcn_exp2f(SC[i]); SC[i + 1] = __builtin_amdgcn_exp2f(SC[i + 1]); SC[i + 2] = __builtin_amdgcn_exp2f(SC[i + 2]); SC[i + 3] = __builtin_amdgcn_exp2f(SC[i + 3]); } while (0)
#define SLOT(SC, SN, PC0, PC1, PP0, PP1, K0, K1, QN, V0, V1, OP, DOQK, DOPV)                             \
    do {                                                                                                  \
        if (DOQK) SN = MFMA(K0, QN[0], zero);                                                             \
        EX4(SC, 0); PIN(SC); SBAR();                                                                      \
        if (DOPV) OP = MFMA(V0, __builtin_bit_cast(bf16x8, PP0), OP);                                     \
        EX4(SC, 4); PC0[0] = cvtpk(SC[0], SC[1]); PC0[1] = cvtpk(SC[2], SC[3]); PIN(SC); PIN(PC0); SBAR(); \
        if (DOQK) SN = MFMA(K1, QN[1], SN);                                                               \
        EX4(SC, 8); PC0[2] = cvtpk(SC[4], SC[5]); PC0[3] = cvtpk(SC[6], SC[7]); PIN(SC); PIN(PC0); SBAR(); \
        if (DOPV) OP = MFMA(V1, __builtin_bit_cast(bf16x8, PP1), OP);                                     \
        EX4(SC, 12); PC1[0] = cvtpk(SC[8], SC[9]); PC1[1] = cvtpk(SC[10], SC[11]); PIN(SC); PIN(PC1); SBAR(); \
        PC1[2] = cvtpk(SC[12], SC[13]); PC1[3] = cvtpk(SC[14], SC[15]); PIN(PC1); SBAR();                 \
    } while (0)
    STAMP(1);
    sA = MFMA(kA0, q[0][0], zero); sA = MFMA(kA1, q[0][1], sA);
    pB0 = (u32x4){0, 0, 0, 0}; pB1 = pB0;
#pragma unroll 1
    for (int t = 0; t < 16; t += 2) {
        const bf16x8* nx = kvp + (size_t)((t + 2) & 15) * 256;
        SLOT(sA, sB, pA0, pA1, pB0, pB1, kA0, kA1, q[1], vB0, vB1, o[3], 1, 1);
        if (t) { vB0 = kvp[(size_t)(t + 1) * 256 + 128]; vB1 = kvp[(size_t)(t + 1) * 256 + 192]; } SBAR();
        SLOT(sB, sA, pB0, pB1, pA0, pA1, kA0, kA1, q[2], vA0, vA1, o[0], 1, 1);
        SLOT(sA, sB, pA0, pA1, pB0, pB1, kA0, kA1, q[3], vA0, vA1, o[1], 1, 1);
        kA0 = nx[0]; kA1 = nx[64]; SBAR();
        SLOT(sB, sA, pB0, pB1, pA0, pA1, kB0, kB1, q[0], vA0, vA1, o[2], 1, 1);
        SLOT(sA, sB, pA0, pA1, pB0, pB1, kB0, kB1, q[1], vA0, vA1, o[3], 1, 1);
        vA0 = nx[128]; vA1 = nx[192]; SBAR();
        SLOT(sB, sA, pB0, pB1, pA0, pA1, kB0, kB1, q[2], vB0, vB1, o[0], 1, 1);
        SLOT(sA, sB, pA0, pA1, pB0, pB1, kB0, kB1, q[3], vB0, vB1, o[1], 1, 1);
        kB0 = nx[256]; kB1 = nx[256 + 64]; SBAR();
        SLOT(sB, sA, pB0, pB1, pA0, pA1, kA0, kA1, q[0], vB0, vB1, o[2], 1, 1);
    }
    o[3] = MFMA(vB0, __builtin_bit_cast(bf16x8, pB0), o[3]);
    o[3] = MFMA(vB1, __builtin_bit_cast(bf16x8, pB1), o[3]);
#undef SLOT
    STAMP(2);
#pragma unroll
    for (int i = 0; i < 4; ++i) {
        if (i != (int)w) {
#pragma unroll
            for (int g = 0; g < 4; ++g)
                *(f32x4*)(lds + (w * 4 + i) * 4096 + g * 1024 + lane * 16) = (f32x4){o[i][4 * g], o[i][4 * g + 1], o[i][4 * g + 2], o[i][4 * g + 3]};
        }
    }
    f32x16 acc = (w == 0) ? o[0] : (w == 1) ? o[1] : (w == 2) ? o[2] : o[3];
    __syncthreads();
#pragma unroll
    for (int s = 1; s < 4; ++s) {
        const unsigned src = (w + s) & 3;
#pragma unroll
        for (int g = 0; g < 4; ++g) {
            const f32x4 v = *(const f32x4*)(lds + (src * 4 + w) * 4096 + g * 1024 + lane * 16);
            acc[4 * g] += v[0]; acc[4 * g + 1] += v[1]; acc[4 * g + 2] += v[2]; acc[4 * g + 3] += v[3];
        }
    }
    STAMP(3);
    float l = half_hi(acc[12]);
    if (__builtin_expect(__any(!(l >= L_MIN)), 0)) {
        const bf16x8* kall = (const bf16x8*)kvh + lane;
        bf16x8 q0 = qp[w * 128], q1 = qp[w * 128 + 64];
        if (h) q1[4] = 0;
        float m = -__builtin_inff();
#pragma unroll 1
        for (int t = 0; t < 64; ++t) {
            f32x16 s = MFMA(kall[t * 256], q0, zero);
            s = MFMA(kall[t * 256 + 64], q1, s);
#pragma unroll
            for (int i = 0; i < 16; ++i) m = __builtin_fmaxf(m, s[i]);
        }
        m = __builtin_fmaxf(half_lo(m), half_hi(m));
        const unsigned m0 = bf16_bits(-m); const float r1 = -m - bf16_val(m0);
        const unsigned m1 = bf16_bits(r1); const float r2 = r1 - bf16_val(m1);
        const unsigned m2 = bf16_bits(r2);
        if (h) { q1[4] = (short)m0; q1[5] = (short)m1; q1[6] = (short)m2; }
        acc = zero;
#pragma unroll 1
        for (int t = 0; t < 64; ++t) {
            f32x16 s = MFMA(kall[t * 256], q0, zero);
            s = MFMA(kall[t * 256 + 64], q1, s);
#pragma unroll
            for (int i = 0; i < 16; ++i) s[i] = __builtin_amdgcn_exp2f(s[i]);
            acc = MFMA(kall[t * 256 + 128], pack8(s, 0), acc);
            acc = MFMA(kall[t * 256 + 192], pack8(s, 1), acc);
        }
        l = half_hi(acc[12]);
    }
    const float rl = 1.0f / l;
#pragma unroll
    for (int i = 0; i < 16; ++i) acc[i] = (i >= 12 && h) ? 0.f : acc[i] * rl;
    char* of = ws + WS_OF + (size_t)((qt * 4 + w) * NH + hd) * 2048 + lane * 16;
    *(bf16x8*)(of) = pack8(acc, 0);
    *(bf16x8*)(of + 1024) = pack8(acc, 1);
    STAMP(4); asm volatile("s_waitcnt vmcnt(0)" ::: "memory"); STAMP(5);
    STAMP_FLUSH(1);
}

constexpr int K3_LDS = 480 * 128;
__global__ __launch_bounds__(256, 1) void proj_kernel(const char* __restrict__ ws, const char* __restrict__ wo, char* __restrict__ out) {
    __shared__ __attribute__((aligned(16))) char lds[K3_LDS];
    STAMP_DECL STAMP(0);
    const int tid = threadIdx.x, lane = tid & 63;
    const unsigned w = rfl(tid >> 6);
    const int r = lane & 31, h = lane >> 5;
    const unsigned b = blockIdx.x, rb = b / 7, cb = b % 7;
    const unsigned lds0 = (unsigned)(uintptr_t)lds;
    const __amdgpu_buffer_rsrc_t rsW = __builtin_amdgcn_make_buffer_rsrc((void*)wo, (short)0, DM * DM * 2, 0x00020000);
    const unsigned wvo = (8 * w + (lane >> 3)) * (DM * 2) + cb * 128 + (((lane & 7) ^ (((lane >> 4) & 1) << 2)) * 16);
    asm volatile("s_nop 4" ::: "memory");
#pragma unroll
    for (int m = 0; m < 15; ++m) dma16(lds0 + (m * 32 + 8 * w) * 128, wvo, rsW, m * 32 * (DM * 2));
    const unsigned qt = rb * 2 + (w >> 1), cw = w & 1;
    const bf16x8* op = (const bf16x8*)(ws + WS_OF + (size_t)qt * (NH * 2048)) + lane;
    bf16x8 of[32];
#pragma unroll
    for (int i = 0; i < 32; ++i) of[i] = op[i * 64];
    STAMP(1);
    asm volatile("s_waitcnt vmcnt(32)\n\ts_barrier" ::: "memory");
    STAMP(2);
    const int i16 = lane & 15, q4 = i16 >> 2;
    LAS const char* wb = (LAS const char*)lds + (4 * h + q4) * 128 + ((cw ^ (q4 >> 1)) * 64) + ((lane >> 4) & 1) * 32 + (i16 & 3) * 8;
    f32x16 acc = {};
#pragma unroll
    for (int i = 0; i < 32; ++i) {
        const int ro = ((i >> 1) * DH + (i & 1) * 16) * 128;
        const bf16x8 wf = cat(vtr(wb + ro), vtr(wb + ro + 8 * 128));
        acc = MFMA(wf, of[i], acc);
    }
    STAMP(3);
    char* orow = out + (size_t)(qt * 32 + r) * (DM * 2) + cb * 128 + cw * 64 + h * 16;
#pragma unroll
    for (int g = 0; g < 4; g += 2) {
        unsigned a0 = cvtpk(acc[4 * g], acc[4 * g + 1]), a1 = cvtpk(acc[4 * g + 2], acc[4 * g + 3]);
        unsigned b0 = cvtpk(acc[4 * g + 4], acc[4 * g + 5]), b1 = cvtpk(acc[4 * g + 6], acc[4 * g + 7]);
        auto s0 = __builtin_amdgcn_permlane32_swap(a0, b0, false, false);
        auto s1 = __builtin_amdgcn_permlane32_swap(a1, b1, false, false);
        *(u32x4*)(orow + g * 16) = (u32x4){s0[0], s1[0], s0[1], s1[1]};
    }
    STAMP(4); asm volatile("s_waitcnt vmcnt(0)" ::: "memory"); STAMP(5);
    STAMP_FLUSH(2);
}

extern "C" void kernel_launch(void* const* d_in, const int* in_sizes, int n_in, void* d_out, int out_size, void* d_ws, size_t ws_size,
                              hipStream_t stream) {
    const char* x = (const char*)d_in[0];
    const char* wqkv = (const char*)d_in[1];
    const char* wo = (const char*)d_in[2];
    char* ws = (char*)d_ws;
    (void)hipFuncSetAttribute((const void*)qkv_kernel, hipFuncAttributeMaxDynamicSharedMemorySize, K1_LDS);
    qkv_kernel<<<256, 256, K1_LDS, stream>>>(x, wqkv, ws);
    attn_kernel<<<256, 256, 0, stream>>>(ws);
    proj_kernel<<<224, 256, 0, stream>>>(ws, wo, (char*)d_out);
}
```

```cpp
#include <hip/hip_runtime.h>
#include <hip/hip_bf16.h>
#include <stdint.h>

typedef short bf16x8 __attribute__((ext_vector_type(8)));
typedef short s16x4 __attribute__((ext_vector_type(4)));
typedef float f32x16 __attribute__((ext_vector_type(16)));
typedef float f32x4 __attribute__((ext_vector_type(4)));
typedef float f32x2 __attribute__((ext_vector_type(2)));
typedef unsigned u32x4 __attribute__((ext_vector_type(4)));
typedef __bf16 bf16x2_t __attribute__((ext_vector_type(2)));
typedef short v4i16_t __attribute__((ext_vector_type(4)));
typedef unsigned long long u64;
#define LAS __attribute__((address_space(3)))
#define GAS __attribute__((address_space(1)))
#define DI __device__ __forceinline__
#define MFMA(a, b, c) __builtin_amdgcn_mfma_f32_32x32x16_bf16((a), (b), (c), 0, 0, 0)
#define SBAR() __builtin_amdgcn_sched_barrier(0)
#define RLX_AGENT __ATOMIC_RELAXED, __HIP_MEMORY_SCOPE_AGENT
#define BAR() asm volatile("s_waitcnt lgkmcnt(0)\n\ts_barrier" ::: "memory")

#define MAPPING 1
constexpr int SEQ = 2048, DM = 448, NH = 16, DH = 28, N3 = 1344;
constexpr float QSCALE = 0.18898223650461362f * 1.4426950408889634f;
constexpr float L_MIN = 9.094947017729282e-13f;
constexpr unsigned KT = 56 * 512;
constexpr unsigned VROW = 464 * 16, VT = 4 * VROW;
constexpr unsigned WS_K = 0, WS_V = 2u << 20, WS_O = 4u << 20, WS_F = 6u << 20;
constexpr unsigned F_KV = 0, F_O = 8192, F_G = 16384;
constexpr unsigned L_WQ = 0, L_RING = 28672, STG = 24576;
constexpr unsigned L_QX = 131072, L_Q2 = L_QX + 16384, L_GB = L_Q2 + 512;
constexpr unsigned L_WO = 0, L_CB = 114688;
constexpr unsigned LDS_BYTES = 163840;

#define STAMP_DECL
#define STAMP(i)
#define STAMP_FLUSH()

DI unsigned rfl(unsigned x) { return __builtin_amdgcn_readfirstlane(x); }
DI unsigned cvtpk(float lo, float hi) { f32x2 v = {lo, hi}; bf16x2_t b = __builtin_convertvector(v, bf16x2_t); return __builtin_bit_cast(unsigned, b); }
DI u32x4 pack8(const f32x16& x, int s) {
    return (u32x4){cvtpk(x[8 * s], x[8 * s + 1]), cvtpk(x[8 * s + 2], x[8 * s + 3]), cvtpk(x[8 * s + 4], x[8 * s + 5]), cvtpk(x[8 * s + 6], x[8 * s + 7])};
}
DI bf16x8 asbf(u32x4 x) { return __builtin_bit_cast(bf16x8, x); }
DI s16x4 vtr(LAS const char* p) { return __builtin_bit_cast(s16x4, __builtin_amdgcn_ds_read_tr16_b64_v4i16((LAS v4i16_t*)p)); }
DI bf16x8 cat(s16x4 lo, s16x4 hi) { return __builtin_shufflevector(lo, hi, 0, 1, 2, 3, 4, 5, 6, 7); }
DI float half_lo(float x) { auto r = __builtin_amdgcn_permlane32_swap(__float_as_uint(x), __float_as_uint(x), false, false); return __uint_as_float(r[0]); }
DI float half_hi(float x) { auto r = __builtin_amdgcn_permlane32_swap(__float_as_uint(x), __float_as_uint(x), false, false); return __uint_as_float(r[1]); }
DI unsigned wave_max_u(unsigned x) {
#define DPPMAX(ctrl, rm) x = max(x, (unsigned)__builtin_amdgcn_update_dpp((int)x, (int)x, ctrl, rm, 0xf, false))
    DPPMAX(0x111, 0xf); DPPMAX(0x112, 0xf); DPPMAX(0x114, 0xf); DPPMAX(0x118, 0xf); DPPMAX(0x142, 0xa); DPPMAX(0x143, 0xc);
#undef DPPMAX
    return (unsigned)__builtin_amdgcn_readlane((int)x, 63);
}
DI void dma16(unsigned lds_dst, unsigned voff, __amdgpu_buffer_rsrc_t rs, unsigned soff) {
    asm volatile("s_mov_b32 m0, %0\n\ts_nop 0\n\tbuffer_load_dwordx4 %1, %2, %3 offen lds" ::"s"(lds_dst), "v"(voff), "s"(rs), "s"(soff) : "m0", "memory");
}
DI void st16_wt(u32x4 v, __amdgpu_buffer_rsrc_t rs, unsigned voff, unsigned soff) { __builtin_amdgcn_raw_buffer_store_b128(v, rs, voff, soff, 16); }
DI u32x4 ld16_wt(__amdgpu_buffer_rsrc_t rs, unsigned voff, unsigned soff) { return __builtin_amdgcn_raw_buffer_load_b128(rs, voff, soff, 16); }
DI __amdgpu_buffer_rsrc_t mkrs(const void* p, unsigned bytes) { return __builtin_amdgcn_make_buffer_rsrc((void*)p, (short)0, (int)bytes, 0x00020000); }
DI unsigned poll_tags(const GAS u64* g, unsigned tag) {
    u64 v;
    for (unsigned spins = 0; spins < (1u << 16); ++spins) {
        v = __hip_atomic_load(g, RLX_AGENT);
        if (__all((unsigned)v == tag)) break;
        __builtin_amdgcn_s_sleep(1);
    }
    __builtin_amdgcn_fence(__ATOMIC_ACQUIRE, "wavefront");
    return (unsigned)(v >> 32);
}

template <bool ISK>
DI void kv_gemm(LAS char* lds, unsigned w, int lane, bf16x8 (&xf)[7][4], f32x16& a0, f32x16& a1) {
    const int r = lane & 31, h = lane >> 5, i16 = lane & 15, q4 = i16 >> 2, p4 = i16 & 3, g16 = (lane >> 4) & 1;
    const int pc = ISK ? (((p4 & 1) << 1) | (p4 >> 1)) : p4;
    LAS const char* xb = lds + L_RING + w * 4096 + r * 128;
    LAS const char* wb0 = lds + L_RING + 16384 + (8 * h + q4) * 128 + ((q4 >> 1) * 64) + g16 * 32 + pc * 8;
    LAS const char* wb1 = lds + L_RING + 16384 + (8 * h + q4) * 128 + (((q4 >> 1) ^ 1) * 64) + g16 * 32 + pc * 8;
    const int xs = (r >> 1) & 7;
    bf16x8 wf[2][4][2];
#define RD(i) do { const int so = ((i) % 5) * STG; _Pragma("unroll") for (int ks = 0; ks < 4; ++ks) {                          \
        xf[i][ks] = *(LAS const bf16x8*)(xb + so + (((2 * ks + h) ^ xs) * 16));                                                \
        wf[(i) & 1][ks][0] = cat(vtr(wb0 + so + ks * 2048), vtr(wb0 + so + ks * 2048 + 512));                                  \
        wf[(i) & 1][ks][1] = cat(vtr(wb1 + so + ks * 2048), vtr(wb1 + so + ks * 2048 + 512)); } } while (0)
#define MM(i) do { _Pragma("unroll") for (int ks = 0; ks < 4; ++ks) {                                                          \
        if (ISK) { a0 = MFMA(wf[(i) & 1][ks][0], xf[i][ks], a0); a1 = MFMA(wf[(i) & 1][ks][1], xf[i][ks], a1); }               \
        else     { a0 = MFMA(xf[i][ks], wf[(i) & 1][ks][0], a0); a1 = MFMA(xf[i][ks], wf[(i) & 1][ks][1], a1); } } } while (0)
    BAR(); RD(0);
#pragma unroll
    for (int i = 0; i < 5; ++i) { BAR(); RD(i + 1); SBAR(); MM(i); SBAR(); }
    MM(5); SBAR();
    BAR(); RD(6); MM(6);
#undef RD
#undef MM
}

__global__ __launch_bounds__(512, 2) void mha_kernel(const char* __restrict__ x, const char* __restrict__ wqkv, const char* __restrict__ wo,
                                                     char* __restrict__ out, char* __restrict__ ws) {
    extern __shared__ __attribute__((aligned(16))) char lds_[];
    LAS char* lds = (LAS char*)lds_;
    STAMP_DECL STAMP(0);
    const int lane = threadIdx.x & 63;
    const unsigned wv = rfl(threadIdx.x >> 6), w = wv & 3;
    const int r = lane & 31, h = lane >> 5, i16 = lane & 15, q4 = i16 >> 2, p4 = i16 & 3, g16 = (lane >> 4) & 1;
    const unsigned b = blockIdx.x, xcd = b & 7, idx = b >> 3;
    const unsigned hd = xcd * 2 + (idx & 1), qt = idx >> 1;
    const unsigned ch = hd < 14 ? hd : hd - 14;
    const bool isK = ch < 7;
    const unsigned rot = (hd + 2 * qt) % 7;
    const unsigned g0 = (7 * hd) >> 1;
    const unsigned cg = hd >> 2, q32 = qt * 4 + (hd & 3);
    const unsigned lds0 = (unsigned)(uintptr_t)lds_;
    GAS char* wsg = (GAS char*)ws;
    const __amdgpu_buffer_rsrc_t rsOI = mkrs(ws + WS_O, 2u << 20);
    unsigned kbi[7];
#pragma unroll
    for (int i = 0; i < 7; ++i) { const unsigned k = rot + i; kbi[i] = k >= 7 ? k - 7 : k; }

    if (wv >= 4) {
        const __amdgpu_buffer_rsrc_t rsW = mkrs(wqkv, DM * N3 * 2), rsX = mkrs(x, SEQ * DM * 2), rsF = mkrs(ws + WS_F, 32768);
        const unsigned xrow = qt * 128 + w * 32 + (lane >> 3);
        const unsigned xvo0 = xrow * (DM * 2) + (((lane & 7) ^ (lane >> 4)) * 16);
        const unsigned xvo1 = xrow * (DM * 2) + (((lane & 7) ^ (4 + (lane >> 4))) * 16);
        const unsigned wvo = (16 * w + (lane >> 3)) * (N3 * 2) + 896 + ch * 128 + (((lane & 7) ^ (((lane >> 4) & 1) << 2)) * 16);
        const unsigned qvo = (16 * w + (lane >> 2)) * (N3 * 2) + g0 * 16 + (lane & 3) * 16;
#define STAGE(i)                                                                                                              \
    do {                                                                                                                      \
        const unsigned sl_ = lds0 + L_RING + ((i) % 5) * STG;                                                                 \
        _Pragma("unroll") for (int m = 0; m < 2; ++m) dma16(sl_ + 16384 + (16 * w + 8 * m) * 128, wvo, rsW, (kbi[i] * 64 + 8 * m) * (N3 * 2)); \
        _Pragma("unroll") for (int m = 0; m < 4; ++m) dma16(sl_ + w * 4096 + m * 1024, (m & 1) ? xvo1 : xvo0, rsX, kbi[i] * 128 + m * 8 * (DM * 2)); \
    } while (0)
#define VBAR(NV) asm volatile("s_waitcnt vmcnt(" #NV ")\n\ts_barrier" ::: "memory")
        asm volatile("s_nop 4" ::: "memory");
        STAGE(0); STAGE(1); STAGE(2);
        STAMP(1);
        VBAR(12); STAGE(3);
        STAMP(2);
        VBAR(12); STAGE(4);
        VBAR(12); STAGE(5);
        VBAR(12); STAGE(6);
        VBAR(12);
        unsigned gen;
        { const unsigned vo = F_G; asm volatile("buffer_load_dword %0, %1, %2, 0 offen sc1" : "=v"(gen) : "v"(vo), "s"(rsF) : "memory"); }
#pragma unroll
        for (int j = 0; j < 7; ++j) dma16(lds0 + L_WQ + (16 * w + 64 * kbi[j]) * 64, qvo, rsW, kbi[j] * 64 * (N3 * 2));
        VBAR(14); VBAR(8);
        STAMP(3);
        asm volatile("s_waitcnt vmcnt(7)\n\ts_barrier" : "+v"(gen)::"memory");
        unsigned tag = rfl(gen);
        { const unsigned t = (tag ^ (tag >> 15)) * 0x2c1b3c6du + 0x9e3779b9u; tag = (t == tag) ? t + 1 : t; }
        if (w == 0 && hd < 14) {
            const u32x4 gb = *(LAS const u32x4*)(lds + L_GB);
            const unsigned g = max(max(gb[0], gb[1]), max(gb[2], gb[3]));
            const unsigned hh = lane & 15, wh = (lane >> 4) & 1;
            const unsigned c = isK ? (((7 * hh >> 1) + 3 * wh) >> 3) : 7 + ((28 * hh + 27 * wh) >> 6);
            if (lane < 32 && c == ch)
                __hip_atomic_store((GAS u64*)(wsg + WS_F + F_KV + ((hh * 4 + (isK ? 0 : 2) + wh) * 16 + qt) * 8), ((u64)g << 32) | tag, RLX_AGENT);
            asm volatile("s_waitcnt vmcnt(1)" ::: "memory");
        } else {
            asm volatile("s_waitcnt vmcnt(0)" ::: "memory");
        }
        STAMP(4);
        asm volatile("s_barrier" ::: "memory");
        if (w == 0) {
            const unsigned g = wave_max_u(poll_tags((const GAS u64*)(wsg + WS_F + F_KV + hd * 512) + lane, tag));
            if (lane == 0) *(LAS unsigned*)(lds + L_GB + 16) = g;
        }
        STAMP(5);
        BAR();
        STAMP(6);
        asm volatile("s_barrier" ::: "memory");
        {
            const __amdgpu_buffer_rsrc_t rsO = mkrs(wo, DM * DM * 2);
            const unsigned vow = (4 * w + (lane >> 4)) * (DM * 2) + cg * 256 + ((((lane >> 2) & 3) ^ (lane >> 4)) * 64) + (lane & 3) * 16;
            __builtin_amdgcn_s_sleep(32);
#pragma unroll 1
            for (unsigned j = 0; j < 28; ++j) { dma16(lds0 + L_WO + (4 * j + w) * 1024, vow, rsO, j * 14336); __builtin_amdgcn_s_sleep(4); }
        }
        VBAR(0);
        STAMP(8);
        BAR();
        STAMP(9);
        if (w == 0) {
            if (lane == 0) __hip_atomic_store((GAS u64*)(wsg + WS_F + F_O + (qt * 16 + hd) * 8), (u64)tag, RLX_AGENT);
            (void)poll_tags((const GAS u64*)(wsg + WS_F + F_O + qt * 128) + (lane & 15), tag);
        }
        BAR();
        STAMP(10);
        {
            u32x4 t[4];
#pragma unroll
            for (int f = 0; f < 4; ++f) t[f] = ld16_wt(rsOI, lane * 16 + f * 1024, q32 * (NH * 2048) + wv * 4096);
#pragma unroll
            for (int f = 0; f < 4; ++f) *(LAS u32x4*)(lds + L_CB + (wv * 4 + f) * 1024 + lane * 16) = t[f];
        }
        BAR();
        STAMP(11);
        if (w == 1 && b == 0) {
#pragma unroll 1
            for (int j = 0; j < 4; ++j) (void)poll_tags((const GAS u64*)(wsg + WS_F + F_O) + j * 64 + lane, tag);
            if (lane == 0) __hip_atomic_store((GAS unsigned*)(wsg + WS_F + F_G), tag, RLX_AGENT);
        }
        STAMP(13);
        STAMP_FLUSH();
        return;
    }

    f32x16 a0 = {}, a1 = {};
    bf16x8 xf[7][4];
    const unsigned kt = qt * 4 + w;
    unsigned gmx = 0;
    if (isK) {
        kv_gemm<true>(lds, w, lane, xf, a0, a1);
        STAMP(3);
        if (hd < 14) {
            const __amdgpu_buffer_rsrc_t rs = mkrs(ws + WS_K, 64 * KT);
            const unsigned vo = (8 * ch + h) * 512 + r * 16, so = kt * KT;
            st16_wt(pack8(a0, 0), rs, vo, so); st16_wt(pack8(a0, 1), rs, vo + 1024, so);
            st16_wt(pack8(a1, 0), rs, vo + 2048, so); st16_wt(pack8(a1, 1), rs, vo + 3072, so);
            float s0 = 0.f, s1 = 0.f, s2 = 0.f, s3 = 0.f;
#pragma unroll
            for (int j = 0; j < 8; ++j) { s0 += a0[j] * a0[j]; s1 += a0[8 + j] * a0[8 + j]; s2 += a1[j] * a1[j]; s3 += a1[8 + j] * a1[8 + j]; }
            gmx = max(max(__float_as_uint(s0), __float_as_uint(s1)), max(__float_as_uint(s2), __float_as_uint(s3)));
            gmx = wave_max_u(gmx);
        }
    } else {
        kv_gemm<false>(lds, w, lane, xf, a0, a1);
        STAMP(3);
        const __amdgpu_buffer_rsrc_t rs = mkrs(ws + WS_V, 64 * VT);
        const unsigned n0 = 64 * (ch - 7) + r, n1 = n0 + 32, so = kt * VT;
        const unsigned v0 = h * VROW + (n0 + (n0 * 2341 >> 16)) * 16, v1 = h * VROW + (n1 + (n1 * 2341 >> 16)) * 16;
        st16_wt(pack8(a0, 0), rs, v0, so); st16_wt(pack8(a0, 1), rs, v0 + 2 * VROW, so);
        st16_wt(pack8(a1, 0), rs, v1, so); st16_wt(pack8(a1, 1), rs, v1 + 2 * VROW, so);
        const unsigned hc = (64 * (ch - 7) * 2341 >> 16) + (lane >> 2), last = 28 * hc + 27;
        if (lane < 16 && (last >> 6) == ch - 7)
            st16_wt((u32x4){0x3f803f80u, 0x3f803f80u, 0x3f803f80u, 0x3f803f80u}, rs, (lane & 3) * VROW + (29 * hc + 28) * 16, so);
    }
    if (lane == 0) *(LAS unsigned*)(lds + L_GB + w * 4) = gmx;
    STAMP(1);
    asm volatile("s_waitcnt vmcnt(0) lgkmcnt(0)" ::: "memory");
    STAMP(2);
    asm volatile("s_barrier" ::: "memory");
    STAMP(4);
    asm volatile("s_barrier" ::: "memory");
    f32x16 aq = {};
    {
        LAS const char* qb = lds + L_WQ + (8 * h + q4) * 64 + g16 * 32 + (((p4 & 1) << 1) | (p4 >> 1)) * 8;
#pragma unroll
        for (int i = 0; i < 7; ++i) {
            LAS const char* qk = qb + kbi[i] * 4096;
#pragma unroll
            for (int ks = 0; ks < 4; ++ks) aq = MFMA(cat(vtr(qk + ks * 1024), vtr(qk + ks * 1024 + 256)), xf[i][ks], aq);
        }
    }
    float q2 = 0.f;
    {
        const bool zlo = (hd & 1) && !h, zhi = !(hd & 1) && h;
#pragma unroll
        for (int i = 0; i < 16; ++i) {
            aq[i] *= QSCALE;
            if (i < 4) aq[i] = zlo ? 0.f : aq[i];
            if (i >= 12) aq[i] = zhi ? 0.f : aq[i];
            q2 += aq[i] * aq[i];
        }
        q2 = half_lo(q2) + half_hi(q2);
    }
    const u32x4 qo0 = pack8(aq, 0), qo1 = pack8(aq, 1);
    *(LAS u32x4*)(lds + L_QX + w * 2048 + lane * 16) = qo0;
    *(LAS u32x4*)(lds + L_QX + w * 2048 + 1024 + lane * 16) = qo1;
    *(LAS u32x4*)(lds + L_QX + 8192 + w * 2048 + lane * 16) = qo0;
    *(LAS u32x4*)(lds + L_QX + 8192 + w * 2048 + 1024 + lane * 16) = qo1;
    if (!h) *(LAS float*)(lds + L_Q2 + (r * 4 + w) * 4) = q2;
    STAMP(5);
    BAR();
    STAMP(6);

    f32x16 o0, o1, o2, o3;
    const unsigned vok = (g0 + h) * 512 + r * 16, vov = h * VROW + (29 * hd + r) * 16;
    {
        const __amdgpu_buffer_rsrc_t rsK = mkrs(ws + WS_K, (16 * w + 16) * KT), rsV = mkrs(ws + WS_V, (16 * w + 16) * VT);
        unsigned sk = 16 * w * KT, sv = 16 * w * VT, cnt = 4;
        unsigned q2a = lds0 + L_Q2 + r * 16, kba = lds0 + L_GB + 16;
        asm volatile(
        "buffer_load_dwordx4 v[160:163], v224, %[rsk], %[sk] offen sc1\n\t"
        "buffer_load_dwordx4 v[164:167], v224, %[rsk], %[sk] offen offset:1024 sc1\n\t"
        "buffer_load_dwordx4 v[168:171], v225, %[rsv], %[sv] offen sc1\n\t"
        "buffer_load_dwordx4 v[172:175], v226, %[rsv], %[sv] offen sc1\n\t"
        "s_add_u32 %[sk], %[sk], 28672\n\t"
        "s_add_u32 %[sv], %[sv], 29696\n\t"
        "buffer_load_dwordx4 v[176:179], v224, %[rsk], %[sk] offen sc1\n\t"
        "buffer_load_dwordx4 v[180:183], v224, %[rsk], %[sk] offen offset:1024 sc1\n\t"
        "buffer_load_dwordx4 v[184:187], v225, %[rsv], %[sv] offen sc1\n\t"
        "buffer_load_dwordx4 v[188:191], v226, %[rsv], %[sv] offen sc1\n\t"
        "s_add_u32 %[sk], %[sk], 28672\n\t"
        "s_add_u32 %[sv], %[sv], 29696\n\t"
        "buffer_load_dwordx4 v[192:195], v224, %[rsk], %[sk] offen sc1\n\t"
        "buffer_load_dwordx4 v[196:199], v224, %[rsk], %[sk] offen offset:1024 sc1\n\t"
        "buffer_load_dwordx4 v[200:203], v225, %[rsv], %[sv] offen sc1\n\t"
        "buffer_load_dwordx4 v[204:207], v226, %[rsv], %[sv] offen sc1\n\t"
        "s_add_u32 %[sk], %[sk], 28672\n\t"
        "s_add_u32 %[sv], %[sv], 29696\n\t"
        "ds_read_b128 v[112:115], v227 offset:0\n\t"
        "ds_read_b128 v[116:119], v227 offset:1024\n\t"
        "ds_read_b128 v[120:123], v227 offset:2048\n\t"
        "ds_read_b128 v[124:127], v227 offset:3072\n\t"
        "ds_read_b128 v[128:131], v227 offset:4096\n\t"
        "ds_read_b128 v[132:135], v227 offset:5120\n\t"
        "ds_read_b128 v[136:139], v227 offset:6144\n\t"
        "ds_read_b128 v[140:143], v227 offset:7168\n\t"
        "ds_read_b128 v[80:83], v228\n\t"
        "ds_read_b32 v229, v229\n\t"
        "v_mov_b32 v0, 0\n\t"
        "v_mov_b32 v1, 0\n\t"
        "v_mov_b32 v2, 0\n\t"
        "v_mov_b32 v3, 0\n\t"
        "v_mov_b32 v4, 0\n\t"
        "v_mov_b32 v5, 0\n\t"
        "v_mov_b32 v6, 0\n\t"
        "v_mov_b32 v7, 0\n\t"
        "v_mov_b32 v8, 0\n\t"
        "v_mov_b32 v9, 0\n\t"
        "v_mov_b32 v10, 0\n\t"
        "v_mov_b32 v11, 0\n\t"
        "v_mov_b32 v12, 0\n\t"
        "v_mov_b32 v13, 0\n\t"
        "v_mov_b32 v14, 0\n\t"
        "v_mov_b32 v15, 0\n\t"
        "v_mov_b32 v16, 0\n\t"
        "v_mov_b32 v17, 0\n\t"
        "v_mov_b32 v18, 0\n\t"
        "v_mov_b32 v19, 0\n\t"
        "v_mov_b32 v20, 0\n\t"
        "v_mov_b32 v21, 0\n\t"
        "v_mov_b32 v22, 0\n\t"
        "v_mov_b32 v23, 0\n\t"
        "v_mov_b32 v24, 0\n\t"
        "v_mov_b32 v25, 0\n\t"
        "v_mov_b32 v26, 0\n\t"
        "v_mov_b32 v27, 0\n\t"
        "v_mov_b32 v28, 0\n\t"
        "v_mov_b32 v29, 0\n\t"
        "v_mov_b32 v30, 0\n\t"
        "v_mov_b32 v31, 0\n\t"
        "v_mov_b32 v32, 0\n\t"
        "v_mov_b32 v33, 0\n\t"
        "v_mov_b32 v34, 0\n\t"
        "v_mov_b32 v35, 0\n\t"
        "v_mov_b32 v36, 0\n\t"
        "v_mov_b32 v37, 0\n\t"
        "v_mov_b32 v38, 0\n\t"
        "v_mov_b32 v39, 0\n\t"
        "v_mov_b32 v40, 0\n\t"
        "v_mov_b32 v41, 0\n\t"
        "v_mov_b32 v42, 0\n\t"
        "v_mov_b32 v43, 0\n\t"
        "v_mov_b32 v44, 0\n\t"
        "v_mov_b32 v45, 0\n\t"
        "v_mov_b32 v46, 0\n\t"
        "v_mov_b32 v47, 0\n\t"
        "v_mov_b32 v48, 0\n\t"
        "v_mov_b32 v49, 0\n\t"
        "v_mov_b32 v50, 0\n\t"
        "v_mov_b32 v51, 0\n\t"
        "v_mov_b32 v52, 0\n\t"
        "v_mov_b32 v53, 0\n\t"
        "v_mov_b32 v54, 0\n\t"
        "v_mov_b32 v55, 0\n\t"
        "v_mov_b32 v56, 0\n\t"
        "v_mov_b32 v57, 0\n\t"
        "v_mov_b32 v58, 0\n\t"
        "v_mov_b32 v59, 0\n\t"
        "v_mov_b32 v60, 0\n\t"
        "v_mov_b32 v61, 0\n\t"
        "v_mov_b32 v62, 0\n\t"
        "v_mov_b32 v63, 0\n\t"
        "v_mov_b32 v152, 0\n\t"
        "v_mov_b32 v153, 0\n\t"
        "v_mov_b32 v154, 0\n\t"
        "v_mov_b32 v155, 0\n\t"
        "v_mov_b32 v156, 0\n\t"
        "v_mov_b32 v157, 0\n\t"
        "v_mov_b32 v158, 0\n\t"
        "v_mov_b32 v159, 0\n\t"
        "v_mov_b32 v216, 0\n\t"
        "v_mov_b32 v217, 0\n\t"
        "v_mov_b32 v218, 0\n\t"
        "v_mov_b32 v219, 0\n\t"
        "v_mov_b32 v220, 0\n\t"
        "v_mov_b32 v221, 0\n\t"
        "v_mov_b32 v222, 0\n\t"
        "v_mov_b32 v223, 0\n\t"
        "s_waitcnt lgkmcnt(0)\n\t"
        "s_barrier\n\t"
        "v_max3_f32 v228, v80, v81, v82\n\t"
        "v_max_f32 v228, v228, v83\n\t"
        "v_mul_f32 v228, v228, v229\n\t"
        "v_sqrt_f32 v228, v228\n\t"
        "s_nop 0\n\t"
        "v_mul_f32 v96, 0xc0028f5c, v228\n\t"
        "v_mov_b32 v97, v96\n\t"
        "v_mov_b32 v98, v96\n\t"
        "v_mov_b32 v99, v96\n\t"
        "v_mov_b32 v100, v96\n\t"
        "v_mov_b32 v101, v96\n\t"
        "v_mov_b32 v102, v96\n\t"
        "v_mov_b32 v103, v96\n\t"
        "v_mov_b32 v104, v96\n\t"
        "v_mov_b32 v105, v96\n\t"
        "v_mov_b32 v106, v96\n\t"
        "v_mov_b32 v107, v96\n\t"
        "v_mov_b32 v108, v96\n\t"
        "v_mov_b32 v109, v96\n\t"
        "v_mov_b32 v110, v96\n\t"
        "v_mov_b32 v111, v96\n\t"
        "s_waitcnt vmcnt(8)\n\t"
        "v_mfma_f32_32x32x16_bf16 v[64:79], v[160:163], v[112:115], v[96:111]\n\t"
        "v_mfma_f32_32x32x16_bf16 v[64:79], v[164:167], v[116:119], v[64:79]\n\t"
        "s_nop 15\n\t"
        "s_nop 3\n\t"
        "LOOP%=:\n\t"
        "v_mfma_f32_32x32x16_bf16 v[80:95], v[160:163], v[120:123], v[96:111]\n\t"
        "v_exp_f32 v64, v64\n\t"
        "v_exp_f32 v65, v65\n\t"
        "v_exp_f32 v66, v66\n\t"
        "v_exp_f32 v67, v67\n\t"
        "v_mfma_f32_32x32x16_bf16 v[80:95], v[164:167], v[124:127], v[80:95]\n\t"
        "v_exp_f32 v68, v68\n\t"
        "v_exp_f32 v69, v69\n\t"
        "v_exp_f32 v70, v70\n\t"
        "v_exp_f32 v71, v71\n\t"
        "v_cvt_pk_bf16_f32 v144, v64, v65\n\t"
        "v_cvt_pk_bf16_f32 v145, v66, v67\n\t"
        "v_mfma_f32_32x32x16_bf16 v[48:63], v[216:219], v[152:155], v[48:63]\n\t"
        "v_exp_f32 v72, v72\n\t"
        "v_exp_f32 v73, v73\n\t"
        "v_exp_f32 v74, v74\n\t"
        "v_exp_f32 v75, v75\n\t"
        "v_cvt_pk_bf16_f32 v146, v68, v69\n\t"
        "v_cvt_pk_bf16_f32 v147, v70, v71\n\t"
        "v_mfma_f32_32x32x16_bf16 v[48:63], v[220:223], v[156:159], v[48:63]\n\t"
        "v_exp_f32 v76, v76\n\t"
        "v_exp_f32 v77, v77\n\t"
        "v_exp_f32 v78, v78\n\t"
        "v_exp_f32 v79, v79\n\t"
        "v_cvt_pk_bf16_f32 v148, v72, v73\n\t"
        "v_cvt_pk_bf16_f32 v149, v74, v75\n\t"
        "v_cvt_pk_bf16_f32 v150, v76, v77\n\t"
        "v_cvt_pk_bf16_f32 v151, v78, v79\n\t"
        "v_mfma_f32_32x32x16_bf16 v[64:79], v[160:163], v[128:131], v[96:111]\n\t"
        "v_exp_f32 v80, v80\n\t"
        "buffer_load_dwordx4 v[208:211], v224, %[rsk], %[sk] offen sc1\n\t"
        "v_exp_f32 v81, v81\n\t"
        "buffer_load_dwordx4 v[212:215], v224, %[rsk], %[sk] offen offset:1024 sc1\n\t"
        "v_exp_f32 v82, v82\n\t"
        "buffer_load_dwordx4 v[216:219], v225, %[rsv], %[sv] offen sc1\n\t"
        "v_exp_f32 v83, v83\n\t"
        "buffer_load_dwordx4 v[220:223], v226, %[rsv], %[sv] offen sc1\n\t"
        "v_mfma_f32_32x32x16_bf16 v[64:79], v[164:167], v[132:135], v[64:79]\n\t"
        "v_exp_f32 v84, v84\n\t"
        "s_add_u32 %[sk], %[sk], 28672\n\t"
        "v_exp_f32 v85, v85\n\t"
        "s_add_u32 %[sv], %[sv], 29696\n\t"
        "v_exp_f32 v86, v86\n\t"
        "v_exp_f32 v87, v87\n\t"
        "v_cvt_pk_bf16_f32 v152, v80, v81\n\t"
        "v_cvt_pk_bf16_f32 v153, v82, v83\n\t"
        "v_mfma_f32_32x32x16_bf16 v[0:15], v[168:171], v[144:147], v[0:15]\n\t"
        "v_exp_f32 v88, v88\n\t"
        "v_exp_f32 v89, v89\n\t"
        "v_exp_f32 v90, v90\n\t"
        "v_exp_f32 v91, v91\n\t"
        "v_cvt_pk_bf16_f32 v154, v84, v85\n\t"
        "v_cvt_pk_bf16_f32 v155, v86, v87\n\t"
        "v_mfma_f32_32x32x16_bf16 v[0:15], v[172:175], v[148:151], v[0:15]\n\t"
        "v_exp_f32 v92, v92\n\t"
        "v_exp_f32 v93, v93\n\t"
        "v_exp_f32 v94, v94\n\t"
        "v_exp_f32 v95, v95\n\t"
        "v_cvt_pk_bf16_f32 v156, v88, v89\n\t"
        "v_cvt_pk_bf16_f32 v157, v90, v91\n\t"
        "v_cvt_pk_bf16_f32 v158, v92, v93\n\t"
        "v_cvt_pk_bf16_f32 v159, v94, v95\n\t"
        "v_mfma_f32_32x32x16_bf16 v[80:95], v[160:163], v[136:139], v[96:111]\n\t"
        "v_exp_f32 v64, v64\n\t"
        "v_exp_f32 v65, v65\n\t"
        "v_exp_f32 v66, v66\n\t"
        "v_exp_f32 v67, v67\n\t"
        "v_mfma_f32_32x32x16_bf16 v[80:95], v[164:167], v[140:143], v[80:95]\n\t"
        "v_exp_f32 v68, v68\n\t"
        "v_exp_f32 v69, v69\n\t"
        "v_exp_f32 v70, v70\n\t"
        "v_exp_f32 v71, v71\n\t"
        "v_cvt_pk_bf16_f32 v144, v64, v65\n\t"
        "v_cvt_pk_bf16_f32 v145, v66, v67\n\t"
        "v_mfma_f32_32x32x16_bf16 v[16:31], v[168:171], v[152:155], v[16:31]\n\t"
        "v_exp_f32 v72, v72\n\t"
        "v_exp_f32 v73, v73\n\t"
        "v_exp_f32 v74, v74\n\t"
        "v_exp_f32 v75, v75\n\t"
        "v_cvt_pk_bf16_f32 v146, v68, v69\n\t"
        "v_cvt_pk_bf16_f32 v147, v70, v71\n\t"
        "v_mfma_f32_32x32x16_bf16 v[16:31], v[172:175], v[156:159], v[16:31]\n\t"
        "v_exp_f32 v76, v76\n\t"
        "v_exp_f32 v77, v77\n\t"
        "v_exp_f32 v78, v78\n\t"
        "v_exp_f32 v79, v79\n\t"
        "v_cvt_pk_bf16_f32 v148, v72, v73\n\t"
        "v_cvt_pk_bf16_f32 v149, v74, v75\n\t"
        "v_cvt_pk_bf16_f32 v150, v76, v77\n\t"
        "v_cvt_pk_bf16_f32 v151, v78, v79\n\t"
        "s_waitcnt vmcnt(8)\n\t"
        "v_mfma_f32_32x32x16_bf16 v[64:79], v[176:179], v[112:115], v[96:111]\n\t"
        "v_exp_f32 v80, v80\n\t"
        "v_exp_f32 v81, v81\n\t"
        "v_exp_f32 v82, v82\n\t"
        "v_exp_f32 v83, v83\n\t"
        "v_mfma_f32_32x32x16_bf16 v[64:79], v[180:183], v[116:119], v[64:79]\n\t"
        "v_exp_f32 v84, v84\n\t"
        "v_exp_f32 v85, v85\n\t"
        "v_exp_f32 v86, v86\n\t"
        "v_exp_f32 v87, v87\n\t"
        "v_cvt_pk_bf16_f32 v152, v80, v81\n\t"
        "v_cvt_pk_bf16_f32 v153, v82, v83\n\t"
        "v_mfma_f32_32x32x16_bf16 v[32:47], v[168:171], v[144:147], v[32:47]\n\t"
        "v_exp_f32 v88, v88\n\t"
        "v_exp_f32 v89, v89\n\t"
        "v_exp_f32 v90, v90\n\t"
        "v_exp_f32 v91, v91\n\t"
        "v_cvt_pk_bf16_f32 v154, v84, v85\n\t"
        "v_cvt_pk_bf16_f32 v155, v86, v87\n\t"
        "v_mfma_f32_32x32x16_bf16 v[32:47], v[172:175], v[148:151], v[32:47]\n\t"
        "v_exp_f32 v92, v92\n\t"
        "v_exp_f32 v93, v93\n\t"
        "v_exp_f32 v94, v94\n\t"
        "v_exp_f32 v95, v95\n\t"
        "v_cvt_pk_bf16_f32 v156, v88, v89\n\t"
        "v_cvt_pk_bf16_f32 v157, v90, v91\n\t"
        "v_cvt_pk_bf16_f32 v158, v92, v93\n\t"
        "v_cvt_pk_bf16_f32 v159, v94, v95\n\t"
        "v_mfma_f32_32x32x16_bf16 v[80:95], v[176:179], v[120:123], v[96:111]\n\t"
        "v_exp_f32 v64, v64\n\t"
        "v_exp_f32 v65, v65\n\t"
        "v_exp_f32 v66, v66\n\t"
        "v_exp_f32 v67, v67\n\t"
        "v_mfma_f32_32x32x16_bf16 v[80:95], v[180:183], v[124:127], v[80:95]\n\t"
        "v_exp_f32 v68, v68\n\t"
        "v_exp_f32 v69, v69\n\t"
        "v_exp_f32 v70, v70\n\t"
        "v_exp_f32 v71, v71\n\t"
        "v_cvt_pk_bf16_f32 v144, v64, v65\n\t"
        "v_cvt_pk_bf16_f32 v145, v66, v67\n\t"
        "v_mfma_f32_32x32x16_bf16 v[48:63], v[168:171], v[152:155], v[48:63]\n\t"
        "v_exp_f32 v72, v72\n\t"
        "v_exp_f32 v73, v73\n\t"
        "v_exp_f32 v74, v74\n\t"
        "v_exp_f32 v75, v75\n\t"
        "v_cvt_pk_bf16_f32 v146, v68, v69\n\t"
        "v_cvt_pk_bf16_f32 v147, v70, v71\n\t"
        "v_mfma_f32_32x32x16_bf16 v[48:63], v[172:175], v[156:159], v[48:63]\n\t"
        "v_exp_f32 v76, v76\n\t"
        "v_exp_f32 v77, v77\n\t"
        "v_exp_f32 v78, v78\n\t"
        "v_exp_f32 v79, v79\n\t"
        "v_cvt_pk_bf16_f32 v148, v72, v73\n\t"
        "v_cvt_pk_bf16_f32 v149, v74, v75\n\t"
        "v_cvt_pk_bf16_f32 v150, v76, v77\n\t"
        "v_cvt_pk_bf16_f32 v151, v78, v79\n\t"
        "v_mfma_f32_32x32x16_bf16 v[64:79], v[176:179], v[128:131], v[96:111]\n\t"
        "v_exp_f32 v80, v80\n\t"
        "buffer_load_dwordx4 v[160:163], v224, %[rsk], %[sk] offen sc1\n\t"
        "v_exp_f32 v81, v81\n\t"
        "buffer_load_dwordx4 v[164:167], v224, %[rsk], %[sk] offen offset:1024 sc1\n\t"
        "v_exp_f32 v82, v82\n\t"
        "buffer_load_dwordx4 v[168:171], v225, %[rsv], %[sv] offen sc1\n\t"
        "v_exp_f32 v83, v83\n\t"
        "buffer_load_dwordx4 v[172:175], v226, %[rsv], %[sv] offen sc1\n\t"
        "v_mfma_f32_32x32x16_bf16 v[64:79], v[180:183], v[132:135], v[64:79]\n\t"
        "v_exp_f32 v84, v84\n\t"
        "s_add_u32 %[sk], %[sk], 28672\n\t"
        "v_exp_f32 v85, v85\n\t"
        "s_add_u32 %[sv], %[sv], 29696\n\t"
        "v_exp_f32 v86, v86\n\t"
        "v_exp_f32 v87, v87\n\t"
        "v_cvt_pk_bf16_f32 v152, v80, v81\n\t"
        "v_cvt_pk_bf16_f32 v153, v82, v83\n\t"
        "v_mfma_f32_32x32x16_bf16 v[0:15], v[184:187], v[144:147], v[0:15]\n\t"
        "v_exp_f32 v88, v88\n\t"
        "v_exp_f32 v89, v89\n\t"
        "v_exp_f32 v90, v90\n\t"
        "v_exp_f32 v91, v91\n\t"
        "v_cvt_pk_bf16_f32 v154, v84, v85\n\t"
        "v_cvt_pk_bf16_f32 v155, v86, v87\n\t"
        "v_mfma_f32_32x32x16_bf16 v[0:15], v[188:191], v[148:151], v[0:15]\n\t"
        "v_exp_f32 v92, v92\n\t"
        "v_exp_f32 v93, v93\n\t"
        "v_exp_f32 v94, v94\n\t"
        "v_exp_f32 v95, v95\n\t"
        "v_cvt_pk_bf16_f32 v156, v88, v89\n\t"
        "v_cvt_pk_bf16_f32 v157, v90, v91\n\t"
        "v_cvt_pk_bf16_f32 v158, v92, v93\n\t"
        "v_cvt_pk_bf16_f32 v159, v94, v95\n\t"
        "v_mfma_f32_32x32x16_bf16 v[80:95], v[176:179], v[136:139], v[96:111]\n\t"
        "v_exp_f32 v64, v64\n\t"
        "v_exp_f32 v65, v65\n\t"
        "v_exp_f32 v66, v66\n\t"
        "v_exp_f32 v67, v67\n\t"
        "v_mfma_f32_32x32x16_bf16 v[80:95], v[180:183], v[140:143], v[80:95]\n\t"
        "v_exp_f32 v68, v68\n\t"
        "v_exp_f32 v69, v69\n\t"
        "v_exp_f32 v70, v70\n\t"
        "v_exp_f32 v71, v71\n\t"
        "v_cvt_pk_bf16_f32 v144, v64, v65\n\t"
        "v_cvt_pk_bf16_f32 v145, v66, v67\n\t"
        "v_mfma_f32_32x32x16_bf16 v[16:31], v[184:187], v[152:155], v[16:31]\n\t"
        "v_exp_f32 v72, v72\n\t"
        "v_exp_f32 v73, v73\n\t"
        "v_exp_f32 v74, v74\n\t"
        "v_exp_f32 v75, v75\n\t"
        "v_cvt_pk_bf16_f32 v146, v68, v69\n\t"
        "v_cvt_pk_bf16_f32 v147, v70, v71\n\t"
        "v_mfma_f32_32x32x16_bf16 v[16:31], v[188:191], v[156:159], v[16:31]\n\t"
        "v_exp_f32 v76, v76\n\t"
        "v_exp_f32 v77, v77\n\t"
        "v_exp_f32 v78, v78\n\t"
        "v_exp_f32 v79, v79\n\t"
        "v_cvt_pk_bf16_f32 v148, v72, v73\n\t"
        "v_cvt_pk_bf16_f32 v149, v74, v75\n\t"
        "v_cvt_pk_bf16_f32 v150, v76, v77\n\t"
        "v_cvt_pk_bf16_f32 v151, v78, v79\n\t"
        "s_waitcnt vmcnt(8)\n\t"
        "v_mfma_f32_32x32x16_bf16 v[64:79], v[192:195], v[112:115], v[96:111]\n\t"
        "v_exp_f32 v80, v80\n\t"
        "v_exp_f32 v81, v81\n\t"
        "v_exp_f32 v82, v82\n\t"
        "v_exp_f32 v83, v83\n\t"
        "v_mfma_f32_32x32x16_bf16 v[64:79], v[196:199], v[116:119], v[64:79]\n\t"
        "v_exp_f32 v84, v84\n\t"
        "v_exp_f32 v85, v85\n\t"
        "v_exp_f32 v86, v86\n\t"
        "v_exp_f32 v87, v87\n\t"
        "v_cvt_pk_bf16_f32 v152, v80, v81\n\t"
        "v_cvt_pk_bf16_f32 v153, v82, v83\n\t"
        "v_mfma_f32_32x32x16_bf16 v[32:47], v[184:187], v[144:147], v[32:47]\n\t"
        "v_exp_f32 v88, v88\n\t"
        "v_exp_f32 v89, v89\n\t"
        "v_exp_f32 v90, v90\n\t"
        "v_exp_f32 v91, v91\n\t"
        "v_cvt_pk_bf16_f32 v154, v84, v85\n\t"
        "v_cvt_pk_bf16_f32 v155, v86, v87\n\t"
        "v_mfma_f32_32x32x16_bf16 v[32:47], v[188:191], v[148:151], v[32:47]\n\t"
        "v_exp_f32 v92, v92\n\t"
        "v_exp_f32 v93, v93\n\t"
        "v_exp_f32 v94, v94\n\t"
        "v_exp_f32 v95, v95\n\t"
        "v_cvt_pk_bf16_f32 v156, v88, v89\n\t"
        "v_cvt_pk_bf16_f32 v157, v90, v91\n\t"
        "v_cvt_pk_bf16_f32 v158, v92, v93\n\t"
        "v_cvt_pk_bf16_f32 v159, v94, v95\n\t"
        "v_mfma_f32_32x32x16_bf16 v[80:95], v[192:195], v[120:123], v[96:111]\n\t"
        "v_exp_f32 v64, v64\n\t"
        "v_exp_f32 v65, v65\n\t"
        "v_exp_f32 v66, v66\n\t"
        "v_exp_f32 v67, v67\n\t"
        "v_mfma_f32_32x32x16_bf16 v[80:95], v[196:199], v[124:127], v[80:95]\n\t"
        "v_exp_f32 v68, v68\n\t"
        "v_exp_f32 v69, v69\n\t"
        "v_exp_f32 v70, v70\n\t"
        "v_exp_f32 v71, v71\n\t"
        "v_cvt_pk_bf16_f32 v144, v64, v65\n\t"
        "v_cvt_pk_bf16_f32 v145, v66, v67\n\t"
        "v_mfma_f32_32x32x16_bf16 v[48:63], v[184:187], v[152:155], v[48:63]\n\t"
        "v_exp_f32 v72, v72\n\t"
        "v_exp_f32 v73, v73\n\t"
        "v_exp_f32 v74, v74\n\t"
        "v_exp_f32 v75, v75\n\t"
        "v_cvt_pk_bf16_f32 v146, v68, v69\n\t"
        "v_cvt_pk_bf16_f32 v147, v70, v71\n\t"
        "v_mfma_f32_32x32x16_bf16 v[48:63], v[188:191], v[156:159], v[48:63]\n\t"
        "v_exp_f32 v76, v76\n\t"
        "v_exp_f32 v77, v77\n\t"
        "v_exp_f32 v78, v78\n\t"
        "v_exp_f32 v79, v79\n\t"
        "v_cvt_pk_bf16_f32 v148, v72, v73\n\t"
        "v_cvt_pk_bf16_f32 v149, v74, v75\n\t"
        "v_cvt_pk_bf16_f32 v150, v76, v77\n\t"
        "v_cvt_pk_bf16_f32 v151, v78, v79\n\t"
        "v_mfma_f32_32x32x16_bf16 v[64:79], v[192:195], v[128:131], v[96:111]\n\t"
        "v_exp_f32 v80, v80\n\t"
        "buffer_load_dwordx4 v[176:179], v224, %[rsk], %[sk] offen sc1\n\t"
        "v_exp_f32 v81, v81\n\t"
        "buffer_load_dwordx4 v[180:183], v224, %[rsk], %[sk] offen offset:1024 sc1\n\t"
        "v_exp_f32 v82, v82\n\t"
        "buffer_load_dwordx4 v[184:187], v225, %[rsv], %[sv] offen sc1\n\t"
        "v_exp_f32 v83, v83\n\t"
        "buffer_load_dwordx4 v[188:191], v226, %[rsv], %[sv] offen sc1\n\t"
        "v_mfma_f32_32x32x16_bf16 v[64:79], v[196:199], v[132:135], v[64:79]\n\t"
        "v_exp_f32 v84, v84\n\t"
        "s_add_u32 %[sk], %[sk], 28672\n\t"
        "v_exp_f32 v85, v85\n\t"
        "s_add_u32 %[sv], %[sv], 29696\n\t"
        "v_exp_f32 v86, v86\n\t"
        "v_exp_f32 v87, v87\n\t"
        "v_cvt_pk_bf16_f32 v152, v80, v81\n\t"
        "v_cvt_pk_bf16_f32 v153, v82, v83\n\t"
        "v_mfma_f32_32x32x16_bf16 v[0:15], v[200:203], v[144:147], v[0:15]\n\t"
        "v_exp_f32 v88, v88\n\t"
        "v_exp_f32 v89, v89\n\t"
        "v_exp_f32 v90, v90\n\t"
        "v_exp_f32 v91, v91\n\t"
        "v_cvt_pk_bf16_f32 v154, v84, v85\n\t"
        "v_cvt_pk_bf16_f32 v155, v86, v87\n\t"
        "v_mfma_f32_32x32x16_bf16 v[0:15], v[204:207], v[148:151], v[0:15]\n\t"
        "v_exp_f32 v92, v92\n\t"
        "v_exp_f32 v93, v93\n\t"
        "v_exp_f32 v94, v94\n\t"
        "v_exp_f32 v95, v95\n\t"
        "v_cvt_pk_bf16_f32 v156, v88, v89\n\t"
        "v_cvt_pk_bf16_f32 v157, v90, v91\n\t"
        "v_cvt_pk_bf16_f32 v158, v92, v93\n\t"
        "v_cvt_pk_bf16_f32 v159, v94, v95\n\t"
        "v_mfma_f32_32x32x16_bf16 v[80:95], v[192:195], v[136:139], v[96:111]\n\t"
        "v_exp_f32 v64, v64\n\t"
        "v_exp_f32 v65, v65\n\t"
        "v_exp_f32 v66, v66\n\t"
        "v_exp_f32 v67, v67\n\t"
        "v_mfma_f32_32x32x16_bf16 v[80:95], v[196:199], v[140:143], v[80:95]\n\t"
        "v_exp_f32 v68, v68\n\t"
        "v_exp_f32 v69, v69\n\t"
        "v_exp_f32 v70, v70\n\t"
        "v_exp_f32 v71, v71\n\t"
        "v_cvt_pk_bf16_f32 v144, v64, v65\n\t"
        "v_cvt_pk_bf16_f32 v145, v66, v67\n\t"
        "v_mfma_f32_32x32x16_bf16 v[16:31], v[200:203], v[152:155], v[16:31]\n\t"
        "v_exp_f32 v72, v72\n\t"
        "v_exp_f32 v73, v73\n\t"
        "v_exp_f32 v74, v74\n\t"
        "v_exp_f32 v75, v75\n\t"
        "v_cvt_pk_bf16_f32 v146, v68, v69\n\t"
        "v_cvt_pk_bf16_f32 v147, v70, v71\n\t"
        "v_mfma_f32_32x32x16_bf16 v[16:31], v[204:207], v[156:159], v[16:31]\n\t"
        "v_exp_f32 v76, v76\n\t"
        "v_exp_f32 v77, v77\n\t"
        "v_exp_f32 v78, v78\n\t"
        "v_exp_f32 v79, v79\n\t"
        "v_cvt_pk_bf16_f32 v148, v72, v73\n\t"
        "v_cvt_pk_bf16_f32 v149, v74, v75\n\t"
        "v_cvt_pk_bf16_f32 v150, v76, v77\n\t"
        "v_cvt_pk_bf16_f32 v151, v78, v79\n\t"
        "s_waitcnt vmcnt(8)\n\t"
        "v_mfma_f32_32x32x16_bf16 v[64:79], v[208:211], v[112:115], v[96:111]\n\t"
        "v_exp_f32 v80, v80\n\t"
        "v_exp_f32 v81, v81\n\t"
        "v_exp_f32 v82, v82\n\t"
        "v_exp_f32 v83, v83\n\t"
        "v_mfma_f32_32x32x16_bf16 v[64:79], v[212:215], v[116:119], v[64:79]\n\t"
        "v_exp_f32 v84, v84\n\t"
        "v_exp_f32 v85, v85\n\t"
        "v_exp_f32 v86, v86\n\t"
        "v_exp_f32 v87, v87\n\t"
        "v_cvt_pk_bf16_f32 v152, v80, v81\n\t"
        "v_cvt_pk_bf16_f32 v153, v82, v83\n\t"
        "v_mfma_f32_32x32x16_bf16 v[32:47], v[200:203], v[144:147], v[32:47]\n\t"
        "v_exp_f32 v88, v88\n\t"
        "v_exp_f32 v89, v89\n\t"
        "v_exp_f32 v90, v90\n\t"
        "v_exp_f32 v91, v91\n\t"
        "v_cvt_pk_bf16_f32 v154, v84, v85\n\t"
        "v_cvt_pk_bf16_f32 v155, v86, v87\n\t"
        "v_mfma_f32_32x32x16_bf16 v[32:47], v[204:207], v[148:151], v[32:47]\n\t"
        "v_exp_f32 v92, v92\n\t"
        "v_exp_f32 v93, v93\n\t"
        "v_exp_f32 v94, v94\n\t"
        "v_exp_f32 v95, v95\n\t"
        "v_cvt_pk_bf16_f32 v156, v88, v89\n\t"
        "v_cvt_pk_bf16_f32 v157, v90, v91\n\t"
        "v_cvt_pk_bf16_f32 v158, v92, v93\n\t"
        "v_cvt_pk_bf16_f32 v159, v94, v95\n\t"
        "v_mfma_f32_32x32x16_bf16 v[80:95], v[208:211], v[120:123], v[96:111]\n\t"
        "v_exp_f32 v64, v64\n\t"
        "v_exp_f32 v65, v65\n\t"
        "v_exp_f32 v66, v66\n\t"
        "v_exp_f32 v67, v67\n\t"
        "v_mfma_f32_32x32x16_bf16 v[80:95], v[212:215], v[124:127], v[80:95]\n\t"
        "v_exp_f32 v68, v68\n\t"
        "v_exp_f32 v69, v69\n\t"
        "v_exp_f32 v70, v70\n\t"
        "v_exp_f32 v71, v71\n\t"
        "v_cvt_pk_bf16_f32 v144, v64, v65\n\t"
        "v_cvt_pk_bf16_f32 v145, v66, v67\n\t"
        "v_mfma_f32_32x32x16_bf16 v[48:63], v[200:203], v[152:155], v[48:63]\n\t"
        "v_exp_f32 v72, v72\n\t"
        "v_exp_f32 v73, v73\n\t"
        "v_exp_f32 v74, v74\n\t"
        "v_exp_f32 v75, v75\n\t"
        "v_cvt_pk_bf16_f32 v146, v68, v69\n\t"
        "v_cvt_pk_bf16_f32 v147, v70, v71\n\t"
        "v_mfma_f32_32x32x16_bf16 v[48:63], v[204:207], v[156:159], v[48:63]\n\t"
        "v_exp_f32 v76, v76\n\t"
        "v_exp_f32 v77, v77\n\t"
        "v_exp_f32 v78, v78\n\t"
        "v_exp_f32 v79, v79\n\t"
        "v_cvt_pk_bf16_f32 v148, v72, v73\n\t"
        "v_cvt_pk_bf16_f32 v149, v74, v75\n\t"
        "v_cvt_pk_bf16_f32 v150, v76, v77\n\t"
        "v_cvt_pk_bf16_f32 v151, v78, v79\n\t"
        "v_mfma_f32_32x32x16_bf16 v[64:79], v[208:211], v[128:131], v[96:111]\n\t"
        "v_exp_f32 v80, v80\n\t"
        "buffer_load_dwordx4 v[192:195], v224, %[rsk], %[sk] offen sc1\n\t"
        "v_exp_f32 v81, v81\n\t"
        "buffer_load_dwordx4 v[196:199], v224, %[rsk], %[sk] offen offset:1024 sc1\n\t"
        "v_exp_f32 v82, v82\n\t"
        "buffer_load_dwordx4 v[200:203], v225, %[rsv], %[sv] offen sc1\n\t"
        "v_exp_f32 v83, v83\n\t"
        "buffer_load_dwordx4 v[204:207], v226, %[rsv], %[sv] offen sc1\n\t"
        "v_mfma_f32_32x32x16_bf16 v[64:79], v[212:215], v[132:135], v[64:79]\n\t"
        "v_exp_f32 v84, v84\n\t"
        "s_add_u32 %[sk], %[sk], 28672\n\t"
        "v_exp_f32 v85, v85\n\t"
        "s_add_u32 %[sv], %[sv], 29696\n\t"
        "v_exp_f32 v86, v86\n\t"
        "v_exp_f32 v87, v87\n\t"
        "v_cvt_pk_bf16_f32 v152, v80, v81\n\t"
        "v_cvt_pk_bf16_f32 v153, v82, v83\n\t"
        "v_mfma_f32_32x32x16_bf16 v[0:15], v[216:219], v[144:147], v[0:15]\n\t"
        "v_exp_f32 v88, v88\n\t"
        "v_exp_f32 v89, v89\n\t"
        "v_exp_f32 v90, v90\n\t"
        "v_exp_f32 v91, v91\n\t"
        "v_cvt_pk_bf16_f32 v154, v84, v85\n\t"
        "v_cvt_pk_bf16_f32 v155, v86, v87\n\t"
        "v_mfma_f32_32x32x16_bf16 v[0:15], v[220:223], v[148:151], v[0:15]\n\t"
        "v_exp_f32 v92, v92\n\t"
        "v_exp_f32 v93, v93\n\t"
        "v_exp_f32 v94, v94\n\t"
        "v_exp_f32 v95, v95\n\t"
        "v_cvt_pk_bf16_f32 v156, v88, v89\n\t"
        "v_cvt_pk_bf16_f32 v157, v90, v91\n\t"
        "v_cvt_pk_bf16_f32 v158, v92, v93\n\t"
        "v_cvt_pk_bf16_f32 v159, v94, v95\n\t"
        "v_mfma_f32_32x32x16_bf16 v[80:95], v[208:211], v[136:139], v[96:111]\n\t"
        "v_exp_f32 v64, v64\n\t"
        "v_exp_f32 v65, v65\n\t"
        "v_exp_f32 v66, v66\n\t"
        "v_exp_f32 v67, v67\n\t"
        "v_mfma_f32_32x32x16_bf16 v[80:95], v[212:215], v[140:143], v[80:95]\n\t"
        "v_exp_f32 v68, v68\n\t"
        "v_exp_f32 v69, v69\n\t"
        "v_exp_f32 v70, v70\n\t"
        "v_exp_f32 v71, v71\n\t"
        "v_cvt_pk_bf16_f32 v144, v64, v65\n\t"
        "v_cvt_pk_bf16_f32 v145, v66, v67\n\t"
        "v_mfma_f32_32x32x16_bf16 v[16:31], v[216:219], v[152:155], v[16:31]\n\t"
        "v_exp_f32 v72, v72\n\t"
        "v_exp_f32 v73, v73\n\t"
        "v_exp_f32 v74, v74\n\t"
        "v_exp_f32 v75, v75\n\t"
        "v_cvt_pk_bf16_f32 v146, v68, v69\n\t"
        "v_cvt_pk_bf16_f32 v147, v70, v71\n\t"
        "v_mfma_f32_32x32x16_bf16 v[16:31], v[220:223], v[156:159], v[16:31]\n\t"
        "v_exp_f32 v76, v76\n\t"
        "v_exp_f32 v77, v77\n\t"
        "v_exp_f32 v78, v78\n\t"
        "v_exp_f32 v79, v79\n\t"
        "v_cvt_pk_bf16_f32 v148, v72, v73\n\t"
        "v_cvt_pk_bf16_f32 v149, v74, v75\n\t"
        "v_cvt_pk_bf16_f32 v150, v76, v77\n\t"
        "v_cvt_pk_bf16_f32 v151, v78, v79\n\t"
        "s_waitcnt vmcnt(8)\n\t"
        "v_mfma_f32_32x32x16_bf16 v[64:79], v[160:163], v[112:115], v[96:111]\n\t"
        "v_exp_f32 v80, v80\n\t"
        "s_sub_u32 %[cnt], %[cnt], 1\n\t"
        "v_exp_f32 v81, v81\n\t"
        "s_cmp_lg_u32 %[cnt], 0\n\t"
        "v_exp_f32 v82, v82\n\t"
        "v_exp_f32 v83, v83\n\t"
        "v_mfma_f32_32x32x16_bf16 v[64:79], v[164:167], v[116:119], v[64:79]\n\t"
        "v_exp_f32 v84, v84\n\t"
        "v_exp_f32 v85, v85\n\t"
        "v_exp_f32 v86, v86\n\t"
        "v_exp_f32 v87, v87\n\t"
        "v_cvt_pk_bf16_f32 v152, v80, v81\n\t"
        "v_cvt_pk_bf16_f32 v153, v82, v83\n\t"
        "v_mfma_f32_32x32x16_bf16 v[32:47], v[216:219], v[144:147], v[32:47]\n\t"
        "v_exp_f32 v88, v88\n\t"
        "v_exp_f32 v89, v89\n\t"
        "v_exp_f32 v90, v90\n\t"
        "v_exp_f32 v91, v91\n\t"
        "v_cvt_pk_bf16_f32 v154, v84, v85\n\t"
        "v_cvt_pk_bf16_f32 v155, v86, v87\n\t"
        "v_mfma_f32_32x32x16_bf16 v[32:47], v[220:223], v[148:151], v[32:47]\n\t"
        "v_exp_f32 v92, v92\n\t"
        "v_exp_f32 v93, v93\n\t"
        "v_exp_f32 v94, v94\n\t"
        "v_exp_f32 v95, v95\n\t"
        "v_cvt_pk_bf16_f32 v156, v88, v89\n\t"
        "v_cvt_pk_bf16_f32 v157, v90, v91\n\t"
        "v_cvt_pk_bf16_f32 v158, v92, v93\n\t"
        "v_cvt_pk_bf16_f32 v159, v94, v95\n\t"
        "s_cbranch_scc1 LOOP%=\n\t"
        "v_mfma_f32_32x32x16_bf16 v[48:63], v[216:219], v[152:155], v[48:63]\n\t"
        "v_mfma_f32_32x32x16_bf16 v[48:63], v[220:223], v[156:159], v[48:63]\n\t"
        "s_nop 15\n\t"
        : "={v[0:15]}"(o0), "={v[16:31]}"(o1), "={v[32:47]}"(o2), "={v[48:63]}"(o3), [sk] "+s"(sk), [sv] "+s"(sv), [cnt] "+s"(cnt),
          "+{v228}"(q2a), "+{v229}"(kba)
        : "{v224}"(vok), "{v225}"(vov), "{v226}"(vov + 2 * VROW), "{v227}"(lds0 + L_QX + w * 2048 + lane * 16),
          [rsk] "s"(rsK), [rsv] "s"(rsV)
        : "v64", "v65", "v66", "v67", "v68", "v69", "v70", "v71", "v72", "v73", "v74", "v75", "v76", "v77", "v78", "v79", "v80", "v81", "v82", "v83", "v84", "v85", "v86", "v87", "v88", "v89", "v90", "v91", "v92", "v93", "v94", "v95", "v96", "v97", "v98", "v99", "v100", "v101", "v102", "v103", "v104", "v105", "v106", "v107", "v108", "v109", "v110", "v111", "v112", "v113", "v114", "v115", "v116", "v117", "v118", "v119", "v120", "v121", "v122", "v123", "v124", "v125", "v126", "v127", "v128", "v129", "v130", "v131", "v132", "v133", "v134", "v135", "v136", "v137", "v138", "v139", "v140", "v141", "v142", "v143", "v144", "v145", "v146", "v147", "v148", "v149", "v150", "v151", "v152", "v153", "v154", "v155", "v156", "v157", "v158", "v159", "v160", "v161", "v162", "v163", "v164", "v165", "v166", "v167", "v168", "v169", "v170", "v171", "v172", "v173", "v174", "v175", "v176", "v177", "v178", "v179", "v180", "v181", "v182", "v183", "v184", "v185", "v186", "v187", "v188", "v189", "v190", "v191", "v192", "v193", "v194", "v195", "v196", "v197", "v198", "v199", "v200", "v201", "v202", "v203", "v204", "v205", "v206", "v207", "v208", "v209", "v210", "v211", "v212", "v213", "v214", "v215", "v216", "v217", "v218", "v219", "v220", "v221", "v222", "v223", "scc", "memory");
    }
    STAMP(7);
#define PUT(i, o) do { LAS char* d_ = lds + L_CB + (w * 3 + (i - 1)) * 4096 + lane * 16;                                       \
        _Pragma("unroll") for (int g = 0; g < 4; ++g) *(LAS f32x4*)(d_ + g * 1024) = (f32x4){o[4 * g], o[4 * g + 1], o[4 * g + 2], o[4 * g + 3]}; } while (0)
    PUT(1, o1); PUT(2, o2); PUT(3, o3);
    BAR();
    f32x16 acc = o0;
#pragma unroll
    for (int s = 1; s < 4; ++s) {
        LAS const char* sp = lds + L_CB + (((w - s) & 3) * 3 + (s - 1)) * 4096 + lane * 16;
#pragma unroll
        for (int g = 0; g < 4; ++g) {
            const f32x4 v = *(LAS const f32x4*)(sp + g * 1024);
            acc[4 * g] += v[0]; acc[4 * g + 1] += v[1]; acc[4 * g + 2] += v[2]; acc[4 * g + 3] += v[3];
        }
    }
    float l = half_hi(acc[12]);
    if (__builtin_expect(__any(!(l >= L_MIN)), 0)) {
        const __amdgpu_buffer_rsrc_t rsK = mkrs(ws + WS_K, 64 * KT), rsV = mkrs(ws + WS_V, 64 * VT);
        const f32x16 zero = {};
        float m = -__builtin_inff();
#pragma unroll 1
        for (int t = 0; t < 64; ++t) {
            f32x16 s = MFMA(asbf(ld16_wt(rsK, vok, t * KT)), asbf(qo0), zero);
            s = MFMA(asbf(ld16_wt(rsK, vok + 1024, t * KT)), asbf(qo1), s);
#pragma unroll
            for (int i = 0; i < 16; ++i) m = __builtin_fmaxf(m, s[i]);
        }
        m = __builtin_fmaxf(half_lo(m), half_hi(m));
        acc = zero;
#pragma unroll 1
        for (int t = 0; t < 64; ++t) {
            f32x16 s = MFMA(asbf(ld16_wt(rsK, vok, t * KT)), asbf(qo0), zero);
            s = MFMA(asbf(ld16_wt(rsK, vok + 1024, t * KT)), asbf(qo1), s);
#pragma unroll
            for (int i = 0; i < 16; ++i) s[i] = __builtin_amdgcn_exp2f(s[i] - m);
            acc = MFMA(asbf(ld16_wt(rsV, vov, t * VT)), asbf(pack8(s, 0)), acc);
            acc = MFMA(asbf(ld16_wt(rsV, vov + 2 * VROW, t * VT)), asbf(pack8(s, 1)), acc);
        }
        l = half_hi(acc[12]);
    }
    {
        const float rl = __builtin_amdgcn_rcpf(l);
#pragma unroll
        for (int i = 0; i < 16; ++i) acc[i] = (i >= 12 && h) ? 0.f : acc[i] * rl;
        const unsigned so = (kt * NH + hd) * 2048;
        st16_wt(pack8(acc, 0), rsOI, lane * 16, so);
        st16_wt(pack8(acc, 1), rsOI, lane * 16 + 1024, so);
    }
    STAMP(8);
    asm volatile("s_waitcnt vmcnt(0) lgkmcnt(0)\n\ts_barrier" ::: "memory");
    STAMP(9);

    bf16x8 wf[32];
    {
        LAS const char* wb = lds + L_WO + (4 * h + q4) * 256 + ((w ^ q4) * 64) + g16 * 32 + p4 * 8;
#pragma unroll
        for (int k = 0; k < 32; ++k) {
            const int ro = ((k >> 1) * DH + (k & 1) * 16) * 256;
            wf[k] = cat(vtr(wb + ro), vtr(wb + ro + 2048 - (k == 31 ? h * 1024 : 0)));
        }
    }
    BAR();
    STAMP(10);
    {
        u32x4 t[4];
#pragma unroll
        for (int f = 0; f < 4; ++f) t[f] = ld16_wt(rsOI, lane * 16 + f * 1024, q32 * (NH * 2048) + wv * 4096);
#pragma unroll
        for (int f = 0; f < 4; ++f) *(LAS u32x4*)(lds + L_CB + (wv * 4 + f) * 1024 + lane * 16) = t[f];
    }
    BAR();
    STAMP(11);
    if (cg < 3 || w < 2) {
        f32x16 c = {};
#pragma unroll
        for (int k = 0; k < 32; ++k) c = MFMA(wf[k], *(LAS const bf16x8*)(lds + L_CB + k * 1024 + lane * 16), c);
        STAMP(12);
        char* orow = out + (size_t)(q32 * 32 + r) * (DM * 2) + cg * 256 + w * 64 + h * 16;
#pragma unroll
        for (int g = 0; g < 4; g += 2) {
            const unsigned x0 = cvtpk(c[4 * g], c[4 * g + 1]), x1 = cvtpk(c[4 * g + 2], c[4 * g + 3]);
            const unsigned y0 = cvtpk(c[4 * g + 4], c[4 * g + 5]), y1 = cvtpk(c[4 * g + 6], c[4 * g + 7]);
            auto s0 = __builtin_amdgcn_permlane32_swap(x0, y0, false, false);
            auto s1 = __builtin_amdgcn_permlane32_swap(x1, y1, false, false);
            *(u32x4*)(orow + g * 16) = (u32x4){s0[0], s1[0], s0[1], s1[1]};
        }
    }
    STAMP(13);
    STAMP_FLUSH();
}

extern "C" void kernel_launch(void* const* d_in, const int* in_sizes, int n_in, void* d_out, int out_size, void* d_ws, size_t ws_size,
                              hipStream_t stream) {
    (void)hipFuncSetAttribute((const void*)mha_kernel, hipFuncAttributeMaxDynamicSharedMemorySize, LDS_BYTES);
    mha_kernel<<<256, 512, LDS_BYTES, stream>>>((const char*)d_in[0], (const char*)d_in[1], (const char*)d_in[2], (char*)d_out, (char*)d_ws);
}
```

```cpp
#include <hip/hip_runtime.h>
#include <hip/hip_bf16.h>
#include <stdint.h>

typedef short bf16x8 __attribute__((ext_vector_type(8)));
typedef short s16x4 __attribute__((ext_vector_type(4)));
typedef float f32x16 __attribute__((ext_vector_type(16)));
typedef float f32x4 __attribute__((ext_vector_type(4)));
typedef float f32x2 __attribute__((ext_vector_type(2)));
typedef unsigned u32x4 __attribute__((ext_vector_type(4)));
typedef __bf16 bf16x2_t __attribute__((ext_vector_type(2)));
typedef short v4i16_t __attribute__((ext_vector_type(4)));
typedef unsigned long long u64;
#define LAS __attribute__((address_space(3)))
#define GAS __attribute__((address_space(1)))
#define DI __device__ __forceinline__
#define MFMA(a, b, c) __builtin_amdgcn_mfma_f32_32x32x16_bf16((a), (b), (c), 0, 0, 0)
#define SBAR() __builtin_amdgcn_sched_barrier(0)
#define RLX_AGENT __ATOMIC_RELAXED, __HIP_MEMORY_SCOPE_AGENT
#define BAR() asm volatile("s_waitcnt lgkmcnt(0)\n\ts_barrier" ::: "memory")

#define PRETOUCH 2
#define MAPPING 1
constexpr int SEQ = 2048, DM = 448, NH = 16, DH = 28, N3 = 1344;
constexpr float QSCALE = 0.18898223650461362f * 1.4426950408889634f;
constexpr float L_MIN = 9.094947017729282e-13f, L_MAX = 1.099511627776e12f;
constexpr unsigned VROW = 464 * 16, TS = 4 * VROW;
constexpr unsigned WS_K = 0, WS_V = 2u << 20, WS_O = 4u << 20, WS_F = 6u << 20;
constexpr unsigned POISON = 0xffffffffu;
constexpr unsigned F_G = 0, F_R = 4096, F_KV = 131072, F_O = 262144;
constexpr unsigned L_WQ = 0, L_RING = 28672, STG = 24576;
constexpr unsigned L_QX = 131072, L_Q2 = L_QX + 16384, L_GB = L_Q2 + 512;
constexpr unsigned L_WO = 0, L_CB = 114688;
constexpr unsigned LDS_BYTES = 163840;

#define STAMP_DECL
#define STAMP(i)
#define STAMP_FLUSH()

DI unsigned rfl(unsigned x) { return __builtin_amdgcn_readfirstlane(x); }
DI unsigned cvtpk(float lo, float hi) { f32x2 v = {lo, hi}; bf16x2_t b = __builtin_convertvector(v, bf16x2_t); return __builtin_bit_cast(unsigned, b); }
DI u32x4 pack8(const f32x16& x, int s) {
    return (u32x4){cvtpk(x[8 * s], x[8 * s + 1]), cvtpk(x[8 * s + 2], x[8 * s + 3]), cvtpk(x[8 * s + 4], x[8 * s + 5]), cvtpk(x[8 * s + 6], x[8 * s + 7])};
}
DI bf16x8 asbf(u32x4 x) { return __builtin_bit_cast(bf16x8, x); }
DI s16x4 vtr(LAS const char* p) { return __builtin_bit_cast(s16x4, __builtin_amdgcn_ds_read_tr16_b64_v4i16((LAS v4i16_t*)p)); }
DI bf16x8 cat(s16x4 lo, s16x4 hi) { return __builtin_shufflevector(lo, hi, 0, 1, 2, 3, 4, 5, 6, 7); }
DI float half_lo(float x) { auto r = __builtin_amdgcn_permlane32_swap(__float_as_uint(x), __float_as_uint(x), false, false); return __uint_as_float(r[0]); }
DI float half_hi(float x) { auto r = __builtin_amdgcn_permlane32_swap(__float_as_uint(x), __float_as_uint(x), false, false); return __uint_as_float(r[1]); }
DI unsigned wave_max_u(unsigned x) {
#define DPPMAX(ctrl, rm) x = max(x, (unsigned)__builtin_amdgcn_update_dpp((int)x, (int)x, ctrl, rm, 0xf, false))
    DPPMAX(0x111, 0xf); DPPMAX(0x112, 0xf); DPPMAX(0x114, 0xf); DPPMAX(0x118, 0xf); DPPMAX(0x142, 0xa); DPPMAX(0x143, 0xc);
#undef DPPMAX
    return (unsigned)__builtin_amdgcn_readlane((int)x, 63);
}
DI void dma16(unsigned lds_dst, unsigned voff, __amdgpu_buffer_rsrc_t rs, unsigned soff) {
    asm volatile("s_mov_b32 m0, %0\n\ts_nop 0\n\tbuffer_load_dwordx4 %1, %2, %3 offen lds" ::"s"(lds_dst), "v"(voff), "s"(rs), "s"(soff) : "m0", "memory");
}
DI void st16_wt(u32x4 v, __amdgpu_buffer_rsrc_t rs, unsigned voff, unsigned soff) { __builtin_amdgcn_raw_buffer_store_b128(v, rs, voff, soff, 16); }
DI u32x4 ld16_wt(__amdgpu_buffer_rsrc_t rs, unsigned voff, unsigned soff) { return __builtin_amdgcn_raw_buffer_load_b128(rs, voff, soff, 16); }
DI __amdgpu_buffer_rsrc_t mkrs(const void* p, unsigned bytes) { return __builtin_amdgcn_make_buffer_rsrc((void*)p, (short)0, (int)bytes, 0x00020000); }
DI unsigned poll_tags(const GAS u64* g, unsigned tag) {
    u64 v = __hip_atomic_load(g, RLX_AGENT);
#pragma nounroll
    for (unsigned spins = 0; spins < (1u << 16); ++spins) {
        if (__all((unsigned)v == tag)) break;
        if ((unsigned)v != tag) v = __hip_atomic_load(g, RLX_AGENT);
    }
    __builtin_amdgcn_fence(__ATOMIC_ACQUIRE, "wavefront");
    return (unsigned)(v >> 32);
}
DI unsigned touch(const GAS char* p) { unsigned t; asm volatile("global_load_dword %0, %1, off" : "=v"(t) : "v"(p) : "memory"); return t; }

template <bool ISK>
DI void kv_gemm(LAS char* lds, unsigned w, int lane, bf16x8 (&xf)[7][4], f32x16& a0, f32x16& a1) {
    const int r = lane & 31, h = lane >> 5, i16 = lane & 15, q4 = i16 >> 2, p4 = i16 & 3, g16 = (lane >> 4) & 1;
    const int pc = ISK ? (((p4 & 1) << 1) | (p4 >> 1)) : p4;
    LAS const char* xb = lds + L_RING + w * 4096 + r * 128;
    LAS const char* wb0 = lds + L_RING + 16384 + (8 * h + q4) * 128 + ((q4 >> 1) * 64) + g16 * 32 + pc * 8;
    LAS const char* wb1 = lds + L_RING + 16384 + (8 * h + q4) * 128 + (((q4 >> 1) ^ 1) * 64) + g16 * 32 + pc * 8;
    const int xs = (r >> 1) & 7;
    bf16x8 wf[2][4][2];
#define RD(i) do { const int so = ((i) % 5) * STG; _Pragma("unroll") for (int ks = 0; ks < 4; ++ks) {                          \
        xf[i][ks] = *(LAS const bf16x8*)(xb + so + (((2 * ks + h) ^ xs) * 16));                                                \
        wf[(i) & 1][ks][0] = cat(vtr(wb0 + so + ks * 2048), vtr(wb0 + so + ks * 2048 + 512));                                  \
        wf[(i) & 1][ks][1] = cat(vtr(wb1 + so + ks * 2048), vtr(wb1 + so + ks * 2048 + 512)); } } while (0)
#define MM(i) do { _Pragma("unroll") for (int ks = 0; ks < 4; ++ks) {                                                          \
        if (ISK) { a0 = MFMA(wf[(i) & 1][ks][0], xf[i][ks], a0); a1 = MFMA(wf[(i) & 1][ks][1], xf[i][ks], a1); }               \
        else     { a0 = MFMA(xf[i][ks], wf[(i) & 1][ks][0], a0); a1 = MFMA(xf[i][ks], wf[(i) & 1][ks][1], a1); } } } while (0)
    BAR(); RD(0);
#pragma unroll
    for (int i = 0; i < 5; ++i) { BAR(); RD(i + 1); SBAR(); MM(i); SBAR(); }
    MM(5); SBAR();
    BAR(); RD(6); MM(6);
#undef RD
#undef MM
}

DI void stage_dma(unsigned mw, unsigned mx, unsigned sw, unsigned sx, unsigned wvo, unsigned xvo0, unsigned xvo1, __amdgpu_buffer_rsrc_t rsW,
                  __amdgpu_buffer_rsrc_t rsX) {
    unsigned t;
    asm volatile("s_mov_b32 m0, %1\n\ts_add_u32 %0, %3, 21504\n\tbuffer_load_dwordx4 %5, %8, %3 offen lds\n\t"
                 "s_add_u32 m0, m0, 1024\n\ts_nop 0\n\tbuffer_load_dwordx4 %5, %8, %0 offen lds\n\t"
                 "s_mov_b32 m0, %2\n\ts_add_u32 %0, %4, 7168\n\tbuffer_load_dwordx4 %6, %9, %4 offen lds\n\t"
                 "s_add_u32 m0, m0, 1024\n\ts_nop 0\n\tbuffer_load_dwordx4 %7, %9, %0 offen lds\n\t"
                 "s_add_u32 m0, m0, 1024\n\ts_add_u32 %0, %4, 14336\n\tbuffer_load_dwordx4 %6, %9, %0 offen lds\n\t"
                 "s_add_u32 m0, m0, 1024\n\ts_add_u32 %0, %4, 21504\n\tbuffer_load_dwordx4 %7, %9, %0 offen lds"
                 : "=&s"(t) : "s"(mw), "s"(mx), "s"(sw), "s"(sx), "v"(wvo), "v"(xvo0), "v"(xvo1), "s"(rsW), "s"(rsX) : "m0", "scc", "memory");
}

__global__ __launch_bounds__(512, 2) void mha_kernel(const char* __restrict__ x, const char* __restrict__ wqkv, const char* __restrict__ wo,
                                                     char* __restrict__ out, char* __restrict__ ws) {
    extern __shared__ __attribute__((aligned(16))) char lds_[];
    LAS char* lds = (LAS char*)lds_;
    STAMP_DECL STAMP(0);
    unsigned long long pc0;
    asm volatile("s_getpc_b64 %0" : "=s"(pc0));
    const int lane = threadIdx.x & 63;
    const unsigned wv = rfl(threadIdx.x >> 6), w = wv & 3;
    const int r = lane & 31, h = lane >> 5, i16 = lane & 15, q4 = i16 >> 2, p4 = i16 & 3, g16 = (lane >> 4) & 1;
    const unsigned b = blockIdx.x, xcd = b & 7, idx = b >> 3;
    const unsigned hd = xcd * 2 + (idx & 1), qt = idx >> 1;
    const unsigned ch = hd < 14 ? hd : hd - 14;
    const bool isK = ch < 7;
    const unsigned rot = (hd + 2 * qt) % 7;
    const unsigned g0 = (7 * hd) >> 1;
    const unsigned cg = hd >> 2, q32 = qt * 4 + (hd & 3);
    const unsigned lds0 = (unsigned)(uintptr_t)lds_;
    unsigned kbi[7];
#pragma unroll
    for (int i = 0; i < 7; ++i) { const unsigned k = rot + i; kbi[i] = k >= 7 ? k - 7 : k; }
    const unsigned xrow = qt * 128 + w * 32 + (lane >> 3);
    const unsigned xvo0 = xrow * (DM * 2) + (((lane & 7) ^ (lane >> 4)) * 16);
    const unsigned xvo1 = xrow * (DM * 2) + (((lane & 7) ^ (4 + (lane >> 4))) * 16);
    const unsigned wvo = (16 * w + (lane >> 3)) * (N3 * 2) + 896 + ch * 128 + (((lane & 7) ^ (((lane >> 4) & 1) << 2)) * 16);
    asm volatile("" ::"s"(x), "s"(wqkv), "s"(wo), "s"(out), "s"(ws), "v"(xvo0), "v"(xvo1), "v"(wvo), "s"(kbi[0]), "s"(kbi[1]), "s"(kbi[2]), "s"(kbi[3]),
                 "s"(kbi[4]), "s"(kbi[5]), "s"(kbi[6]));
    GAS char* wsg = (GAS char*)ws;
    const __amdgpu_buffer_rsrc_t rsOI = mkrs(ws + WS_O, 2u << 20);

    if (__builtin_expect(wv >= 4, 1)) {
        const __amdgpu_buffer_rsrc_t rsW = mkrs(wqkv, DM * N3 * 2), rsX = mkrs(x, SEQ * DM * 2), rsF = mkrs(ws + WS_F, 4096);
#define STAGE(i) stage_dma(lds0 + L_RING + ((i) % 5) * STG + 16384 + w * 2048, lds0 + L_RING + ((i) % 5) * STG + w * 4096, kbi[i] * (64 * N3 * 2), kbi[i] * 128, \
                           wvo, xvo0, xvo1, rsW, rsX)
#define VBAR(NV) asm volatile("s_waitcnt vmcnt(" #NV ")\n\ts_barrier" ::: "memory")
        STAMP(14);
        STAGE(0); STAGE(1); STAGE(2);
        STAMP(1);
        VBAR(12); STAGE(3);
        STAMP(2);
        VBAR(12); STAGE(4);
        VBAR(12); STAGE(5);
        VBAR(12); STAGE(6);
        VBAR(12);
        unsigned gen;
        { const unsigned vo = F_G; asm volatile("buffer_load_dword %0, %1, %2, 0 offen sc1" : "=v"(gen) : "v"(vo), "s"(rsF) : "memory"); }
        {
            const unsigned qvo = (16 * w + (lane >> 2)) * (N3 * 2) + g0 * 16 + (lane & 3) * 16;
#pragma unroll
            for (int j = 0; j < 7; ++j) dma16(lds0 + L_WQ + (16 * w + 64 * kbi[j]) * 64, qvo, rsW, kbi[j] * 64 * (N3 * 2));
        }
        VBAR(14); VBAR(8);
        STAMP(3);
        asm volatile("s_waitcnt vmcnt(7) lgkmcnt(0)\n\ts_barrier" : "+v"(gen)::"memory");
        unsigned tag = rfl(gen);
        { const unsigned t = (tag ^ (tag >> 15)) * 0x2c1b3c6du + 0x9e3779b9u; tag = (t == tag) ? t + 1 : t; }
        if (w == 0) {
            const u32x4 gb = *(LAS const u32x4*)(lds + L_GB);
            const unsigned g = max(max(gb[0], gb[1]), max(gb[2], gb[3]));
            const unsigned hh = lane & 15, wh = (lane >> 4) & 1;
            const unsigned c = isK ? (((7 * hh >> 1) + 3 * wh) >> 3) : 7 + ((28 * hh + 27 * wh) >> 6);
            const unsigned fo = lane < 32 ? F_KV + ((hh * 4 + (isK ? 0 : 2) + wh) * 16 + qt) * 128 : F_R + (qt * 16 + hd) * 8;
            if (lane == 0) *(LAS unsigned*)(lds + L_GB + 20) = tag;
            if ((lane < 32 && c == hd) || lane == 32) __hip_atomic_store((GAS u64*)(wsg + WS_F + fo), ((u64)g << 32) | tag, RLX_AGENT);
            asm volatile("s_waitcnt vmcnt(1)" ::: "memory");
        } else {
            asm volatile("s_waitcnt vmcnt(0)" ::: "memory");
        }
        STAMP(4);
        asm volatile("s_waitcnt lgkmcnt(0)\n\ts_barrier" ::: "memory");
        LAS unsigned* ord = (LAS unsigned*)(lds + L_CB + w * 12288 + (w == 2 ? 10240 : 0));
        if (lane < 5) ord[lane] = lane == 4 ? 0x40000000u : 0xffffffffu;
#pragma nounroll
        for (unsigned j = 0; j < 4; ++j) {
            const unsigned tt = w + 4 * ((j + (qt >> 2)) & 3);
            const unsigned g = poll_tags((const GAS u64*)(wsg + WS_F + F_KV + hd * 8192 + ((lane & 3) * 16 + tt) * 128), tag);
            if (lane == 0) ord[j] = tt * (4 * TS);
            if (j == 0) {
                const unsigned gm = wave_max_u(g);
                if (tt == qt && lane == 0) *(LAS unsigned*)(lds + L_GB + 16) = gm;
                STAMP(5);
                BAR();
                STAMP(6);
                asm volatile("s_barrier" ::: "memory");
            }
        }
        {
            const __amdgpu_buffer_rsrc_t rsO = mkrs(wo, DM * DM * 2);
            const unsigned vow = (4 * w + (lane >> 4)) * (DM * 2) + cg * 256 + ((((lane >> 2) & 3) ^ (lane >> 4)) * 64) + (lane & 3) * 16;
            __builtin_amdgcn_s_sleep(8);
#pragma unroll 1
            for (unsigned j = 0; j < 28; ++j) { dma16(lds0 + L_WO + (4 * j + w) * 1024, vow, rsO, j * 14336); __builtin_amdgcn_s_sleep(2); }
        }
        st16_wt((u32x4){POISON, POISON, POISON, POISON}, rsOI, lane * 16, ((qt * 4 + w) * NH + hd) * 2048);
        st16_wt((u32x4){POISON, POISON, POISON, POISON}, rsOI, lane * 16 + 1024, ((qt * 4 + w) * NH + hd) * 2048);
        if (lane < 4) __hip_atomic_store((GAS u64*)(wsg + WS_F + F_O + (qt * 16 + lane * 4 + w) * 2048 + hd * 128), (u64)(tag ^ 1), RLX_AGENT);
        if (w == 1 && b == 0) {
#pragma unroll 1
            for (int j = 0; j < 4; ++j) (void)poll_tags((const GAS u64*)(wsg + WS_F + F_R) + j * 64 + lane, tag);
            if (lane == 0) __hip_atomic_store((GAS unsigned*)(wsg + WS_F + F_G), tag, RLX_AGENT);
        }
        VBAR(0);
        STAMP(8);
        asm volatile("s_barrier" ::: "memory");
        STAMP(9);
        {
            u32x4 t[8];
            (void)poll_tags((const GAS u64*)(wsg + WS_F + F_O + (qt * 16 + hd) * 2048 + (w * 4 + (lane & 3)) * 128), tag);
            STAMP(7);
#pragma nounroll
            for (unsigned spins = 0; spins < (1u << 16); ++spins) {
                unsigned m = 0;
                asm volatile("" ::: "memory");
#pragma unroll
                for (int f = 0; f < 8; ++f) t[f] = ld16_wt(rsOI, lane * 16 + f * 1024, q32 * (NH * 2048) + w * 8192);
#pragma unroll
                for (int f = 0; f < 8; ++f) m = max(m, max(max(t[f][0], t[f][1]), max(t[f][2], t[f][3])));
                if (__all(m != POISON)) break;
            }
            STAMP(10);
#pragma unroll
            for (int f = 0; f < 8; ++f) *(LAS u32x4*)(lds + L_CB + (w * 8 + f) * 1024 + lane * 16) = t[f];
        }
        BAR();
        STAMP(11);
        STAMP_FLUSH();
        return;
    }

    f32x16 a0 = {}, a1 = {};
    bf16x8 xf[7][4];
    const unsigned kt = qt * 4 + w;
    unsigned gmx = 0;
    unsigned tch0 = 0, tch1 = 0;
    tch1 = touch((const GAS char*)(pc0 + ((idx & 3) * 4 + w) * 1024 + (lane & 7) * 128));
    if (isK) {
        kv_gemm<true>(lds, w, lane, xf, a0, a1);
        STAMP(3);
        if (hd < 14) {
            const __amdgpu_buffer_rsrc_t rs = mkrs(ws + WS_K, 64 * TS);
            const unsigned vo = (8 * ch + h) * 512 + r * 16, so = kt * TS;
            st16_wt(pack8(a0, 0), rs, vo, so); st16_wt(pack8(a0, 1), rs, vo + 1024, so);
            st16_wt(pack8(a1, 0), rs, vo + 2048, so); st16_wt(pack8(a1, 1), rs, vo + 3072, so);
            float s0 = 0.f, s1 = 0.f, s2 = 0.f, s3 = 0.f;
#pragma unroll
            for (int j = 0; j < 8; ++j) { s0 += a0[j] * a0[j]; s1 += a0[8 + j] * a0[8 + j]; s2 += a1[j] * a1[j]; s3 += a1[8 + j] * a1[8 + j]; }
            gmx = max(max(__float_as_uint(s0), __float_as_uint(s1)), max(__float_as_uint(s2), __float_as_uint(s3)));
            gmx = wave_max_u(gmx);
        }
    } else {
        kv_gemm<false>(lds, w, lane, xf, a0, a1);
        STAMP(3);
        const __amdgpu_buffer_rsrc_t rs = mkrs(ws + WS_V, 64 * TS);
        const unsigned n0 = 64 * (ch - 7) + r, n1 = n0 + 32, so = kt * TS;
        const unsigned v0 = h * VROW + (n0 + (n0 * 2341 >> 16)) * 16, v1 = h * VROW + (n1 + (n1 * 2341 >> 16)) * 16;
        st16_wt(pack8(a0, 0), rs, v0, so); st16_wt(pack8(a0, 1), rs, v0 + 2 * VROW, so);
        st16_wt(pack8(a1, 0), rs, v1, so); st16_wt(pack8(a1, 1), rs, v1 + 2 * VROW, so);
        const unsigned hc = (64 * (ch - 7) * 2341 >> 16) + (lane >> 2), last = 28 * hc + 27;
        if (lane < 16 && (last >> 6) == ch - 7)
            st16_wt((u32x4){0x3f803f80u, 0x3f803f80u, 0x3f803f80u, 0x3f803f80u}, rs, (lane & 3) * VROW + (29 * hc + 28) * 16, so);
    }
    if (lane == 0) *(LAS unsigned*)(lds + L_GB + w * 4) = gmx;
    STAMP(1);
    asm volatile("s_waitcnt vmcnt(0) lgkmcnt(0)" : "+v"(tch0), "+v"(tch1)::"memory");
    STAMP(2);
    asm volatile("s_barrier" ::: "memory");
    STAMP(4);
    asm volatile("s_barrier" ::: "memory");
    const unsigned tag = rfl(*(LAS const unsigned*)(lds + L_GB + 20));
    f32x16 aq = {};
    {
        LAS const char* qb = lds + L_WQ + (8 * h + q4) * 64 + g16 * 32 + (((p4 & 1) << 1) | (p4 >> 1)) * 8;
        bf16x8 wq[3][4];
#define RDQ(i) do { LAS const char* qk = qb + kbi[i] * 4096; _Pragma("unroll") for (int ks = 0; ks < 4; ++ks)                  \
            wq[(i) % 3][ks] = cat(vtr(qk + ks * 1024), vtr(qk + ks * 1024 + 256)); } while (0)
        RDQ(0); RDQ(1); SBAR();
#pragma unroll
        for (int i = 0; i < 7; ++i) {
            if (i + 2 < 7) RDQ(i + 2);
            SBAR();
#pragma unroll
            for (int ks = 0; ks < 4; ++ks) aq = MFMA(wq[i % 3][ks], xf[i][ks], aq);
            SBAR();
        }
#undef RDQ
    }
    float q2 = 0.f;
    {
        const bool zlo = (hd & 1) && !h, zhi = !(hd & 1) && h;
#pragma unroll
        for (int i = 0; i < 16; ++i) {
            aq[i] *= QSCALE;
            if (i < 4) aq[i] = zlo ? 0.f : aq[i];
            if (i >= 12) aq[i] = zhi ? 0.f : aq[i];
            q2 += aq[i] * aq[i];
        }
        q2 = half_lo(q2) + half_hi(q2);
    }
    u32x4 qo0 = pack8(aq, 0), qo1 = pack8(aq, 1);
    *(LAS u32x4*)(lds + L_QX + w * 2048 + lane * 16) = qo0;
    *(LAS u32x4*)(lds + L_QX + w * 2048 + 1024 + lane * 16) = qo1;
    *(LAS u32x4*)(lds + L_QX + 8192 + w * 2048 + lane * 16) = qo0;
    *(LAS u32x4*)(lds + L_QX + 8192 + w * 2048 + 1024 + lane * 16) = qo1;
    if (!h) *(LAS float*)(lds + L_Q2 + (r * 4 + w) * 4) = q2;
    const unsigned vok = WS_K + (g0 + h) * 512 + r * 16, vov = WS_V + h * VROW + (29 * hd + r) * 16;
    const __amdgpu_buffer_rsrc_t rsKV = mkrs(ws, 4u << 20);
    const unsigned orda = lds0 + L_CB + w * 12288 + (w == 2 ? 10240 : 0), qxa = lds0 + L_QX + w * 2048 + lane * 16;
    unsigned q2a = lds0 + L_Q2 + r * 16, kba = lds0 + L_GB + 16;
    STAMP(5);

    f32x16 o0, o1, o2, o3;
    {
        unsigned sb = (w + (qt & 12)) * (4 * TS), cnt = 4, ordn = orda + 4;
        asm volatile("s_waitcnt lgkmcnt(0)\n\ts_barrier\n\t"
        "buffer_load_dwordx4 v[160:163], v224, %[rs], %[sb] offen sc1\n\t"
        "buffer_load_dwordx4 v[164:167], v224, %[rs], %[sb] offen offset:1024 sc1\n\t"
        "buffer_load_dwordx4 v[168:171], v225, %[rs], %[sb] offen sc1\n\t"
        "buffer_load_dwordx4 v[172:175], v226, %[rs], %[sb] offen sc1\n\t"
        "buffer_load_dwordx4 v[176:179], v227, %[rs], %[sb] offen sc1\n\t"
        "buffer_load_dwordx4 v[180:183], v227, %[rs], %[sb] offen offset:1024 sc1\n\t"
        "buffer_load_dwordx4 v[184:187], v228, %[rs], %[sb] offen sc1\n\t"
        "buffer_load_dwordx4 v[188:191], v229, %[rs], %[sb] offen sc1\n\t"
        "buffer_load_dwordx4 v[192:195], v230, %[rs], %[sb] offen sc1\n\t"
        "buffer_load_dwordx4 v[196:199], v230, %[rs], %[sb] offen offset:1024 sc1\n\t"
        "buffer_load_dwordx4 v[200:203], v231, %[rs], %[sb] offen sc1\n\t"
        "buffer_load_dwordx4 v[204:207], v232, %[rs], %[sb] offen sc1\n\t"
        "ds_read_b128 v[112:115], v236 offset:0\n\t"
        "ds_read_b128 v[116:119], v236 offset:1024\n\t"
        "ds_read_b128 v[120:123], v236 offset:2048\n\t"
        "ds_read_b128 v[124:127], v236 offset:3072\n\t"
        "ds_read_b128 v[128:131], v236 offset:4096\n\t"
        "ds_read_b128 v[132:135], v236 offset:5120\n\t"
        "ds_read_b128 v[136:139], v236 offset:6144\n\t"
        "ds_read_b128 v[140:143], v236 offset:7168\n\t"
        "ds_read_b128 v[80:83], v237\n\t"
        "ds_read_b32 v238, v238\n\t"
        "v_mov_b32 v0, 0\n\t"
        "v_mov_b32 v1, 0\n\t"
        "v_mov_b32 v2, 0\n\t"
        "v_mov_b32 v3, 0\n\t"
        "v_mov_b32 v4, 0\n\t"
        "v_mov_b32 v5, 0\n\t"
        "v_mov_b32 v6, 0\n\t"
        "v_mov_b32 v7, 0\n\t"
        "v_mov_b32 v8, 0\n\t"
        "v_mov_b32 v9, 0\n\t"
        "v_mov_b32 v10, 0\n\t"
        "v_mov_b32 v11, 0\n\t"
        "v_mov_b32 v12, 0\n\t"
        "v_mov_b32 v13, 0\n\t"
        "v_mov_b32 v14, 0\n\t"
        "v_mov_b32 v15, 0\n\t"
        "v_mov_b32 v16, 0\n\t"
        "v_mov_b32 v17, 0\n\t"
        "v_mov_b32 v18, 0\n\t"
        "v_mov_b32 v19, 0\n\t"
        "v_mov_b32 v20, 0\n\t"
        "v_mov_b32 v21, 0\n\t"
        "v_mov_b32 v22, 0\n\t"
        "v_mov_b32 v23, 0\n\t"
        "v_mov_b32 v24, 0\n\t"
        "v_mov_b32 v25, 0\n\t"
        "v_mov_b32 v26, 0\n\t"
        "v_mov_b32 v27, 0\n\t"
        "v_mov_b32 v28, 0\n\t"
        "v_mov_b32 v29, 0\n\t"
        "v_mov_b32 v30, 0\n\t"
        "v_mov_b32 v31, 0\n\t"
        "v_mov_b32 v32, 0\n\t"
        "v_mov_b32 v33, 0\n\t"
        "v_mov_b32 v34, 0\n\t"
        "v_mov_b32 v35, 0\n\t"
        "v_mov_b32 v36, 0\n\t"
        "v_mov_b32 v37, 0\n\t"
        "v_mov_b32 v38, 0\n\t"
        "v_mov_b32 v39, 0\n\t"
        "v_mov_b32 v40, 0\n\t"
        "v_mov_b32 v41, 0\n\t"
        "v_mov_b32 v42, 0\n\t"
        "v_mov_b32 v43, 0\n\t"
        "v_mov_b32 v44, 0\n\t"
        "v_mov_b32 v45, 0\n\t"
        "v_mov_b32 v46, 0\n\t"
        "v_mov_b32 v47, 0\n\t"
        "v_mov_b32 v48, 0\n\t"
        "v_mov_b32 v49, 0\n\t"
        "v_mov_b32 v50, 0\n\t"
        "v_mov_b32 v51, 0\n\t"
        "v_mov_b32 v52, 0\n\t"
        "v_mov_b32 v53, 0\n\t"
        "v_mov_b32 v54, 0\n\t"
        "v_mov_b32 v55, 0\n\t"
        "v_mov_b32 v56, 0\n\t"
        "v_mov_b32 v57, 0\n\t"
        "v_mov_b32 v58, 0\n\t"
        "v_mov_b32 v59, 0\n\t"
        "v_mov_b32 v60, 0\n\t"
        "v_mov_b32 v61, 0\n\t"
        "v_mov_b32 v62, 0\n\t"
        "v_mov_b32 v63, 0\n\t"
        "v_mov_b32 v152, 0\n\t"
        "v_mov_b32 v153, 0\n\t"
        "v_mov_b32 v154, 0\n\t"
        "v_mov_b32 v155, 0\n\t"
        "v_mov_b32 v156, 0\n\t"
        "v_mov_b32 v157, 0\n\t"
        "v_mov_b32 v158, 0\n\t"
        "v_mov_b32 v159, 0\n\t"
        "v_mov_b32 v216, 0\n\t"
        "v_mov_b32 v217, 0\n\t"
        "v_mov_b32 v218, 0\n\t"
        "v_mov_b32 v219, 0\n\t"
        "v_mov_b32 v220, 0\n\t"
        "v_mov_b32 v221, 0\n\t"
        "v_mov_b32 v222, 0\n\t"
        "v_mov_b32 v223, 0\n\t"
        "s_waitcnt lgkmcnt(0)\n\t"
        "s_barrier\n\t"
        "v_max3_f32 v237, v80, v81, v82\n\t"
        "v_max_f32 v237, v237, v83\n\t"
        "v_mul_f32 v237, v237, v238\n\t"
        "v_sqrt_f32 v237, v237\n\t"
        "s_nop 0\n\t"
        "v_mul_f32 v96, 0xc0028f5c, v237\n\t"
        "v_mov_b32 v97, v96\n\t"
        "v_mov_b32 v98, v96\n\t"
        "v_mov_b32 v99, v96\n\t"
        "v_mov_b32 v100, v96\n\t"
        "v_mov_b32 v101, v96\n\t"
        "v_mov_b32 v102, v96\n\t"
        "v_mov_b32 v103, v96\n\t"
        "v_mov_b32 v104, v96\n\t"
        "v_mov_b32 v105, v96\n\t"
        "v_mov_b32 v106, v96\n\t"
        "v_mov_b32 v107, v96\n\t"
        "v_mov_b32 v108, v96\n\t"
        "v_mov_b32 v109, v96\n\t"
        "v_mov_b32 v110, v96\n\t"
        "v_mov_b32 v111, v96\n\t"
        "s_waitcnt vmcnt(8)\n\t"
        "v_mfma_f32_32x32x16_bf16 v[64:79], v[160:163], v[112:115], v[96:111]\n\t"
        "v_mfma_f32_32x32x16_bf16 v[64:79], v[164:167], v[116:119], v[64:79]\n\t"
        "s_nop 15\n\t"
        "s_nop 3\n\t"
        "LOOP%=:\n\t"
        "v_mfma_f32_32x32x16_bf16 v[80:95], v[160:163], v[120:123], v[96:111]\n\t"
        "v_exp_f32 v64, v64\n\t"
        "v_exp_f32 v65, v65\n\t"
        "v_exp_f32 v66, v66\n\t"
        "v_exp_f32 v67, v67\n\t"
        "v_mfma_f32_32x32x16_bf16 v[80:95], v[164:167], v[124:127], v[80:95]\n\t"
        "v_exp_f32 v68, v68\n\t"
        "v_exp_f32 v69, v69\n\t"
        "v_exp_f32 v70, v70\n\t"
        "v_exp_f32 v71, v71\n\t"
        "v_cvt_pk_bf16_f32 v144, v64, v65\n\t"
        "v_cvt_pk_bf16_f32 v145, v66, v67\n\t"
        "v_mfma_f32_32x32x16_bf16 v[48:63], v[216:219], v[152:155], v[48:63]\n\t"
        "v_exp_f32 v72, v72\n\t"
        "v_exp_f32 v73, v73\n\t"
        "v_exp_f32 v74, v74\n\t"
        "v_exp_f32 v75, v75\n\t"
        "v_cvt_pk_bf16_f32 v146, v68, v69\n\t"
        "v_cvt_pk_bf16_f32 v147, v70, v71\n\t"
        "v_mfma_f32_32x32x16_bf16 v[48:63], v[220:223], v[156:159], v[48:63]\n\t"
        "v_exp_f32 v76, v76\n\t"
        "v_exp_f32 v77, v77\n\t"
        "v_exp_f32 v78, v78\n\t"
        "v_exp_f32 v79, v79\n\t"
        "v_cvt_pk_bf16_f32 v148, v72, v73\n\t"
        "v_cvt_pk_bf16_f32 v149, v74, v75\n\t"
        "v_cvt_pk_bf16_f32 v150, v76, v77\n\t"
        "v_cvt_pk_bf16_f32 v151, v78, v79\n\t"
        "v_mfma_f32_32x32x16_bf16 v[64:79], v[160:163], v[128:131], v[96:111]\n\t"
        "buffer_load_dwordx4 v[208:211], v233, %[rs], %[sb] offen sc1\n\t"
        "buffer_load_dwordx4 v[212:215], v233, %[rs], %[sb] offen offset:1024 sc1\n\t"
        "buffer_load_dwordx4 v[216:219], v234, %[rs], %[sb] offen sc1\n\t"
        "buffer_load_dwordx4 v[220:223], v235, %[rs], %[sb] offen sc1\n\t"
        "v_exp_f32 v80, v80\n\t"
        "v_exp_f32 v81, v81\n\t"
        "v_exp_f32 v82, v82\n\t"
        "v_exp_f32 v83, v83\n\t"
        "v_mfma_f32_32x32x16_bf16 v[64:79], v[164:167], v[132:135], v[64:79]\n\t"
        "v_exp_f32 v84, v84\n\t"
        "v_exp_f32 v85, v85\n\t"
        "v_exp_f32 v86, v86\n\t"
        "v_exp_f32 v87, v87\n\t"
        "v_cvt_pk_bf16_f32 v152, v80, v81\n\t"
        "v_cvt_pk_bf16_f32 v153, v82, v83\n\t"
        "v_mfma_f32_32x32x16_bf16 v[0:15], v[168:171], v[144:147], v[0:15]\n\t"
        "v_exp_f32 v88, v88\n\t"
        "v_exp_f32 v89, v89\n\t"
        "v_exp_f32 v90, v90\n\t"
        "v_exp_f32 v91, v91\n\t"
        "v_cvt_pk_bf16_f32 v154, v84, v85\n\t"
        "v_cvt_pk_bf16_f32 v155, v86, v87\n\t"
        "v_mfma_f32_32x32x16_bf16 v[0:15], v[172:175], v[148:151], v[0:15]\n\t"
        "v_exp_f32 v92, v92\n\t"
        "v_exp_f32 v93, v93\n\t"
        "v_exp_f32 v94, v94\n\t"
        "v_exp_f32 v95, v95\n\t"
        "v_cvt_pk_bf16_f32 v156, v88, v89\n\t"
        "v_cvt_pk_bf16_f32 v157, v90, v91\n\t"
        "v_cvt_pk_bf16_f32 v158, v92, v93\n\t"
        "v_cvt_pk_bf16_f32 v159, v94, v95\n\t"
        "v_mfma_f32_32x32x16_bf16 v[80:95], v[160:163], v[136:139], v[96:111]\n\t"
        "ds_read_b32 v240, v239\n\t"
        "v_exp_f32 v64, v64\n\t"
        "v_exp_f32 v65, v65\n\t"
        "v_exp_f32 v66, v66\n\t"
        "v_exp_f32 v67, v67\n\t"
        "v_mfma_f32_32x32x16_bf16 v[80:95], v[164:167], v[140:143], v[80:95]\n\t"
        "v_exp_f32 v68, v68\n\t"
        "v_exp_f32 v69, v69\n\t"
        "v_exp_f32 v70, v70\n\t"
        "v_exp_f32 v71, v71\n\t"
        "v_cvt_pk_bf16_f32 v144, v64, v65\n\t"
        "v_cvt_pk_bf16_f32 v145, v66, v67\n\t"
        "v_mfma_f32_32x32x16_bf16 v[16:31], v[168:171], v[152:155], v[16:31]\n\t"
        "v_exp_f32 v72, v72\n\t"
        "v_exp_f32 v73, v73\n\t"
        "v_exp_f32 v74, v74\n\t"
        "v_exp_f32 v75, v75\n\t"
        "v_cvt_pk_bf16_f32 v146, v68, v69\n\t"
        "v_cvt_pk_bf16_f32 v147, v70, v71\n\t"
        "v_mfma_f32_32x32x16_bf16 v[16:31], v[172:175], v[156:159], v[16:31]\n\t"
        "v_exp_f32 v76, v76\n\t"
        "v_exp_f32 v77, v77\n\t"
        "v_exp_f32 v78, v78\n\t"
        "v_exp_f32 v79, v79\n\t"
        "v_cvt_pk_bf16_f32 v148, v72, v73\n\t"
        "v_cvt_pk_bf16_f32 v149, v74, v75\n\t"
        "v_cvt_pk_bf16_f32 v150, v76, v77\n\t"
        "v_cvt_pk_bf16_f32 v151, v78, v79\n\t"
        "s_waitcnt vmcnt(8)\n\t"
        "v_mfma_f32_32x32x16_bf16 v[64:79], v[176:179], v[112:115], v[96:111]\n\t"
        "v_exp_f32 v80, v80\n\t"
        "v_exp_f32 v81, v81\n\t"
        "v_exp_f32 v82, v82\n\t"
        "v_exp_f32 v83, v83\n\t"
        "v_mfma_f32_32x32x16_bf16 v[64:79], v[180:183], v[116:119], v[64:79]\n\t"
        "v_exp_f32 v84, v84\n\t"
        "v_exp_f32 v85, v85\n\t"
        "v_exp_f32 v86, v86\n\t"
        "v_exp_f32 v87, v87\n\t"
        "v_cvt_pk_bf16_f32 v152, v80, v81\n\t"
        "v_cvt_pk_bf16_f32 v153, v82, v83\n\t"
        "v_mfma_f32_32x32x16_bf16 v[32:47], v[168:171], v[144:147], v[32:47]\n\t"
        "v_exp_f32 v88, v88\n\t"
        "v_exp_f32 v89, v89\n\t"
        "v_exp_f32 v90, v90\n\t"
        "v_exp_f32 v91, v91\n\t"
        "v_cvt_pk_bf16_f32 v154, v84, v85\n\t"
        "v_cvt_pk_bf16_f32 v155, v86, v87\n\t"
        "v_mfma_f32_32x32x16_bf16 v[32:47], v[172:175], v[148:151], v[32:47]\n\t"
        "v_exp_f32 v92, v92\n\t"
        "v_exp_f32 v93, v93\n\t"
        "v_exp_f32 v94, v94\n\t"
        "v_exp_f32 v95, v95\n\t"
        "v_cvt_pk_bf16_f32 v156, v88, v89\n\t"
        "v_cvt_pk_bf16_f32 v157, v90, v91\n\t"
        "v_cvt_pk_bf16_f32 v158, v92, v93\n\t"
        "v_cvt_pk_bf16_f32 v159, v94, v95\n\t"
        "v_mfma_f32_32x32x16_bf16 v[80:95], v[176:179], v[120:123], v[96:111]\n\t"
        "CHK%=:\n\t"
        "s_waitcnt lgkmcnt(0)\n\t"
        "v_readfirstlane_b32 %[sb], v240\n\t"
        "ds_read_b32 v240, v239\n\t"
        "s_cmp_eq_u32 %[sb], -1\n\t"
        "s_cbranch_scc1 CHK%=\n\t"
        "v_add_u32 v239, 4, v239\n\t"
        "v_exp_f32 v64, v64\n\t"
        "v_exp_f32 v65, v65\n\t"
        "v_exp_f32 v66, v66\n\t"
        "v_exp_f32 v67, v67\n\t"
        "v_mfma_f32_32x32x16_bf16 v[80:95], v[180:183], v[124:127], v[80:95]\n\t"
        "v_exp_f32 v68, v68\n\t"
        "v_exp_f32 v69, v69\n\t"
        "v_exp_f32 v70, v70\n\t"
        "v_exp_f32 v71, v71\n\t"
        "v_cvt_pk_bf16_f32 v144, v64, v65\n\t"
        "v_cvt_pk_bf16_f32 v145, v66, v67\n\t"
        "v_mfma_f32_32x32x16_bf16 v[48:63], v[168:171], v[152:155], v[48:63]\n\t"
        "v_exp_f32 v72, v72\n\t"
        "v_exp_f32 v73, v73\n\t"
        "v_exp_f32 v74, v74\n\t"
        "v_exp_f32 v75, v75\n\t"
        "v_cvt_pk_bf16_f32 v146, v68, v69\n\t"
        "v_cvt_pk_bf16_f32 v147, v70, v71\n\t"
        "v_mfma_f32_32x32x16_bf16 v[48:63], v[172:175], v[156:159], v[48:63]\n\t"
        "v_exp_f32 v76, v76\n\t"
        "v_exp_f32 v77, v77\n\t"
        "v_exp_f32 v78, v78\n\t"
        "v_exp_f32 v79, v79\n\t"
        "v_cvt_pk_bf16_f32 v148, v72, v73\n\t"
        "v_cvt_pk_bf16_f32 v149, v74, v75\n\t"
        "v_cvt_pk_bf16_f32 v150, v76, v77\n\t"
        "v_cvt_pk_bf16_f32 v151, v78, v79\n\t"
        "v_mfma_f32_32x32x16_bf16 v[64:79], v[176:179], v[128:131], v[96:111]\n\t"
        "buffer_load_dwordx4 v[160:163], v224, %[rs], %[sb] offen sc1\n\t"
        "buffer_load_dwordx4 v[164:167], v224, %[rs], %[sb] offen offset:1024 sc1\n\t"
        "buffer_load_dwordx4 v[168:171], v225, %[rs], %[sb] offen sc1\n\t"
        "buffer_load_dwordx4 v[172:175], v226, %[rs], %[sb] offen sc1\n\t"
        "v_exp_f32 v80, v80\n\t"
        "v_exp_f32 v81, v81\n\t"
        "v_exp_f32 v82, v82\n\t"
        "v_exp_f32 v83, v83\n\t"
        "v_mfma_f32_32x32x16_bf16 v[64:79], v[180:183], v[132:135], v[64:79]\n\t"
        "v_exp_f32 v84, v84\n\t"
        "v_exp_f32 v85, v85\n\t"
        "v_exp_f32 v86, v86\n\t"
        "v_exp_f32 v87, v87\n\t"
        "v_cvt_pk_bf16_f32 v152, v80, v81\n\t"
        "v_cvt_pk_bf16_f32 v153, v82, v83\n\t"
        "v_mfma_f32_32x32x16_bf16 v[0:15], v[184:187], v[144:147], v[0:15]\n\t"
        "v_exp_f32 v88, v88\n\t"
        "v_exp_f32 v89, v89\n\t"
        "v_exp_f32 v90, v90\n\t"
        "v_exp_f32 v91, v91\n\t"
        "v_cvt_pk_bf16_f32 v154, v84, v85\n\t"
        "v_cvt_pk_bf16_f32 v155, v86, v87\n\t"
        "v_mfma_f32_32x32x16_bf16 v[0:15], v[188:191], v[148:151], v[0:15]\n\t"
        "v_exp_f32 v92, v92\n\t"
        "v_exp_f32 v93, v93\n\t"
        "v_exp_f32 v94, v94\n\t"
        "v_exp_f32 v95, v95\n\t"
        "v_cvt_pk_bf16_f32 v156, v88, v89\n\t"
        "v_cvt_pk_bf16_f32 v157, v90, v91\n\t"
        "v_cvt_pk_bf16_f32 v158, v92, v93\n\t"
        "v_cvt_pk_bf16_f32 v159, v94, v95\n\t"
        "v_mfma_f32_32x32x16_bf16 v[80:95], v[176:179], v[136:139], v[96:111]\n\t"
        "v_exp_f32 v64, v64\n\t"
        "v_exp_f32 v65, v65\n\t"
        "v_exp_f32 v66, v66\n\t"
        "v_exp_f32 v67, v67\n\t"
        "v_mfma_f32_32x32x16_bf16 v[80:95], v[180:183], v[140:143], v[80:95]\n\t"
        "v_exp_f32 v68, v68\n\t"
        "v_exp_f32 v69, v69\n\t"
        "v_exp_f32 v70, v70\n\t"
        "v_exp_f32 v71, v71\n\t"
        "v_cvt_pk_bf16_f32 v144, v64, v65\n\t"
        "v_cvt_pk_bf16_f32 v145, v66, v67\n\t"
        "v_mfma_f32_32x32x16_bf16 v[16:31], v[184:187], v[152:155], v[16:31]\n\t"
        "v_exp_f32 v72, v72\n\t"
        "v_exp_f32 v73, v73\n\t"
        "v_exp_f32 v74, v74\n\t"
        "v_exp_f32 v75, v75\n\t"
        "v_cvt_pk_bf16_f32 v146, v68, v69\n\t"
        "v_cvt_pk_bf16_f32 v147, v70, v71\n\t"
        "v_mfma_f32_32x32x16_bf16 v[16:31], v[188:191], v[156:159], v[16:31]\n\t"
        "v_exp_f32 v76, v76\n\t"
        "v_exp_f32 v77, v77\n\t"
        "v_exp_f32 v78, v78\n\t"
        "v_exp_f32 v79, v79\n\t"
        "v_cvt_pk_bf16_f32 v148, v72, v73\n\t"
        "v_cvt_pk_bf16_f32 v149, v74, v75\n\t"
        "v_cvt_pk_bf16_f32 v150, v76, v77\n\t"
        "v_cvt_pk_bf16_f32 v151, v78, v79\n\t"
        "s_waitcnt vmcnt(8)\n\t"
        "v_mfma_f32_32x32x16_bf16 v[64:79], v[192:195], v[112:115], v[96:111]\n\t"
        "v_exp_f32 v80, v80\n\t"
        "v_exp_f32 v81, v81\n\t"
        "v_exp_f32 v82, v82\n\t"
        "v_exp_f32 v83, v83\n\t"
        "v_mfma_f32_32x32x16_bf16 v[64:79], v[196:199], v[116:119], v[64:79]\n\t"
        "v_exp_f32 v84, v84\n\t"
        "v_exp_f32 v85, v85\n\t"
        "v_exp_f32 v86, v86\n\t"
        "v_exp_f32 v87, v87\n\t"
        "v_cvt_pk_bf16_f32 v152, v80, v81\n\t"
        "v_cvt_pk_bf16_f32 v153, v82, v83\n\t"
        "v_mfma_f32_32x32x16_bf16 v[32:47], v[184:187], v[144:147], v[32:47]\n\t"
        "v_exp_f32 v88, v88\n\t"
        "v_exp_f32 v89, v89\n\t"
        "v_exp_f32 v90, v90\n\t"
        "v_exp_f32 v91, v91\n\t"
        "v_cvt_pk_bf16_f32 v154, v84, v85\n\t"
        "v_cvt_pk_bf16_f32 v155, v86, v87\n\t"
        "v_mfma_f32_32x32x16_bf16 v[32:47], v[188:191], v[148:151], v[32:47]\n\t"
        "v_exp_f32 v92, v92\n\t"
        "v_exp_f32 v93, v93\n\t"
        "v_exp_f32 v94, v94\n\t"
        "v_exp_f32 v95, v95\n\t"
        "v_cvt_pk_bf16_f32 v156, v88, v89\n\t"
        "v_cvt_pk_bf16_f32 v157, v90, v91\n\t"
        "v_cvt_pk_bf16_f32 v158, v92, v93\n\t"
        "v_cvt_pk_bf16_f32 v159, v94, v95\n\t"
        "v_mfma_f32_32x32x16_bf16 v[80:95], v[192:195], v[120:123], v[96:111]\n\t"
        "v_exp_f32 v64, v64\n\t"
        "v_exp_f32 v65, v65\n\t"
        "v_exp_f32 v66, v66\n\t"
        "v_exp_f32 v67, v67\n\t"
        "v_mfma_f32_32x32x16_bf16 v[80:95], v[196:199], v[124:127], v[80:95]\n\t"
        "v_exp_f32 v68, v68\n\t"
        "v_exp_f32 v69, v69\n\t"
        "v_exp_f32 v70, v70\n\t"
        "v_exp_f32 v71, v71\n\t"
        "v_cvt_pk_bf16_f32 v144, v64, v65\n\t"
        "v_cvt_pk_bf16_f32 v145, v66, v67\n\t"
        "v_mfma_f32_32x32x16_bf16 v[48:63], v[184:187], v[152:155], v[48:63]\n\t"
        "v_exp_f32 v72, v72\n\t"
        "v_exp_f32 v73, v73\n\t"
        "v_exp_f32 v74, v74\n\t"
        "v_exp_f32 v75, v75\n\t"
        "v_cvt_pk_bf16_f32 v146, v68, v69\n\t"
        "v_cvt_pk_bf16_f32 v147, v70, v71\n\t"
        "v_mfma_f32_32x32x16_bf16 v[48:63], v[188:191], v[156:159], v[48:63]\n\t"
        "v_exp_f32 v76, v76\n\t"
        "v_exp_f32 v77, v77\n\t"
        "v_exp_f32 v78, v78\n\t"
        "v_exp_f32 v79, v79\n\t"
        "v_cvt_pk_bf16_f32 v148, v72, v73\n\t"
        "v_cvt_pk_bf16_f32 v149, v74, v75\n\t"
        "v_cvt_pk_bf16_f32 v150, v76, v77\n\t"
        "v_cvt_pk_bf16_f32 v151, v78, v79\n\t"
        "v_mfma_f32_32x32x16_bf16 v[64:79], v[192:195], v[128:131], v[96:111]\n\t"
        "buffer_load_dwordx4 v[176:179], v227, %[rs], %[sb] offen sc1\n\t"
        "buffer_load_dwordx4 v[180:183], v227, %[rs], %[sb] offen offset:1024 sc1\n\t"
        "buffer_load_dwordx4 v[184:187], v228, %[rs], %[sb] offen sc1\n\t"
        "buffer_load_dwordx4 v[188:191], v229, %[rs], %[sb] offen sc1\n\t"
        "v_exp_f32 v80, v80\n\t"
        "v_exp_f32 v81, v81\n\t"
        "v_exp_f32 v82, v82\n\t"
        "v_exp_f32 v83, v83\n\t"
        "v_mfma_f32_32x32x16_bf16 v[64:79], v[196:199], v[132:135], v[64:79]\n\t"
        "v_exp_f32 v84, v84\n\t"
        "v_exp_f32 v85, v85\n\t"
        "v_exp_f32 v86, v86\n\t"
        "v_exp_f32 v87, v87\n\t"
        "v_cvt_pk_bf16_f32 v152, v80, v81\n\t"
        "v_cvt_pk_bf16_f32 v153, v82, v83\n\t"
        "v_mfma_f32_32x32x16_bf16 v[0:15], v[200:203], v[144:147], v[0:15]\n\t"
        "v_exp_f32 v88, v88\n\t"
        "v_exp_f32 v89, v89\n\t"
        "v_exp_f32 v90, v90\n\t"
        "v_exp_f32 v91, v91\n\t"
        "v_cvt_pk_bf16_f32 v154, v84, v85\n\t"
        "v_cvt_pk_bf16_f32 v155, v86, v87\n\t"
        "v_mfma_f32_32x32x16_bf16 v[0:15], v[204:207], v[148:151], v[0:15]\n\t"
        "v_exp_f32 v92, v92\n\t"
        "v_exp_f32 v93, v93\n\t"
        "v_exp_f32 v94, v94\n\t"
        "v_exp_f32 v95, v95\n\t"
        "v_cvt_pk_bf16_f32 v156, v88, v89\n\t"
        "v_cvt_pk_bf16_f32 v157, v90, v91\n\t"
        "v_cvt_pk_bf16_f32 v158, v92, v93\n\t"
        "v_cvt_pk_bf16_f32 v159, v94, v95\n\t"
        "v_mfma_f32_32x32x16_bf16 v[80:95], v[192:195], v[136:139], v[96:111]\n\t"
        "v_exp_f32 v64, v64\n\t"
        "v_exp_f32 v65, v65\n\t"
        "v_exp_f32 v66, v66\n\t"
        "v_exp_f32 v67, v67\n\t"
        "v_mfma_f32_32x32x16_bf16 v[80:95], v[196:199], v[140:143], v[80:95]\n\t"
        "v_exp_f32 v68, v68\n\t"
        "v_exp_f32 v69, v69\n\t"
        "v_exp_f32 v70, v70\n\t"
        "v_exp_f32 v71, v71\n\t"
        "v_cvt_pk_bf16_f32 v144, v64, v65\n\t"
        "v_cvt_pk_bf16_f32 v145, v66, v67\n\t"
        "v_mfma_f32_32x32x16_bf16 v[16:31], v[200:203], v[152:155], v[16:31]\n\t"
        "v_exp_f32 v72, v72\n\t"
        "v_exp_f32 v73, v73\n\t"
        "v_exp_f32 v74, v74\n\t"
        "v_exp_f32 v75, v75\n\t"
        "v_cvt_pk_bf16_f32 v146, v68, v69\n\t"
        "v_cvt_pk_bf16_f32 v147, v70, v71\n\t"
        "v_mfma_f32_32x32x16_bf16 v[16:31], v[204:207], v[156:159], v[16:31]\n\t"
        "v_exp_f32 v76, v76\n\t"
        "v_exp_f32 v77, v77\n\t"
        "v_exp_f32 v78, v78\n\t"
        "v_exp_f32 v79, v79\n\t"
        "v_cvt_pk_bf16_f32 v148, v72, v73\n\t"
        "v_cvt_pk_bf16_f32 v149, v74, v75\n\t"
        "v_cvt_pk_bf16_f32 v150, v76, v77\n\t"
        "v_cvt_pk_bf16_f32 v151, v78, v79\n\t"
        "s_waitcnt vmcnt(8)\n\t"
        "v_mfma_f32_32x32x16_bf16 v[64:79], v[208:211], v[112:115], v[96:111]\n\t"
        "v_exp_f32 v80, v80\n\t"
        "v_exp_f32 v81, v81\n\t"
        "v_exp_f32 v82, v82\n\t"
        "v_exp_f32 v83, v83\n\t"
        "v_mfma_f32_32x32x16_bf16 v[64:79], v[212:215], v[116:119], v[64:79]\n\t"
        "v_exp_f32 v84, v84\n\t"
        "v_exp_f32 v85, v85\n\t"
        "v_exp_f32 v86, v86\n\t"
        "v_exp_f32 v87, v87\n\t"
        "v_cvt_pk_bf16_f32 v152, v80, v81\n\t"
        "v_cvt_pk_bf16_f32 v153, v82, v83\n\t"
        "v_mfma_f32_32x32x16_bf16 v[32:47], v[200:203], v[144:147], v[32:47]\n\t"
        "v_exp_f32 v88, v88\n\t"
        "v_exp_f32 v89, v89\n\t"
        "v_exp_f32 v90, v90\n\t"
        "v_exp_f32 v91, v91\n\t"
        "v_cvt_pk_bf16_f32 v154, v84, v85\n\t"
        "v_cvt_pk_bf16_f32 v155, v86, v87\n\t"
        "v_mfma_f32_32x32x16_bf16 v[32:47], v[204:207], v[148:151], v[32:47]\n\t"
        "v_exp_f32 v92, v92\n\t"
        "v_exp_f32 v93, v93\n\t"
        "v_exp_f32 v94, v94\n\t"
        "v_exp_f32 v95, v95\n\t"
        "v_cvt_pk_bf16_f32 v156, v88, v89\n\t"
        "v_cvt_pk_bf16_f32 v157, v90, v91\n\t"
        "v_cvt_pk_bf16_f32 v158, v92, v93\n\t"
        "v_cvt_pk_bf16_f32 v159, v94, v95\n\t"
        "v_mfma_f32_32x32x16_bf16 v[80:95], v[208:211], v[120:123], v[96:111]\n\t"
        "v_exp_f32 v64, v64\n\t"
        "v_exp_f32 v65, v65\n\t"
        "v_exp_f32 v66, v66\n\t"
        "v_exp_f32 v67, v67\n\t"
        "v_mfma_f32_32x32x16_bf16 v[80:95], v[212:215], v[124:127], v[80:95]\n\t"
        "v_exp_f32 v68, v68\n\t"
        "v_exp_f32 v69, v69\n\t"
        "v_exp_f32 v70, v70\n\t"
        "v_exp_f32 v71, v71\n\t"
        "v_cvt_pk_bf16_f32 v144, v64, v65\n\t"
        "v_cvt_pk_bf16_f32 v145, v66, v67\n\t"
        "v_mfma_f32_32x32x16_bf16 v[48:63], v[200:203], v[152:155], v[48:63]\n\t"
        "v_exp_f32 v72, v72\n\t"
        "v_exp_f32 v73, v73\n\t"
        "v_exp_f32 v74, v74\n\t"
        "v_exp_f32 v75, v75\n\t"
        "v_cvt_pk_bf16_f32 v146, v68, v69\n\t"
        "v_cvt_pk_bf16_f32 v147, v70, v71\n\t"
        "v_mfma_f32_32x32x16_bf16 v[48:63], v[204:207], v[156:159], v[48:63]\n\t"
        "v_exp_f32 v76, v76\n\t"
        "v_exp_f32 v77, v77\n\t"
        "v_exp_f32 v78, v78\n\t"
        "v_exp_f32 v79, v79\n\t"
        "v_cvt_pk_bf16_f32 v148, v72, v73\n\t"
        "v_cvt_pk_bf16_f32 v149, v74, v75\n\t"
        "v_cvt_pk_bf16_f32 v150, v76, v77\n\t"
        "v_cvt_pk_bf16_f32 v151, v78, v79\n\t"
        "v_mfma_f32_32x32x16_bf16 v[64:79], v[208:211], v[128:131], v[96:111]\n\t"
        "buffer_load_dwordx4 v[192:195], v230, %[rs], %[sb] offen sc1\n\t"
        "buffer_load_dwordx4 v[196:199], v230, %[rs], %[sb] offen offset:1024 sc1\n\t"
        "buffer_load_dwordx4 v[200:203], v231, %[rs], %[sb] offen sc1\n\t"
        "buffer_load_dwordx4 v[204:207], v232, %[rs], %[sb] offen sc1\n\t"
        "v_exp_f32 v80, v80\n\t"
        "v_exp_f32 v81, v81\n\t"
        "v_exp_f32 v82, v82\n\t"
        "v_exp_f32 v83, v83\n\t"
        "v_mfma_f32_32x32x16_bf16 v[64:79], v[212:215], v[132:135], v[64:79]\n\t"
        "v_exp_f32 v84, v84\n\t"
        "v_exp_f32 v85, v85\n\t"
        "v_exp_f32 v86, v86\n\t"
        "v_exp_f32 v87, v87\n\t"
        "v_cvt_pk_bf16_f32 v152, v80, v81\n\t"
        "v_cvt_pk_bf16_f32 v153, v82, v83\n\t"
        "v_mfma_f32_32x32x16_bf16 v[0:15], v[216:219], v[144:147], v[0:15]\n\t"
        "v_exp_f32 v88, v88\n\t"
        "v_exp_f32 v89, v89\n\t"
        "v_exp_f32 v90, v90\n\t"
        "v_exp_f32 v91, v91\n\t"
        "v_cvt_pk_bf16_f32 v154, v84, v85\n\t"
        "v_cvt_pk_bf16_f32 v155, v86, v87\n\t"
        "v_mfma_f32_32x32x16_bf16 v[0:15], v[220:223], v[148:151], v[0:15]\n\t"
        "v_exp_f32 v92, v92\n\t"
        "v_exp_f32 v93, v93\n\t"
        "v_exp_f32 v94, v94\n\t"
        "v_exp_f32 v95, v95\n\t"
        "v_cvt_pk_bf16_f32 v156, v88, v89\n\t"
        "v_cvt_pk_bf16_f32 v157, v90, v91\n\t"
        "v_cvt_pk_bf16_f32 v158, v92, v93\n\t"
        "v_cvt_pk_bf16_f32 v159, v94, v95\n\t"
        "v_mfma_f32_32x32x16_bf16 v[80:95], v[208:211], v[136:139], v[96:111]\n\t"
        "v_exp_f32 v64, v64\n\t"
        "v_exp_f32 v65, v65\n\t"
        "v_exp_f32 v66, v66\n\t"
        "v_exp_f32 v67, v67\n\t"
        "v_mfma_f32_32x32x16_bf16 v[80:95], v[212:215], v[140:143], v[80:95]\n\t"
        "v_exp_f32 v68, v68\n\t"
        "v_exp_f32 v69, v69\n\t"
        "v_exp_f32 v70, v70\n\t"
        "v_exp_f32 v71, v71\n\t"
        "v_cvt_pk_bf16_f32 v144, v64, v65\n\t"
        "v_cvt_pk_bf16_f32 v145, v66, v67\n\t"
        "v_mfma_f32_32x32x16_bf16 v[16:31], v[216:219], v[152:155], v[16:31]\n\t"
        "v_exp_f32 v72, v72\n\t"
        "v_exp_f32 v73, v73\n\t"
        "v_exp_f32 v74, v74\n\t"
        "v_exp_f32 v75, v75\n\t"
        "v_cvt_pk_bf16_f32 v146, v68, v69\n\t"
        "v_cvt_pk_bf16_f32 v147, v70, v71\n\t"
        "v_mfma_f32_32x32x16_bf16 v[16:31], v[220:223], v[156:159], v[16:31]\n\t"
        "v_exp_f32 v76, v76\n\t"
        "v_exp_f32 v77, v77\n\t"
        "v_exp_f32 v78, v78\n\t"
        "v_exp_f32 v79, v79\n\t"
        "v_cvt_pk_bf16_f32 v148, v72, v73\n\t"
        "v_cvt_pk_bf16_f32 v149, v74, v75\n\t"
        "v_cvt_pk_bf16_f32 v150, v76, v77\n\t"
        "v_cvt_pk_bf16_f32 v151, v78, v79\n\t"
        "s_waitcnt vmcnt(8)\n\t"
        "v_mfma_f32_32x32x16_bf16 v[64:79], v[160:163], v[112:115], v[96:111]\n\t"
        "s_sub_u32 %[cnt], %[cnt], 1\n\t"
        "s_cmp_lg_u32 %[cnt], 0\n\t"
        "v_exp_f32 v80, v80\n\t"
        "v_exp_f32 v81, v81\n\t"
        "v_exp_f32 v82, v82\n\t"
        "v_exp_f32 v83, v83\n\t"
        "v_mfma_f32_32x32x16_bf16 v[64:79], v[164:167], v[116:119], v[64:79]\n\t"
        "v_exp_f32 v84, v84\n\t"
        "v_exp_f32 v85, v85\n\t"
        "v_exp_f32 v86, v86\n\t"
        "v_exp_f32 v87, v87\n\t"
        "v_cvt_pk_bf16_f32 v152, v80, v81\n\t"
        "v_cvt_pk_bf16_f32 v153, v82, v83\n\t"
        "v_mfma_f32_32x32x16_bf16 v[32:47], v[216:219], v[144:147], v[32:47]\n\t"
        "v_exp_f32 v88, v88\n\t"
        "v_exp_f32 v89, v89\n\t"
        "v_exp_f32 v90, v90\n\t"
        "v_exp_f32 v91, v91\n\t"
        "v_cvt_pk_bf16_f32 v154, v84, v85\n\t"
        "v_cvt_pk_bf16_f32 v155, v86, v87\n\t"
        "v_mfma_f32_32x32x16_bf16 v[32:47], v[220:223], v[148:151], v[32:47]\n\t"
        "v_exp_f32 v92, v92\n\t"
        "v_exp_f32 v93, v93\n\t"
        "v_exp_f32 v94, v94\n\t"
        "v_exp_f32 v95, v95\n\t"
        "v_cvt_pk_bf16_f32 v156, v88, v89\n\t"
        "v_cvt_pk_bf16_f32 v157, v90, v91\n\t"
        "v_cvt_pk_bf16_f32 v158, v92, v93\n\t"
        "v_cvt_pk_bf16_f32 v159, v94, v95\n\t"
        "s_cbranch_scc1 LOOP%=\n\t"
        "v_mfma_f32_32x32x16_bf16 v[48:63], v[216:219], v[152:155], v[48:63]\n\t"
        "v_mfma_f32_32x32x16_bf16 v[48:63], v[220:223], v[156:159], v[48:63]\n\t"
        "s_waitcnt lgkmcnt(0)\n\t"
        "s_nop 15\n\t"
        : "={v[0:15]}"(o0), "={v[16:31]}"(o1), "={v[32:47]}"(o2), "={v[48:63]}"(o3), [sb] "+s"(sb), [cnt] "+s"(cnt),
          "+{v237}"(q2a), "+{v238}"(kba), "+{v239}"(ordn), "={v[112:115]}"(qo0), "={v[116:119]}"(qo1)
        : "{v224}"(vok + 0 * TS), "{v225}"(vov + 0 * TS), "{v226}"(vov + 2 * VROW + 0 * TS), "{v227}"(vok + 1 * TS), "{v228}"(vov + 1 * TS), "{v229}"(vov + 2 * VROW + 1 * TS), "{v230}"(vok + 2 * TS), "{v231}"(vov + 2 * TS), "{v232}"(vov + 2 * VROW + 2 * TS), "{v233}"(vok + 3 * TS), "{v234}"(vov + 3 * TS), "{v235}"(vov + 2 * VROW + 3 * TS),
          "{v236}"(qxa), [rs] "s"(rsKV)
        : "v64", "v65", "v66", "v67", "v68", "v69", "v70", "v71", "v72", "v73", "v74", "v75", "v76", "v77", "v78", "v79", "v80", "v81", "v82", "v83", "v84", "v85", "v86", "v87", "v88", "v89", "v90", "v91", "v92", "v93", "v94", "v95", "v96", "v97", "v98", "v99", "v100", "v101", "v102", "v103", "v104", "v105", "v106", "v107", "v108", "v109", "v110", "v111", "v120", "v121", "v122", "v123", "v124", "v125", "v126", "v127", "v128", "v129", "v130", "v131", "v132", "v133", "v134", "v135", "v136", "v137", "v138", "v139", "v140", "v141", "v142", "v143", "v144", "v145", "v146", "v147", "v148", "v149", "v150", "v151", "v152", "v153", "v154", "v155", "v156", "v157", "v158", "v159", "v160", "v161", "v162", "v163", "v164", "v165", "v166", "v167", "v168", "v169", "v170", "v171", "v172", "v173", "v174", "v175", "v176", "v177", "v178", "v179", "v180", "v181", "v182", "v183", "v184", "v185", "v186", "v187", "v188", "v189", "v190", "v191", "v192", "v193", "v194", "v195", "v196", "v197", "v198", "v199", "v200", "v201", "v202", "v203", "v204", "v205", "v206", "v207", "v208", "v209", "v210", "v211", "v212", "v213", "v214", "v215", "v216", "v217", "v218", "v219", "v220", "v221", "v222", "v223", "v240", "scc", "memory");
    }
    STAMP(7);
#define PUT(i, o) do { LAS char* d_ = lds + L_CB + (w * 3 + (i - 1)) * 4096 + lane * 16;                                       \
        _Pragma("unroll") for (int g = 0; g < 4; ++g) *(LAS f32x4*)(d_ + g * 1024) = (f32x4){o[4 * g], o[4 * g + 1], o[4 * g + 2], o[4 * g + 3]}; } while (0)
    PUT(1, o1); PUT(2, o2); PUT(3, o3);
    BAR();
    f32x16 acc = o0;
#pragma unroll
    for (int s = 1; s < 4; ++s) {
        LAS const char* sp = lds + L_CB + (((w - s) & 3) * 3 + (s - 1)) * 4096 + lane * 16;
#pragma unroll
        for (int g = 0; g < 4; ++g) {
            const f32x4 v = *(LAS const f32x4*)(sp + g * 1024);
            acc[4 * g] += v[0]; acc[4 * g + 1] += v[1]; acc[4 * g + 2] += v[2]; acc[4 * g + 3] += v[3];
        }
    }
    float l = half_hi(acc[12]);
    if (__builtin_expect(__any(!(l >= L_MIN && l <= L_MAX)), 0)) {
        const f32x16 zero = {};
        float m = -__builtin_inff();
#pragma unroll 1
        for (int t = 0; t < 64; ++t) {
            f32x16 s = MFMA(asbf(ld16_wt(rsKV, vok, t * TS)), asbf(qo0), zero);
            s = MFMA(asbf(ld16_wt(rsKV, vok + 1024, t * TS)), asbf(qo1), s);
#pragma unroll
            for (int i = 0; i < 16; ++i) m = __builtin_fmaxf(m, s[i]);
        }
        m = __builtin_fmaxf(half_lo(m), half_hi(m));
        acc = zero;
#pragma unroll 1
        for (int t = 0; t < 64; ++t) {
            f32x16 s = MFMA(asbf(ld16_wt(rsKV, vok, t * TS)), asbf(qo0), zero);
            s = MFMA(asbf(ld16_wt(rsKV, vok + 1024, t * TS)), asbf(qo1), s);
#pragma unroll
            for (int i = 0; i < 16; ++i) s[i] = __builtin_amdgcn_exp2f(s[i] - m);
            acc = MFMA(asbf(ld16_wt(rsKV, vov, t * TS)), asbf(pack8(s, 0)), acc);
            acc = MFMA(asbf(ld16_wt(rsKV, vov + 2 * VROW, t * TS)), asbf(pack8(s, 1)), acc);
        }
        l = half_hi(acc[12]);
    }
    {
        const float rl = __builtin_amdgcn_rcpf(l);
#pragma unroll
        for (int i = 0; i < 16; ++i) acc[i] = (i >= 12 && h) ? 0.f : acc[i] * rl;
        const unsigned so = (kt * NH + hd) * 2048;
        u32x4 p0 = pack8(acc, 0), p1 = pack8(acc, 1);
#pragma unroll
        for (int i = 0; i < 4; ++i) { p0[i] = min(p0[i], POISON - 1); p1[i] = min(p1[i], POISON - 1); }
        st16_wt(p0, rsOI, lane * 16, so);
        st16_wt(p1, rsOI, lane * 16 + 1024, so);
        if (lane < 4) __hip_atomic_store((GAS u64*)(wsg + WS_F + F_O + (qt * 16 + lane * 4 + w) * 2048 + hd * 128), (u64)tag, RLX_AGENT);
    }
    STAMP(8);
    BAR();

    bf16x8 wf[32];
    {
        LAS const char* wb = lds + L_WO + (4 * h + q4) * 256 + ((w ^ q4) * 64) + g16 * 32 + p4 * 8;
#pragma unroll
        for (int k = 0; k < 32; ++k) {
            const int ro = ((k >> 1) * DH + (k & 1) * 16) * 256;
            wf[k] = cat(vtr(wb + ro), vtr(wb + ro + 2048 - (k == 31 ? h * 1024 : 0)));
        }
    }
    STAMP(10);
    BAR();
    STAMP(11);
    if (cg < 3 || w < 2) {
        f32x16 c = {};
        bf16x8 of[3][4];
#define RDO(i) do { _Pragma("unroll") for (int j = 0; j < 4; ++j) of[(i) % 3][j] = *(LAS const bf16x8*)(lds + L_CB + ((i) * 4 + j) * 1024 + lane * 16); } while (0)
        RDO(0); RDO(1); SBAR();
#pragma unroll
        for (int i = 0; i < 8; ++i) {
            if (i + 2 < 8) RDO(i + 2);
            SBAR();
#pragma unroll
            for (int j = 0; j < 4; ++j) c = MFMA(wf[i * 4 + j], of[i % 3][j], c);
            SBAR();
        }
#undef RDO
        STAMP(12);
        char* orow = out + (size_t)(q32 * 32 + r) * (DM * 2) + cg * 256 + w * 64 + h * 16;
#pragma unroll
        for (int g = 0; g < 4; g += 2) {
            const unsigned x0 = cvtpk(c[4 * g], c[4 * g + 1]), x1 = cvtpk(c[4 * g + 2], c[4 * g + 3]);
            const unsigned y0 = cvtpk(c[4 * g + 4], c[4 * g + 5]), y1 = cvtpk(c[4 * g + 6], c[4 * g + 7]);
            auto s0 = __builtin_amdgcn_permlane32_swap(x0, y0, false, false);
            auto s1 = __builtin_amdgcn_permlane32_swap(x1, y1, false, false);
            *(u32x4*)(orow + g * 16) = (u32x4){s0[0], s1[0], s0[1], s1[1]};
        }
    }
    STAMP(13);
    STAMP_FLUSH();
}

extern "C" void kernel_launch(void* const* d_in, const int* in_sizes, int n_in, void* d_out, int out_size, void* d_ws, size_t ws_size,
                              hipStream_t stream) {
    (void)hipFuncSetAttribute((const void*)mha_kernel, hipFuncAttributeMaxDynamicSharedMemorySize, LDS_BYTES);
    mha_kernel<<<256, 512, LDS_BYTES, stream>>>((const char*)d_in[0], (const char*)d_in[1], (const char*)d_in[2], (char*)d_out, (char*)d_ws);
}
```

```cpp
#include <hip/hip_runtime.h>
#include <hip/hip_bf16.h>
#include <stdint.h>

typedef short bf16x8 __attribute__((ext_vector_type(8)));
typedef short s16x4 __attribute__((ext_vector_type(4)));
typedef float f32x16 __attribute__((ext_vector_type(16)));
typedef float f32x4 __attribute__((ext_vector_type(4)));
typedef float f32x2 __attribute__((ext_vector_type(2)));
typedef unsigned u32x4 __attribute__((ext_vector_type(4)));
typedef __bf16 bf16x2_t __attribute__((ext_vector_type(2)));
typedef short v4i16_t __attribute__((ext_vector_type(4)));
typedef unsigned long long u64;
#define LAS __attribute__((address_space(3)))
#define GAS __attribute__((address_space(1)))
#define DI __device__ __forceinline__
#define MFMA(a, b, c) __builtin_amdgcn_mfma_f32_32x32x16_bf16((a), (b), (c), 0, 0, 0)
#define SBAR() __builtin_amdgcn_sched_barrier(0)
#define RLX_AGENT __ATOMIC_RELAXED, __HIP_MEMORY_SCOPE_AGENT
#define BAR() asm volatile("s_waitcnt lgkmcnt(0)\n\ts_barrier" ::: "memory")

#define PRETOUCH 2
#define MAPPING 1
#define MAP_HD(b) (((b) & 7) * 2 + (((b) >> 3) & 1))
#define MAP_QT(b) ((b) >> 4)
constexpr int SEQ = 2048, DM = 448, NH = 16, DH = 28, N3 = 1344;
constexpr float QSCALE = 0.18898223650461362f * 1.4426950408889634f;
constexpr float L_MIN = 9.094947017729282e-13f, L_MAX = 1.099511627776e12f;
constexpr unsigned VROW = 464 * 16, TS = 4 * VROW;
constexpr unsigned WS_K = 0, WS_V = 2u << 20, WS_O = 4u << 20, WS_F = 6u << 20;
constexpr unsigned POISON = 0xffffffffu;
constexpr unsigned F_G = 0, F_R = 4096, F_KV = 131072, F_O = 262144;
constexpr unsigned L_WQ = 0, L_RING = 28672, STG = 24576;
constexpr unsigned L_QX = 131072, L_Q2 = L_QX + 8192, L_GB = L_Q2 + 512;
constexpr unsigned L_WO = 0, L_CB = 114688;
constexpr unsigned LDS_BYTES = 163840;

#define STAMP_DECL
#define STAMP(i)
#define STAMP_FLUSH()

DI unsigned rfl(unsigned x) { return __builtin_amdgcn_readfirstlane(x); }
DI unsigned cvtpk(float lo, float hi) { f32x2 v = {lo, hi}; bf16x2_t b = __builtin_convertvector(v, bf16x2_t); return __builtin_bit_cast(unsigned, b); }
DI u32x4 pack8(const f32x16& x, int s) {
    return (u32x4){cvtpk(x[8 * s], x[8 * s + 1]), cvtpk(x[8 * s + 2], x[8 * s + 3]), cvtpk(x[8 * s + 4], x[8 * s + 5]), cvtpk(x[8 * s + 6], x[8 * s + 7])};
}
DI bf16x8 asbf(u32x4 x) { return __builtin_bit_cast(bf16x8, x); }
DI s16x4 vtr(LAS const char* p) { return __builtin_bit_cast(s16x4, __builtin_amdgcn_ds_read_tr16_b64_v4i16((LAS v4i16_t*)p)); }
DI bf16x8 cat(s16x4 lo, s16x4 hi) { return __builtin_shufflevector(lo, hi, 0, 1, 2, 3, 4, 5, 6, 7); }
DI float half_lo(float x) { auto r = __builtin_amdgcn_permlane32_swap(__float_as_uint(x), __float_as_uint(x), false, false); return __uint_as_float(r[0]); }
DI float half_hi(float x) { auto r = __builtin_amdgcn_permlane32_swap(__float_as_uint(x), __float_as_uint(x), false, false); return __uint_as_float(r[1]); }
DI unsigned wave_max_u(unsigned x) {
#define DPPMAX(ctrl, rm) x = max(x, (unsigned)__builtin_amdgcn_update_dpp((int)x, (int)x, ctrl, rm, 0xf, false))
    DPPMAX(0x111, 0xf); DPPMAX(0x112, 0xf); DPPMAX(0x114, 0xf); DPPMAX(0x118, 0xf); DPPMAX(0x142, 0xa); DPPMAX(0x143, 0xc);
#undef DPPMAX
    return (unsigned)__builtin_amdgcn_readlane((int)x, 63);
}
DI void dma16(unsigned lds_dst, unsigned voff, __amdgpu_buffer_rsrc_t rs, unsigned soff) {
    asm volatile("s_mov_b32 m0, %0\n\ts_nop 0\n\tbuffer_load_dwordx4 %1, %2, %3 offen lds" ::"s"(lds_dst), "v"(voff), "s"(rs), "s"(soff) : "m0", "memory");
}
DI void st16_wt(u32x4 v, __amdgpu_buffer_rsrc_t rs, unsigned voff, unsigned soff) { __builtin_amdgcn_raw_buffer_store_b128(v, rs, voff, soff, 16); }
DI u32x4 ld16_wt(__amdgpu_buffer_rsrc_t rs, unsigned voff, unsigned soff) { return __builtin_amdgcn_raw_buffer_load_b128(rs, voff, soff, 16); }
DI __amdgpu_buffer_rsrc_t mkrs(const void* p, unsigned bytes) { return __builtin_amdgcn_make_buffer_rsrc((void*)p, (short)0, (int)bytes, 0x00020000); }
DI unsigned poll_tags(const GAS u64* g, unsigned tag) {
    u64 v = __hip_atomic_load(g, RLX_AGENT);
#pragma nounroll
    for (unsigned spins = 0; spins < (1u << 16); ++spins) {
        if (__all((unsigned)v == tag)) break;
        if ((unsigned)v != tag) v = __hip_atomic_load(g, RLX_AGENT);
    }
    __builtin_amdgcn_fence(__ATOMIC_ACQUIRE, "wavefront");
    return (unsigned)(v >> 32);
}
DI unsigned touch(const GAS char* p) { unsigned t; asm volatile("global_load_dword %0, %1, off" : "=v"(t) : "v"(p) : "memory"); return t; }

template <bool ISK>
DI void kv_gemm(LAS char* lds, unsigned w, int lane, bf16x8 (&xf)[7][4], f32x16& a0, f32x16& a1) {
    const int r = lane & 31, h = lane >> 5, i16 = lane & 15, q4 = i16 >> 2, p4 = i16 & 3, g16 = (lane >> 4) & 1;
    const int pc = ISK ? (((p4 & 1) << 1) | (p4 >> 1)) : p4;
    LAS const char* xb = lds + L_RING + w * 4096 + r * 128;
    LAS const char* wb0 = lds + L_RING + 16384 + (8 * h + q4) * 128 + ((q4 >> 1) * 64) + g16 * 32 + pc * 8;
    LAS const char* wb1 = lds + L_RING + 16384 + (8 * h + q4) * 128 + (((q4 >> 1) ^ 1) * 64) + g16 * 32 + pc * 8;
    const int xs = (r >> 1) & 7;
    bf16x8 wf[2][4][2];
#define RD(i) do { const int so = ((i) % 5) * STG; _Pragma("unroll") for (int ks = 0; ks < 4; ++ks) {                          \
        xf[i][ks] = *(LAS const bf16x8*)(xb + so + (((2 * ks + h) ^ xs) * 16));                                                \
        wf[(i) & 1][ks][0] = cat(vtr(wb0 + so + ks * 2048), vtr(wb0 + so + ks * 2048 + 512));                                  \
        wf[(i) & 1][ks][1] = cat(vtr(wb1 + so + ks * 2048), vtr(wb1 + so + ks * 2048 + 512)); } } while (0)
#define MM(i) do { _Pragma("unroll") for (int ks = 0; ks < 4; ++ks) {                                                          \
        if (ISK) { a0 = MFMA(wf[(i) & 1][ks][0], xf[i][ks], a0); a1 = MFMA(wf[(i) & 1][ks][1], xf[i][ks], a1); }               \
        else     { a0 = MFMA(xf[i][ks], wf[(i) & 1][ks][0], a0); a1 = MFMA(xf[i][ks], wf[(i) & 1][ks][1], a1); } } } while (0)
    BAR(); RD(0);
#pragma unroll
    for (int i = 0; i < 5; ++i) { BAR(); RD(i + 1); SBAR(); MM(i); SBAR(); }
    MM(5); SBAR();
    BAR(); RD(6); MM(6);
#undef RD
#undef MM
}

DI void stage_dma(unsigned mw, unsigned mx, unsigned sw, unsigned sx, unsigned wvo, unsigned xvo0, unsigned xvo1, __amdgpu_buffer_rsrc_t rsW,
                  __amdgpu_buffer_rsrc_t rsX) {
    unsigned t;
    asm volatile("s_mov_b32 m0, %1\n\ts_add_u32 %0, %3, 21504\n\tbuffer_load_dwordx4 %5, %8, %3 offen lds\n\t"
                 "s_add_u32 m0, m0, 1024\n\ts_nop 0\n\tbuffer_load_dwordx4 %5, %8, %0 offen lds\n\t"
                 "s_mov_b32 m0, %2\n\ts_add_u32 %0, %4, 7168\n\tbuffer_load_dwordx4 %6, %9, %4 offen lds\n\t"
                 "s_add_u32 m0, m0, 1024\n\ts_nop 0\n\tbuffer_load_dwordx4 %7, %9, %0 offen lds\n\t"
                 "s_add_u32 m0, m0, 1024\n\ts_add_u32 %0, %4, 14336\n\tbuffer_load_dwordx4 %6, %9, %0 offen lds\n\t"
                 "s_add_u32 m0, m0, 1024\n\ts_add_u32 %0, %4, 21504\n\tbuffer_load_dwordx4 %7, %9, %0 offen lds"
                 : "=&s"(t) : "s"(mw), "s"(mx), "s"(sw), "s"(sx), "v"(wvo), "v"(xvo0), "v"(xvo1), "s"(rsW), "s"(rsX) : "m0", "scc", "memory");
}

__global__ __launch_bounds__(512, 2) void mha_kernel(const char* __restrict__ x, const char* __restrict__ wqkv, const char* __restrict__ wo,
                                                     char* __restrict__ out, char* __restrict__ ws) {
    extern __shared__ __attribute__((aligned(16))) char lds_[];
    LAS char* lds = (LAS char*)lds_;
    STAMP_DECL STAMP(0);
    const unsigned wv = rfl(threadIdx.x >> 6), w = wv & 3;
    const unsigned lds0 = (unsigned)(uintptr_t)lds_;
    GAS char* wsg = (GAS char*)ws;
#define DECODE()                                                                                                              \
    unsigned b = blockIdx.x, lane = threadIdx.x & 63;                                                                        \
    asm volatile("" : "+s"(b)); asm volatile("" : "+v"(lane));                                                                \
    const unsigned hd = MAP_HD(b), qt = MAP_QT(b);                                                                            \
    const unsigned ch = hd < 14 ? hd : hd - 2;         \
    const unsigned rot = (hd + 2 * qt) % 7;
#define DECODE2()                                                                                                             \
    const bool isK = ch < 7;                                                                                                  \
    const unsigned g0 = (7 * hd) >> 1;                                                  \
    const unsigned cg = hd >> 2, q32 = qt * 4 + (hd & 3);                   \
    unsigned kbi[7];                                                                                                          \
    _Pragma("unroll") for (int i = 0; i < 7; ++i) { const unsigned k = rot + i; kbi[i] = k >= 7 ? k - 7 : k; }               \
    const __amdgpu_buffer_rsrc_t rsOI = mkrs(ws + WS_O, 2u << 20);

    if (__builtin_expect(wv >= 4, 1)) {
        DECODE();
        const unsigned xrow = qt * 128 + w * 32 + (lane >> 3);
        const unsigned xvo0 = xrow * (DM * 2) + (((lane & 7) ^ (lane >> 4)) * 16), xvo1 = xvo0 ^ 64;
        const unsigned wvo = (16 * w + (lane >> 3)) * (N3 * 2) + 896 + ch * 128 + (((lane & 7) ^ (((lane >> 4) & 1) << 2)) * 16);
        const __amdgpu_buffer_rsrc_t rsW = mkrs(wqkv, DM * N3 * 2), rsX = mkrs(x, SEQ * DM * 2);
        STAMP(14);
        stage_dma(lds0 + L_RING + 16384 + w * 2048, lds0 + L_RING + w * 4096, rot * (64 * N3 * 2), rot * 128, wvo, xvo0, xvo1, rsW, rsX);
        DECODE2();
        const __amdgpu_buffer_rsrc_t rsF = mkrs(ws + WS_F, 4096);
#define STAGE(i) stage_dma(lds0 + L_RING + ((i) % 5) * STG + 16384 + w * 2048, lds0 + L_RING + ((i) % 5) * STG + w * 4096, kbi[i] * (64 * N3 * 2), kbi[i] * 128, \
                           wvo, xvo0, xvo1, rsW, rsX)
#define VBAR(NV) asm volatile("s_waitcnt vmcnt(" #NV ")\n\ts_barrier" ::: "memory")
        STAGE(1); STAGE(2);
        STAMP(1);
        VBAR(12); STAGE(3);
        STAMP(2);
        VBAR(12); STAGE(4);
        VBAR(12); STAGE(5);
        VBAR(12); STAGE(6);
        VBAR(12);
        unsigned gen;
        { const unsigned vo = F_G; asm volatile("buffer_load_dword %0, %1, %2, 0 offen sc1" : "=v"(gen) : "v"(vo), "s"(rsF) : "memory"); }
        {
            const unsigned qvo = (16 * w + (lane >> 2)) * (N3 * 2) + g0 * 16 + (lane & 3) * 16;
#pragma unroll
            for (int j = 0; j < 7; ++j) dma16(lds0 + L_WQ + (16 * w + 64 * kbi[j]) * 64, qvo, rsW, kbi[j] * 64 * (N3 * 2));
        }
        VBAR(14); VBAR(8);
        STAMP(3);
        asm volatile("s_waitcnt vmcnt(7)" : "+v"(gen)::"memory");
        unsigned tag = rfl(gen);
        { const unsigned t = (tag ^ (tag >> 15)) * 0x2c1b3c6du + 0x9e3779b9u; tag = (t == tag) ? t + 1 : t; }
        const unsigned hh = lane & 15, wh = (lane >> 4) & 1;
        const unsigned c = isK ? (((7 * hh >> 1) + 3 * wh) >> 3) : 7 + ((28 * hh + 27 * wh) >> 6);
        GAS u64* fp = (GAS u64*)(wsg + WS_F + (lane < 32 ? F_KV + ((hh * 4 + (isK ? 0 : 2) + wh) * 16 + qt) * 128 : F_R + (qt * 16 + hd) * 8));
        const bool fon = w == 0 && ((lane < 32 && c == hd) || lane == 32);
        if (w == 0 && lane == 0) *(LAS unsigned*)(lds + L_GB + 20) = tag;
        asm volatile("s_waitcnt lgkmcnt(0)\n\ts_barrier" : "+v"(fp)::"memory");
        if (w == 0) {
            const u32x4 gb = *(LAS const u32x4*)(lds + L_GB);
            if (fon) __hip_atomic_store(fp, ((u64)max(max(gb[0], gb[1]), max(gb[2], gb[3])) << 32) | tag, RLX_AGENT);
            asm volatile("s_waitcnt vmcnt(1)" ::: "memory");
        } else {
            asm volatile("s_waitcnt vmcnt(0)" ::: "memory");
        }
        STAMP(4);
        asm volatile("s_waitcnt lgkmcnt(0)\n\ts_barrier" ::: "memory");
        LAS unsigned* ord = (LAS unsigned*)(lds + L_CB + w * 12288 + (w == 2 ? 2048 : 0));
        if (lane < 5) ord[lane] = lane == 4 ? 0x40000000u : 0xffffffffu;
#pragma nounroll
        for (unsigned j = 0; j < 4; ++j) {
            const unsigned tt = w + 4 * ((j + (qt >> 2)) & 3);
            const unsigned g = poll_tags((const GAS u64*)(wsg + WS_F + F_KV + hd * 8192 + ((lane & 3) * 16 + tt) * 128), tag);
            if (lane == 0) ord[j] = tt * (4 * TS);
            if (j == 0) {
                const unsigned gm = wave_max_u(g);
                if (tt == qt && lane == 0) *(LAS unsigned*)(lds + L_GB + 16) = gm;
                STAMP(5);
                BAR();
                STAMP(6);
                asm volatile("s_barrier" ::: "memory");
            }
        }
        {
            const __amdgpu_buffer_rsrc_t rsO = mkrs(wo, DM * DM * 2);
            const unsigned vow = (4 * w + (lane >> 4)) * (DM * 2) + cg * 256 + ((((lane >> 2) & 3) ^ (lane >> 4)) * 64) + (lane & 3) * 16;
            __builtin_amdgcn_s_sleep(8);
#pragma unroll 1
            for (unsigned j = 0; j < 28; ++j) { dma16(lds0 + L_WO + (4 * j + w) * 1024, vow, rsO, j * 14336); __builtin_amdgcn_s_sleep(1); }
        }
        st16_wt((u32x4){POISON, POISON, POISON, POISON}, rsOI, lane * 16, ((qt * 4 + w) * NH + hd) * 2048);
        st16_wt((u32x4){POISON, POISON, POISON, POISON}, rsOI, lane * 16 + 1024, ((qt * 4 + w) * NH + hd) * 2048);
        if (lane < 4) __hip_atomic_store((GAS u64*)(wsg + WS_F + F_O + (qt * 16 + lane * 4 + w) * 2048 + hd * 128), (u64)(tag ^ 1), RLX_AGENT);
        VBAR(0);
        STAMP(8);
        asm volatile("s_barrier" ::: "memory");
        STAMP(9);
        {
            u32x4 t[8];
            (void)poll_tags((const GAS u64*)(wsg + WS_F + F_O + (qt * 16 + hd) * 2048 + (w * 4 + (lane & 3)) * 128), tag);
            STAMP(7);
#pragma nounroll
            for (unsigned spins = 0; spins < (1u << 16); ++spins) {
                unsigned m = 0;
                asm volatile("" ::: "memory");
#pragma unroll
                for (int f = 0; f < 8; ++f) t[f] = ld16_wt(rsOI, lane * 16 + f * 1024, q32 * (NH * 2048) + w * 8192);
#pragma unroll
                for (int f = 0; f < 8; ++f) m = max(m, max(max(t[f][0], t[f][1]), max(t[f][2], t[f][3])));
                if (__all(m != POISON)) break;
            }
            STAMP(10);
#pragma unroll
            for (int f = 0; f < 8; ++f) *(LAS u32x4*)(lds + L_CB + (w * 8 + f) * 1024 + lane * 16) = t[f];
        }
        BAR();
        STAMP(11);
        if (w == 1 && b == 0) {
#pragma unroll 1
            for (int j = 0; j < 4; ++j) (void)poll_tags((const GAS u64*)(wsg + WS_F + F_R) + j * 64 + lane, tag);
            if (lane == 0) __hip_atomic_store((GAS unsigned*)(wsg + WS_F + F_G), tag, RLX_AGENT);
        }
        STAMP_FLUSH();
        return;
    }

    DECODE(); DECODE2();
    const int r = lane & 31, h = lane >> 5, i16 = lane & 15, q4 = i16 >> 2, p4 = i16 & 3, g16 = (lane >> 4) & 1;
    f32x16 a0 = {}, a1 = {};
    bf16x8 xf[7][4];
    const unsigned kt = qt * 4 + w;
    unsigned gmx = 0;
    unsigned tch1 = 0;
    {
        unsigned long long pc0;
        asm volatile("s_getpc_b64 %0" : "=s"(pc0));
        tch1 = touch((const GAS char*)(pc0 + (((b >> 3) % 3) * 4 + w) * 1024 + (lane & 7) * 128));
    }
    if (isK) {
        kv_gemm<true>(lds, w, lane, xf, a0, a1);
        STAMP(3);
        {
            const __amdgpu_buffer_rsrc_t rs = mkrs(ws + WS_K, 64 * TS);
            const unsigned vo = (8 * ch + h) * 512 + r * 16, so = kt * TS;
            st16_wt(pack8(a0, 0), rs, vo, so); st16_wt(pack8(a0, 1), rs, vo + 1024, so);
            st16_wt(pack8(a1, 0), rs, vo + 2048, so); st16_wt(pack8(a1, 1), rs, vo + 3072, so);
            float s0 = 0.f, s1 = 0.f, s2 = 0.f, s3 = 0.f;
#pragma unroll
            for (int j = 0; j < 8; ++j) { s0 += a0[j] * a0[j]; s1 += a0[8 + j] * a0[8 + j]; s2 += a1[j] * a1[j]; s3 += a1[8 + j] * a1[8 + j]; }
            gmx = max(max(__float_as_uint(s0), __float_as_uint(s1)), max(__float_as_uint(s2), __float_as_uint(s3)));
            gmx = wave_max_u(gmx);
        }
    } else {
        kv_gemm<false>(lds, w, lane, xf, a0, a1);
        STAMP(3);
        if (hd < 14) {
        const __amdgpu_buffer_rsrc_t rs = mkrs(ws + WS_V, 64 * TS);
        const unsigned n0 = 64 * (ch - 7) + r, n1 = n0 + 32, so = kt * TS;
        const unsigned v0 = h * VROW + (n0 + (n0 * 2341 >> 16)) * 16, v1 = h * VROW + (n1 + (n1 * 2341 >> 16)) * 16;
        st16_wt(pack8(a0, 0), rs, v0, so); st16_wt(pack8(a0, 1), rs, v0 + 2 * VROW, so);
        st16_wt(pack8(a1, 0), rs, v1, so); st16_wt(pack8(a1, 1), rs, v1 + 2 * VROW, so);
        const unsigned hc = (64 * (ch - 7) * 2341 >> 16) + (lane >> 2), last = 28 * hc + 27;
        if (lane < 16 && (last >> 6) == ch - 7)
            st16_wt((u32x4){0x3f803f80u, 0x3f803f80u, 0x3f803f80u, 0x3f803f80u}, rs, (lane & 3) * VROW + (29 * hc + 28) * 16, so);
        }
    }
    if (lane == 0) *(LAS unsigned*)(lds + L_GB + w * 4) = gmx;
    unsigned vok = WS_K + (g0 + h) * 512 + r * 16, vov = WS_V + h * VROW + (29 * hd + r) * 16;
    const __amdgpu_buffer_rsrc_t rsKV = mkrs(ws, 4u << 20);
    unsigned q2a = lds0 + L_Q2 + r * 16, kba = lds0 + L_GB + 16, ordn = lds0 + L_CB + w * 12288 + (w == 2 ? 2048 : 0) + 4, puta = lds0 + L_CB + w * 12288 + lane * 16;
    unsigned qxa[4];
#pragma unroll
    for (int i = 0; i < 4; ++i) qxa[i] = lds0 + L_QX + ((w + 1 + i) & 3) * 2048 + lane * 16;
    LAS const char* qb = lds + L_WQ + (8 * h + q4) * 64 + g16 * 32 + (((p4 & 1) << 1) | (p4 >> 1)) * 8;
    asm volatile("" : "+v"(vok), "+v"(vov), "+v"(q2a), "+v"(kba), "+v"(ordn), "+v"(puta), "+v"(qxa[0]), "+v"(qxa[1]), "+v"(qxa[2]), "+v"(qxa[3]), "+v"(qb));
    STAMP(1);
    asm volatile("s_waitcnt vmcnt(0) lgkmcnt(0)" : "+v"(tch1)::"memory");
    STAMP(2);
    asm volatile("s_barrier" ::: "memory");
    STAMP(4);
    asm volatile("s_barrier" ::: "memory");
    STAMP(14);
    const unsigned tag = rfl(*(LAS const unsigned*)(lds + L_GB + 20));
    f32x16 aq = {};
    {
        bf16x8 wq[3][4];
#define RDQ(i) do { LAS const char* qk = qb + kbi[i] * 4096; _Pragma("unroll") for (int ks = 0; ks < 4; ++ks)                  \
            wq[(i) % 3][ks] = cat(vtr(qk + ks * 1024), vtr(qk + ks * 1024 + 256)); } while (0)
        RDQ(0); RDQ(1); SBAR();
#pragma unroll
        for (int i = 0; i < 7; ++i) {
            if (i + 2 < 7) RDQ(i + 2);
            SBAR();
#pragma unroll
            for (int ks = 0; ks < 4; ++ks) aq = MFMA(wq[i % 3][ks], xf[i][ks], aq);
            SBAR();
        }
#undef RDQ
    }
    asm volatile("" : "+v"(aq));
    STAMP(15);
    {
        const bool zlo = (hd & 1) && !h, zhi = !(hd & 1) && h;
        float qs[4] = {0.f, 0.f, 0.f, 0.f};
#pragma unroll
        for (int i = 0; i < 16; ++i) {
            aq[i] *= QSCALE;
            if (i < 4) aq[i] = zlo ? 0.f : aq[i];
            if (i >= 12) aq[i] = zhi ? 0.f : aq[i];
            qs[i & 3] += aq[i] * aq[i];
        }
        *(LAS u32x4*)(lds + (qxa[3] - lds0)) = pack8(aq, 0);
        *(LAS u32x4*)(lds + (qxa[3] - lds0) + 1024) = pack8(aq, 1);
        float q2 = (qs[0] + qs[1]) + (qs[2] + qs[3]);
        q2 = half_lo(q2) + half_hi(q2);
        *(LAS float*)(lds + (q2a - lds0) + w * 4) = q2;
    }
    STAMP(5);

    f32x16 acc;
    u32x4 qo0, qo1;
    {
        unsigned sb = (w + (qt & 12)) * (4 * TS), cnt = 4;
        asm volatile("s_setprio 2\n\ts_waitcnt lgkmcnt(0)\n\ts_barrier\n\t"
        "buffer_load_dwordx4 v[160:163], v224, %[rs], %[sb] offen sc1\n\t"
        "buffer_load_dwordx4 v[164:167], v224, %[rs], %[sb] offen offset:1024 sc1\n\t"
        "buffer_load_dwordx4 v[168:171], v225, %[rs], %[sb] offen sc1\n\t"
        "buffer_load_dwordx4 v[172:175], v226, %[rs], %[sb] offen sc1\n\t"
        "buffer_load_dwordx4 v[176:179], v227, %[rs], %[sb] offen sc1\n\t"
        "buffer_load_dwordx4 v[180:183], v227, %[rs], %[sb] offen offset:1024 sc1\n\t"
        "buffer_load_dwordx4 v[184:187], v228, %[rs], %[sb] offen sc1\n\t"
        "buffer_load_dwordx4 v[188:191], v229, %[rs], %[sb] offen sc1\n\t"
        "buffer_load_dwordx4 v[192:195], v230, %[rs], %[sb] offen sc1\n\t"
        "buffer_load_dwordx4 v[196:199], v230, %[rs], %[sb] offen offset:1024 sc1\n\t"
        "buffer_load_dwordx4 v[200:203], v231, %[rs], %[sb] offen sc1\n\t"
        "buffer_load_dwordx4 v[204:207], v232, %[rs], %[sb] offen sc1\n\t"
        "ds_read_b128 v[112:115], v241\n\t"
        "ds_read_b128 v[116:119], v241 offset:1024\n\t"
        "ds_read_b128 v[120:123], v242\n\t"
        "ds_read_b128 v[124:127], v242 offset:1024\n\t"
        "ds_read_b128 v[128:131], v243\n\t"
        "ds_read_b128 v[132:135], v243 offset:1024\n\t"
        "ds_read_b128 v[136:139], v244\n\t"
        "ds_read_b128 v[140:143], v244 offset:1024\n\t"
        "ds_read_b128 v[80:83], v236\n\t"
        "ds_read_b32 v237, v237\n\t"
        "v_mov_b32 v0, 0\n\t"
        "v_mov_b32 v1, 0\n\t"
        "v_mov_b32 v2, 0\n\t"
        "v_mov_b32 v3, 0\n\t"
        "v_mov_b32 v4, 0\n\t"
        "v_mov_b32 v5, 0\n\t"
        "v_mov_b32 v6, 0\n\t"
        "v_mov_b32 v7, 0\n\t"
        "v_mov_b32 v8, 0\n\t"
        "v_mov_b32 v9, 0\n\t"
        "v_mov_b32 v10, 0\n\t"
        "v_mov_b32 v11, 0\n\t"
        "v_mov_b32 v12, 0\n\t"
        "v_mov_b32 v13, 0\n\t"
        "v_mov_b32 v14, 0\n\t"
        "v_mov_b32 v15, 0\n\t"
        "v_mov_b32 v16, 0\n\t"
        "v_mov_b32 v17, 0\n\t"
        "v_mov_b32 v18, 0\n\t"
        "v_mov_b32 v19, 0\n\t"
        "v_mov_b32 v20, 0\n\t"
        "v_mov_b32 v21, 0\n\t"
        "v_mov_b32 v22, 0\n\t"
        "v_mov_b32 v23, 0\n\t"
        "v_mov_b32 v24, 0\n\t"
        "v_mov_b32 v25, 0\n\t"
        "v_mov_b32 v26, 0\n\t"
        "v_mov_b32 v27, 0\n\t"
        "v_mov_b32 v28, 0\n\t"
        "v_mov_b32 v29, 0\n\t"
        "v_mov_b32 v30, 0\n\t"
        "v_mov_b32 v31, 0\n\t"
        "v_mov_b32 v32, 0\n\t"
        "v_mov_b32 v33, 0\n\t"
        "v_mov_b32 v34, 0\n\t"
        "v_mov_b32 v35, 0\n\t"
        "v_mov_b32 v36, 0\n\t"
        "v_mov_b32 v37, 0\n\t"
        "v_mov_b32 v38, 0\n\t"
        "v_mov_b32 v39, 0\n\t"
        "v_mov_b32 v40, 0\n\t"
        "v_mov_b32 v41, 0\n\t"
        "v_mov_b32 v42, 0\n\t"
        "v_mov_b32 v43, 0\n\t"
        "v_mov_b32 v44, 0\n\t"
        "v_mov_b32 v45, 0\n\t"
        "v_mov_b32 v46, 0\n\t"
        "v_mov_b32 v47, 0\n\t"
        "v_mov_b32 v48, 0\n\t"
        "v_mov_b32 v49, 0\n\t"
        "v_mov_b32 v50, 0\n\t"
        "v_mov_b32 v51, 0\n\t"
        "v_mov_b32 v52, 0\n\t"
        "v_mov_b32 v53, 0\n\t"
        "v_mov_b32 v54, 0\n\t"
        "v_mov_b32 v55, 0\n\t"
        "v_mov_b32 v56, 0\n\t"
        "v_mov_b32 v57, 0\n\t"
        "v_mov_b32 v58, 0\n\t"
        "v_mov_b32 v59, 0\n\t"
        "v_mov_b32 v60, 0\n\t"
        "v_mov_b32 v61, 0\n\t"
        "v_mov_b32 v62, 0\n\t"
        "v_mov_b32 v63, 0\n\t"
        "v_mov_b32 v152, 0\n\t"
        "v_mov_b32 v153, 0\n\t"
        "v_mov_b32 v154, 0\n\t"
        "v_mov_b32 v155, 0\n\t"
        "v_mov_b32 v156, 0\n\t"
        "v_mov_b32 v157, 0\n\t"
        "v_mov_b32 v158, 0\n\t"
        "v_mov_b32 v159, 0\n\t"
        "v_mov_b32 v216, 0\n\t"
        "v_mov_b32 v217, 0\n\t"
        "v_mov_b32 v218, 0\n\t"
        "v_mov_b32 v219, 0\n\t"
        "v_mov_b32 v220, 0\n\t"
        "v_mov_b32 v221, 0\n\t"
        "v_mov_b32 v222, 0\n\t"
        "v_mov_b32 v223, 0\n\t"
        "s_waitcnt lgkmcnt(0)\n\t"
        "s_barrier\n\t"
        "v_max3_f32 v236, v80, v81, v82\n\t"
        "v_max_f32 v236, v236, v83\n\t"
        "v_mul_f32 v236, v236, v237\n\t"
        "v_sqrt_f32 v236, v236\n\t"
        "s_nop 0\n\t"
        "v_mul_f32 v96, 0xc0028f5c, v236\n\t"
        "v_mov_b32 v97, v96\n\t"
        "v_mov_b32 v98, v96\n\t"
        "v_mov_b32 v99, v96\n\t"
        "v_mov_b32 v100, v96\n\t"
        "v_mov_b32 v101, v96\n\t"
        "v_mov_b32 v102, v96\n\t"
        "v_mov_b32 v103, v96\n\t"
        "v_mov_b32 v104, v96\n\t"
        "v_mov_b32 v105, v96\n\t"
        "v_mov_b32 v106, v96\n\t"
        "v_mov_b32 v107, v96\n\t"
        "v_mov_b32 v108, v96\n\t"
        "v_mov_b32 v109, v96\n\t"
        "v_mov_b32 v110, v96\n\t"
        "v_mov_b32 v111, v96\n\t"
        "s_waitcnt vmcnt(8)\n\t"
        "v_mfma_f32_32x32x16_bf16 v[64:79], v[160:163], v[112:115], v[96:111]\n\t"
        "v_mfma_f32_32x32x16_bf16 v[64:79], v[164:167], v[116:119], v[64:79]\n\t"
        "s_nop 15\n\t"
        "s_nop 3\n\t"
        "LOOP%=:\n\t"
        "v_mfma_f32_32x32x16_bf16 v[80:95], v[160:163], v[120:123], v[96:111]\n\t"
        "v_exp_f32 v64, v64\n\t"
        "v_exp_f32 v65, v65\n\t"
        "v_exp_f32 v66, v66\n\t"
        "v_exp_f32 v67, v67\n\t"
        "v_mfma_f32_32x32x16_bf16 v[80:95], v[164:167], v[124:127], v[80:95]\n\t"
        "v_exp_f32 v68, v68\n\t"
        "v_exp_f32 v69, v69\n\t"
        "v_exp_f32 v70, v70\n\t"
        "v_exp_f32 v71, v71\n\t"
        "v_cvt_pk_bf16_f32 v144, v64, v65\n\t"
        "v_cvt_pk_bf16_f32 v145, v66, v67\n\t"
        "v_mfma_f32_32x32x16_bf16 v[48:63], v[216:219], v[152:155], v[48:63]\n\t"
        "v_exp_f32 v72, v72\n\t"
        "v_exp_f32 v73, v73\n\t"
        "v_exp_f32 v74, v74\n\t"
        "v_exp_f32 v75, v75\n\t"
        "v_cvt_pk_bf16_f32 v146, v68, v69\n\t"
        "v_cvt_pk_bf16_f32 v147, v70, v71\n\t"
        "v_mfma_f32_32x32x16_bf16 v[48:63], v[220:223], v[156:159], v[48:63]\n\t"
        "v_exp_f32 v76, v76\n\t"
        "v_exp_f32 v77, v77\n\t"
        "v_exp_f32 v78, v78\n\t"
        "v_exp_f32 v79, v79\n\t"
        "v_cvt_pk_bf16_f32 v148, v72, v73\n\t"
        "v_cvt_pk_bf16_f32 v149, v74, v75\n\t"
        "v_cvt_pk_bf16_f32 v150, v76, v77\n\t"
        "v_cvt_pk_bf16_f32 v151, v78, v79\n\t"
        "v_mfma_f32_32x32x16_bf16 v[64:79], v[160:163], v[128:131], v[96:111]\n\t"
        "buffer_load_dwordx4 v[208:211], v233, %[rs], %[sb] offen sc1\n\t"
        "buffer_load_dwordx4 v[212:215], v233, %[rs], %[sb] offen offset:1024 sc1\n\t"
        "buffer_load_dwordx4 v[216:219], v234, %[rs], %[sb] offen sc1\n\t"
        "buffer_load_dwordx4 v[220:223], v235, %[rs], %[sb] offen sc1\n\t"
        "v_exp_f32 v80, v80\n\t"
        "v_exp_f32 v81, v81\n\t"
        "v_exp_f32 v82, v82\n\t"
        "v_exp_f32 v83, v83\n\t"
        "v_mfma_f32_32x32x16_bf16 v[64:79], v[164:167], v[132:135], v[64:79]\n\t"
        "v_exp_f32 v84, v84\n\t"
        "v_exp_f32 v85, v85\n\t"
        "v_exp_f32 v86, v86\n\t"
        "v_exp_f32 v87, v87\n\t"
        "v_cvt_pk_bf16_f32 v152, v80, v81\n\t"
        "v_cvt_pk_bf16_f32 v153, v82, v83\n\t"
        "v_mfma_f32_32x32x16_bf16 v[0:15], v[168:171], v[144:147], v[0:15]\n\t"
        "v_exp_f32 v88, v88\n\t"
        "v_exp_f32 v89, v89\n\t"
        "v_exp_f32 v90, v90\n\t"
        "v_exp_f32 v91, v91\n\t"
        "v_cvt_pk_bf16_f32 v154, v84, v85\n\t"
        "v_cvt_pk_bf16_f32 v155, v86, v87\n\t"
        "v_mfma_f32_32x32x16_bf16 v[0:15], v[172:175], v[148:151], v[0:15]\n\t"
        "v_exp_f32 v92, v92\n\t"
        "v_exp_f32 v93, v93\n\t"
        "v_exp_f32 v94, v94\n\t"
        "v_exp_f32 v95, v95\n\t"
        "v_cvt_pk_bf16_f32 v156, v88, v89\n\t"
        "v_cvt_pk_bf16_f32 v157, v90, v91\n\t"
        "v_cvt_pk_bf16_f32 v158, v92, v93\n\t"
        "v_cvt_pk_bf16_f32 v159, v94, v95\n\t"
        "v_mfma_f32_32x32x16_bf16 v[80:95], v[160:163], v[136:139], v[96:111]\n\t"
        "ds_read_b32 v239, v238\n\t"
        "v_exp_f32 v64, v64\n\t"
        "v_exp_f32 v65, v65\n\t"
        "v_exp_f32 v66, v66\n\t"
        "v_exp_f32 v67, v67\n\t"
        "v_mfma_f32_32x32x16_bf16 v[80:95], v[164:167], v[140:143], v[80:95]\n\t"
        "v_exp_f32 v68, v68\n\t"
        "v_exp_f32 v69, v69\n\t"
        "v_exp_f32 v70, v70\n\t"
        "v_exp_f32 v71, v71\n\t"
        "v_cvt_pk_bf16_f32 v144, v64, v65\n\t"
        "v_cvt_pk_bf16_f32 v145, v66, v67\n\t"
        "v_mfma_f32_32x32x16_bf16 v[16:31], v[168:171], v[152:155], v[16:31]\n\t"
        "v_exp_f32 v72, v72\n\t"
        "v_exp_f32 v73, v73\n\t"
        "v_exp_f32 v74, v74\n\t"
        "v_exp_f32 v75, v75\n\t"
        "v_cvt_pk_bf16_f32 v146, v68, v69\n\t"
        "v_cvt_pk_bf16_f32 v147, v70, v71\n\t"
        "v_mfma_f32_32x32x16_bf16 v[16:31], v[172:175], v[156:159], v[16:31]\n\t"
        "v_exp_f32 v76, v76\n\t"
        "v_exp_f32 v77, v77\n\t"
        "v_exp_f32 v78, v78\n\t"
        "v_exp_f32 v79, v79\n\t"
        "v_cvt_pk_bf16_f32 v148, v72, v73\n\t"
        "v_cvt_pk_bf16_f32 v149, v74, v75\n\t"
        "v_cvt_pk_bf16_f32 v150, v76, v77\n\t"
        "v_cvt_pk_bf16_f32 v151, v78, v79\n\t"
        "s_waitcnt vmcnt(8)\n\t"
        "v_mfma_f32_32x32x16_bf16 v[64:79], v[176:179], v[112:115], v[96:111]\n\t"
        "v_exp_f32 v80, v80\n\t"
        "v_exp_f32 v81, v81\n\t"
        "v_exp_f32 v82, v82\n\t"
        "v_exp_f32 v83, v83\n\t"
        "v_mfma_f32_32x32x16_bf16 v[64:79], v[180:183], v[116:119], v[64:79]\n\t"
        "v_exp_f32 v84, v84\n\t"
        "v_exp_f32 v85, v85\n\t"
        "v_exp_f32 v86, v86\n\t"
        "v_exp_f32 v87, v87\n\t"
        "v_cvt_pk_bf16_f32 v152, v80, v81\n\t"
        "v_cvt_pk_bf16_f32 v153, v82, v83\n\t"
        "v_mfma_f32_32x32x16_bf16 v[32:47], v[168:171], v[144:147], v[32:47]\n\t"
        "v_exp_f32 v88, v88\n\t"
        "v_exp_f32 v89, v89\n\t"
        "v_exp_f32 v90, v90\n\t"
        "v_exp_f32 v91, v91\n\t"
        "v_cvt_pk_bf16_f32 v154, v84, v85\n\t"
        "v_cvt_pk_bf16_f32 v155, v86, v87\n\t"
        "v_mfma_f32_32x32x16_bf16 v[32:47], v[172:175], v[148:151], v[32:47]\n\t"
        "v_exp_f32 v92, v92\n\t"
        "v_exp_f32 v93, v93\n\t"
        "v_exp_f32 v94, v94\n\t"
        "v_exp_f32 v95, v95\n\t"
        "v_cvt_pk_bf16_f32 v156, v88, v89\n\t"
        "v_cvt_pk_bf16_f32 v157, v90, v91\n\t"
        "v_cvt_pk_bf16_f32 v158, v92, v93\n\t"
        "v_cvt_pk_bf16_f32 v159, v94, v95\n\t"
        "v_mfma_f32_32x32x16_bf16 v[80:95], v[176:179], v[120:123], v[96:111]\n\t"
        "CHK%=:\n\t"
        "s_waitcnt lgkmcnt(0)\n\t"
        "v_readfirstlane_b32 %[sb], v239\n\t"
        "ds_read_b32 v239, v238\n\t"
        "s_cmp_eq_u32 %[sb], -1\n\t"
        "s_cbranch_scc1 CHK%=\n\t"
        "v_add_u32 v238, 4, v238\n\t"
        "v_exp_f32 v64, v64\n\t"
        "v_exp_f32 v65, v65\n\t"
        "v_exp_f32 v66, v66\n\t"
        "v_exp_f32 v67, v67\n\t"
        "v_mfma_f32_32x32x16_bf16 v[80:95], v[180:183], v[124:127], v[80:95]\n\t"
        "v_exp_f32 v68, v68\n\t"
        "v_exp_f32 v69, v69\n\t"
        "v_exp_f32 v70, v70\n\t"
        "v_exp_f32 v71, v71\n\t"
        "v_cvt_pk_bf16_f32 v144, v64, v65\n\t"
        "v_cvt_pk_bf16_f32 v145, v66, v67\n\t"
        "v_mfma_f32_32x32x16_bf16 v[48:63], v[168:171], v[152:155], v[48:63]\n\t"
        "v_exp_f32 v72, v72\n\t"
        "v_exp_f32 v73, v73\n\t"
        "v_exp_f32 v74, v74\n\t"
        "v_exp_f32 v75, v75\n\t"
        "v_cvt_pk_bf16_f32 v146, v68, v69\n\t"
        "v_cvt_pk_bf16_f32 v147, v70, v71\n\t"
        "v_mfma_f32_32x32x16_bf16 v[48:63], v[172:175], v[156:159], v[48:63]\n\t"
        "v_exp_f32 v76, v76\n\t"
        "v_exp_f32 v77, v77\n\t"
        "v_exp_f32 v78, v78\n\t"
        "v_exp_f32 v79, v79\n\t"
        "v_cvt_pk_bf16_f32 v148, v72, v73\n\t"
        "v_cvt_pk_bf16_f32 v149, v74, v75\n\t"
        "v_cvt_pk_bf16_f32 v150, v76, v77\n\t"
        "v_cvt_pk_bf16_f32 v151, v78, v79\n\t"
        "v_mfma_f32_32x32x16_bf16 v[64:79], v[176:179], v[128:131], v[96:111]\n\t"
        "buffer_load_dwordx4 v[160:163], v224, %[rs], %[sb] offen sc1\n\t"
        "buffer_load_dwordx4 v[164:167], v224, %[rs], %[sb] offen offset:1024 sc1\n\t"
        "buffer_load_dwordx4 v[168:171], v225, %[rs], %[sb] offen sc1\n\t"
        "buffer_load_dwordx4 v[172:175], v226, %[rs], %[sb] offen sc1\n\t"
        "v_exp_f32 v80, v80\n\t"
        "v_exp_f32 v81, v81\n\t"
        "v_exp_f32 v82, v82\n\t"
        "v_exp_f32 v83, v83\n\t"
        "v_mfma_f32_32x32x16_bf16 v[64:79], v[180:183], v[132:135], v[64:79]\n\t"
        "v_exp_f32 v84, v84\n\t"
        "v_exp_f32 v85, v85\n\t"
        "v_exp_f32 v86, v86\n\t"
        "v_exp_f32 v87, v87\n\t"
        "v_cvt_pk_bf16_f32 v152, v80, v81\n\t"
        "v_cvt_pk_bf16_f32 v153, v82, v83\n\t"
        "v_mfma_f32_32x32x16_bf16 v[0:15], v[184:187], v[144:147], v[0:15]\n\t"
        "v_exp_f32 v88, v88\n\t"
        "v_exp_f32 v89, v89\n\t"
        "v_exp_f32 v90, v90\n\t"
        "v_exp_f32 v91, v91\n\t"
        "v_cvt_pk_bf16_f32 v154, v84, v85\n\t"
        "v_cvt_pk_bf16_f32 v155, v86, v87\n\t"
        "v_mfma_f32_32x32x16_bf16 v[0:15], v[188:191], v[148:151], v[0:15]\n\t"
        "v_exp_f32 v92, v92\n\t"
        "v_exp_f32 v93, v93\n\t"
        "v_exp_f32 v94, v94\n\t"
        "v_exp_f32 v95, v95\n\t"
        "v_cvt_pk_bf16_f32 v156, v88, v89\n\t"
        "v_cvt_pk_bf16_f32 v157, v90, v91\n\t"
        "v_cvt_pk_bf16_f32 v158, v92, v93\n\t"
        "v_cvt_pk_bf16_f32 v159, v94, v95\n\t"
        "v_mfma_f32_32x32x16_bf16 v[80:95], v[176:179], v[136:139], v[96:111]\n\t"
        "v_exp_f32 v64, v64\n\t"
        "v_exp_f32 v65, v65\n\t"
        "v_exp_f32 v66, v66\n\t"
        "v_exp_f32 v67, v67\n\t"
        "v_mfma_f32_32x32x16_bf16 v[80:95], v[180:183], v[140:143], v[80:95]\n\t"
        "v_exp_f32 v68, v68\n\t"
        "v_exp_f32 v69, v69\n\t"
        "v_exp_f32 v70, v70\n\t"
        "v_exp_f32 v71, v71\n\t"
        "v_cvt_pk_bf16_f32 v144, v64, v65\n\t"
        "v_cvt_pk_bf16_f32 v145, v66, v67\n\t"
        "v_mfma_f32_32x32x16_bf16 v[16:31], v[184:187], v[152:155], v[16:31]\n\t"
        "v_exp_f32 v72, v72\n\t"
        "v_exp_f32 v73, v73\n\t"
        "v_exp_f32 v74, v74\n\t"
        "v_exp_f32 v75, v75\n\t"
        "v_cvt_pk_bf16_f32 v146, v68, v69\n\t"
        "v_cvt_pk_bf16_f32 v147, v70, v71\n\t"
        "v_mfma_f32_32x32x16_bf16 v[16:31], v[188:191], v[156:159], v[16:31]\n\t"
        "v_exp_f32 v76, v76\n\t"
        "v_exp_f32 v77, v77\n\t"
        "v_exp_f32 v78, v78\n\t"
        "v_exp_f32 v79, v79\n\t"
        "v_cvt_pk_bf16_f32 v148, v72, v73\n\t"
        "v_cvt_pk_bf16_f32 v149, v74, v75\n\t"
        "v_cvt_pk_bf16_f32 v150, v76, v77\n\t"
        "v_cvt_pk_bf16_f32 v151, v78, v79\n\t"
        "s_waitcnt vmcnt(8)\n\t"
        "v_mfma_f32_32x32x16_bf16 v[64:79], v[192:195], v[112:115], v[96:111]\n\t"
        "v_exp_f32 v80, v80\n\t"
        "v_exp_f32 v81, v81\n\t"
        "v_exp_f32 v82, v82\n\t"
        "v_exp_f32 v83, v83\n\t"
        "v_mfma_f32_32x32x16_bf16 v[64:79], v[196:199], v[116:119], v[64:79]\n\t"
        "v_exp_f32 v84, v84\n\t"
        "v_exp_f32 v85, v85\n\t"
        "v_exp_f32 v86, v86\n\t"
        "v_exp_f32 v87, v87\n\t"
        "v_cvt_pk_bf16_f32 v152, v80, v81\n\t"
        "v_cvt_pk_bf16_f32 v153, v82, v83\n\t"
        "v_mfma_f32_32x32x16_bf16 v[32:47], v[184:187], v[144:147], v[32:47]\n\t"
        "v_exp_f32 v88, v88\n\t"
        "v_exp_f32 v89, v89\n\t"
        "v_exp_f32 v90, v90\n\t"
        "v_exp_f32 v91, v91\n\t"
        "v_cvt_pk_bf16_f32 v154, v84, v85\n\t"
        "v_cvt_pk_bf16_f32 v155, v86, v87\n\t"
        "v_mfma_f32_32x32x16_bf16 v[32:47], v[188:191], v[148:151], v[32:47]\n\t"
        "v_exp_f32 v92, v92\n\t"
        "v_exp_f32 v93, v93\n\t"
        "v_exp_f32 v94, v94\n\t"
        "v_exp_f32 v95, v95\n\t"
        "v_cvt_pk_bf16_f32 v156, v88, v89\n\t"
        "v_cvt_pk_bf16_f32 v157, v90, v91\n\t"
        "v_cvt_pk_bf16_f32 v158, v92, v93\n\t"
        "v_cvt_pk_bf16_f32 v159, v94, v95\n\t"
        "v_mfma_f32_32x32x16_bf16 v[80:95], v[192:195], v[120:123], v[96:111]\n\t"
        "v_exp_f32 v64, v64\n\t"
        "v_exp_f32 v65, v65\n\t"
        "v_exp_f32 v66, v66\n\t"
        "v_exp_f32 v67, v67\n\t"
        "v_mfma_f32_32x32x16_bf16 v[80:95], v[196:199], v[124:127], v[80:95]\n\t"
        "v_exp_f32 v68, v68\n\t"
        "v_exp_f32 v69, v69\n\t"
        "v_exp_f32 v70, v70\n\t"
        "v_exp_f32 v71, v71\n\t"
        "v_cvt_pk_bf16_f32 v144, v64, v65\n\t"
        "v_cvt_pk_bf16_f32 v145, v66, v67\n\t"
        "v_mfma_f32_32x32x16_bf16 v[48:63], v[184:187], v[152:155], v[48:63]\n\t"
        "v_exp_f32 v72, v72\n\t"
        "v_exp_f32 v73, v73\n\t"
        "v_exp_f32 v74, v74\n\t"
        "v_exp_f32 v75, v75\n\t"
        "v_cvt_pk_bf16_f32 v146, v68, v69\n\t"
        "v_cvt_pk_bf16_f32 v147, v70, v71\n\t"
        "v_mfma_f32_32x32x16_bf16 v[48:63], v[188:191], v[156:159], v[48:63]\n\t"
        "v_exp_f32 v76, v76\n\t"
        "v_exp_f32 v77, v77\n\t"
        "v_exp_f32 v78, v78\n\t"
        "v_exp_f32 v79, v79\n\t"
        "v_cvt_pk_bf16_f32 v148, v72, v73\n\t"
        "v_cvt_pk_bf16_f32 v149, v74, v75\n\t"
        "v_cvt_pk_bf16_f32 v150, v76, v77\n\t"
        "v_cvt_pk_bf16_f32 v151, v78, v79\n\t"
        "v_mfma_f32_32x32x16_bf16 v[64:79], v[192:195], v[128:131], v[96:111]\n\t"
        "buffer_load_dwordx4 v[176:179], v227, %[rs], %[sb] offen sc1\n\t"
        "buffer_load_dwordx4 v[180:183], v227, %[rs], %[sb] offen offset:1024 sc1\n\t"
        "buffer_load_dwordx4 v[184:187], v228, %[rs], %[sb] offen sc1\n\t"
        "buffer_load_dwordx4 v[188:191], v229, %[rs], %[sb] offen sc1\n\t"
        "v_exp_f32 v80, v80\n\t"
        "v_exp_f32 v81, v81\n\t"
        "v_exp_f32 v82, v82\n\t"
        "v_exp_f32 v83, v83\n\t"
        "v_mfma_f32_32x32x16_bf16 v[64:79], v[196:199], v[132:135], v[64:79]\n\t"
        "v_exp_f32 v84, v84\n\t"
        "v_exp_f32 v85, v85\n\t"
        "v_exp_f32 v86, v86\n\t"
        "v_exp_f32 v87, v87\n\t"
        "v_cvt_pk_bf16_f32 v152, v80, v81\n\t"
        "v_cvt_pk_bf16_f32 v153, v82, v83\n\t"
        "v_mfma_f32_32x32x16_bf16 v[0:15], v[200:203], v[144:147], v[0:15]\n\t"
        "v_exp_f32 v88, v88\n\t"
        "v_exp_f32 v89, v89\n\t"
        "v_exp_f32 v90, v90\n\t"
        "v_exp_f32 v91, v91\n\t"
        "v_cvt_pk_bf16_f32 v154, v84, v85\n\t"
        "v_cvt_pk_bf16_f32 v155, v86, v87\n\t"
        "v_mfma_f32_32x32x16_bf16 v[0:15], v[204:207], v[148:151], v[0:15]\n\t"
        "v_exp_f32 v92, v92\n\t"
        "v_exp_f32 v93, v93\n\t"
        "v_exp_f32 v94, v94\n\t"
        "v_exp_f32 v95, v95\n\t"
        "v_cvt_pk_bf16_f32 v156, v88, v89\n\t"
        "v_cvt_pk_bf16_f32 v157, v90, v91\n\t"
        "v_cvt_pk_bf16_f32 v158, v92, v93\n\t"
        "v_cvt_pk_bf16_f32 v159, v94, v95\n\t"
        "v_mfma_f32_32x32x16_bf16 v[80:95], v[192:195], v[136:139], v[96:111]\n\t"
        "v_exp_f32 v64, v64\n\t"
        "v_exp_f32 v65, v65\n\t"
        "v_exp_f32 v66, v66\n\t"
        "v_exp_f32 v67, v67\n\t"
        "v_mfma_f32_32x32x16_bf16 v[80:95], v[196:199], v[140:143], v[80:95]\n\t"
        "v_exp_f32 v68, v68\n\t"
        "v_exp_f32 v69, v69\n\t"
        "v_exp_f32 v70, v70\n\t"
        "v_exp_f32 v71, v71\n\t"
        "v_cvt_pk_bf16_f32 v144, v64, v65\n\t"
        "v_cvt_pk_bf16_f32 v145, v66, v67\n\t"
        "v_mfma_f32_32x32x16_bf16 v[16:31], v[200:203], v[152:155], v[16:31]\n\t"
        "v_exp_f32 v72, v72\n\t"
        "v_exp_f32 v73, v73\n\t"
        "v_exp_f32 v74, v74\n\t"
        "v_exp_f32 v75, v75\n\t"
        "v_cvt_pk_bf16_f32 v146, v68, v69\n\t"
        "v_cvt_pk_bf16_f32 v147, v70, v71\n\t"
        "v_mfma_f32_32x32x16_bf16 v[16:31], v[204:207], v[156:159], v[16:31]\n\t"
        "v_exp_f32 v76, v76\n\t"
        "v_exp_f32 v77, v77\n\t"
        "v_exp_f32 v78, v78\n\t"
        "v_exp_f32 v79, v79\n\t"
        "v_cvt_pk_bf16_f32 v148, v72, v73\n\t"
        "v_cvt_pk_bf16_f32 v149, v74, v75\n\t"
        "v_cvt_pk_bf16_f32 v150, v76, v77\n\t"
        "v_cvt_pk_bf16_f32 v151, v78, v79\n\t"
        "s_waitcnt vmcnt(8)\n\t"
        "v_mfma_f32_32x32x16_bf16 v[64:79], v[208:211], v[112:115], v[96:111]\n\t"
        "v_exp_f32 v80, v80\n\t"
        "v_exp_f32 v81, v81\n\t"
        "v_exp_f32 v82, v82\n\t"
        "v_exp_f32 v83, v83\n\t"
        "v_mfma_f32_32x32x16_bf16 v[64:79], v[212:215], v[116:119], v[64:79]\n\t"
        "v_exp_f32 v84, v84\n\t"
        "v_exp_f32 v85, v85\n\t"
        "v_exp_f32 v86, v86\n\t"
        "v_exp_f32 v87, v87\n\t"
        "v_cvt_pk_bf16_f32 v152, v80, v81\n\t"
        "v_cvt_pk_bf16_f32 v153, v82, v83\n\t"
        "v_mfma_f32_32x32x16_bf16 v[32:47], v[200:203], v[144:147], v[32:47]\n\t"
        "v_exp_f32 v88, v88\n\t"
        "v_exp_f32 v89, v89\n\t"
        "v_exp_f32 v90, v90\n\t"
        "v_exp_f32 v91, v91\n\t"
        "v_cvt_pk_bf16_f32 v154, v84, v85\n\t"
        "v_cvt_pk_bf16_f32 v155, v86, v87\n\t"
        "v_mfma_f32_32x32x16_bf16 v[32:47], v[204:207], v[148:151], v[32:47]\n\t"
        "v_exp_f32 v92, v92\n\t"
        "v_exp_f32 v93, v93\n\t"
        "v_exp_f32 v94, v94\n\t"
        "v_exp_f32 v95, v95\n\t"
        "v_cvt_pk_bf16_f32 v156, v88, v89\n\t"
        "v_cvt_pk_bf16_f32 v157, v90, v91\n\t"
        "v_cvt_pk_bf16_f32 v158, v92, v93\n\t"
        "v_cvt_pk_bf16_f32 v159, v94, v95\n\t"
        "v_mfma_f32_32x32x16_bf16 v[80:95], v[208:211], v[120:123], v[96:111]\n\t"
        "v_exp_f32 v64, v64\n\t"
        "v_exp_f32 v65, v65\n\t"
        "v_exp_f32 v66, v66\n\t"
        "v_exp_f32 v67, v67\n\t"
        "v_mfma_f32_32x32x16_bf16 v[80:95], v[212:215], v[124:127], v[80:95]\n\t"
        "v_exp_f32 v68, v68\n\t"
        "v_exp_f32 v69, v69\n\t"
        "v_exp_f32 v70, v70\n\t"
        "v_exp_f32 v71, v71\n\t"
        "v_cvt_pk_bf16_f32 v144, v64, v65\n\t"
        "v_cvt_pk_bf16_f32 v145, v66, v67\n\t"
        "v_mfma_f32_32x32x16_bf16 v[48:63], v[200:203], v[152:155], v[48:63]\n\t"
        "v_exp_f32 v72, v72\n\t"
        "v_exp_f32 v73, v73\n\t"
        "v_exp_f32 v74, v74\n\t"
        "v_exp_f32 v75, v75\n\t"
        "v_cvt_pk_bf16_f32 v146, v68, v69\n\t"
        "v_cvt_pk_bf16_f32 v147, v70, v71\n\t"
        "v_mfma_f32_32x32x16_bf16 v[48:63], v[204:207], v[156:159], v[48:63]\n\t"
        "v_exp_f32 v76, v76\n\t"
        "v_exp_f32 v77, v77\n\t"
        "v_exp_f32 v78, v78\n\t"
        "v_exp_f32 v79, v79\n\t"
        "v_cvt_pk_bf16_f32 v148, v72, v73\n\t"
        "v_cvt_pk_bf16_f32 v149, v74, v75\n\t"
        "v_cvt_pk_bf16_f32 v150, v76, v77\n\t"
        "v_cvt_pk_bf16_f32 v151, v78, v79\n\t"
        "v_mfma_f32_32x32x16_bf16 v[64:79], v[208:211], v[128:131], v[96:111]\n\t"
        "buffer_load_dwordx4 v[192:195], v230, %[rs], %[sb] offen sc1\n\t"
        "buffer_load_dwordx4 v[196:199], v230, %[rs], %[sb] offen offset:1024 sc1\n\t"
        "buffer_load_dwordx4 v[200:203], v231, %[rs], %[sb] offen sc1\n\t"
        "buffer_load_dwordx4 v[204:207], v232, %[rs], %[sb] offen sc1\n\t"
        "s_sub_u32 %[cnt], %[cnt], 1\n\t"
        "s_cmp_eq_u32 %[cnt], 0\n\t"
        "v_exp_f32 v80, v80\n\t"
        "v_exp_f32 v81, v81\n\t"
        "v_exp_f32 v82, v82\n\t"
        "v_exp_f32 v83, v83\n\t"
        "v_mfma_f32_32x32x16_bf16 v[64:79], v[212:215], v[132:135], v[64:79]\n\t"
        "v_exp_f32 v84, v84\n\t"
        "v_exp_f32 v85, v85\n\t"
        "v_exp_f32 v86, v86\n\t"
        "v_exp_f32 v87, v87\n\t"
        "v_cvt_pk_bf16_f32 v152, v80, v81\n\t"
        "v_cvt_pk_bf16_f32 v153, v82, v83\n\t"
        "v_mfma_f32_32x32x16_bf16 v[0:15], v[216:219], v[144:147], v[0:15]\n\t"
        "v_exp_f32 v88, v88\n\t"
        "v_exp_f32 v89, v89\n\t"
        "v_exp_f32 v90, v90\n\t"
        "v_exp_f32 v91, v91\n\t"
        "v_cvt_pk_bf16_f32 v154, v84, v85\n\t"
        "v_cvt_pk_bf16_f32 v155, v86, v87\n\t"
        "v_mfma_f32_32x32x16_bf16 v[0:15], v[220:223], v[148:151], v[0:15]\n\t"
        "v_exp_f32 v92, v92\n\t"
        "v_exp_f32 v93, v93\n\t"
        "v_exp_f32 v94, v94\n\t"
        "v_exp_f32 v95, v95\n\t"
        "v_cvt_pk_bf16_f32 v156, v88, v89\n\t"
        "v_cvt_pk_bf16_f32 v157, v90, v91\n\t"
        "v_cvt_pk_bf16_f32 v158, v92, v93\n\t"
        "v_cvt_pk_bf16_f32 v159, v94, v95\n\t"
        "s_cbranch_scc1 TAIL%=\n\t"
        "v_mfma_f32_32x32x16_bf16 v[80:95], v[208:211], v[136:139], v[96:111]\n\t"
        "v_exp_f32 v64, v64\n\t"
        "v_exp_f32 v65, v65\n\t"
        "v_exp_f32 v66, v66\n\t"
        "v_exp_f32 v67, v67\n\t"
        "v_mfma_f32_32x32x16_bf16 v[80:95], v[212:215], v[140:143], v[80:95]\n\t"
        "v_exp_f32 v68, v68\n\t"
        "v_exp_f32 v69, v69\n\t"
        "v_exp_f32 v70, v70\n\t"
        "v_exp_f32 v71, v71\n\t"
        "v_cvt_pk_bf16_f32 v144, v64, v65\n\t"
        "v_cvt_pk_bf16_f32 v145, v66, v67\n\t"
        "v_mfma_f32_32x32x16_bf16 v[16:31], v[216:219], v[152:155], v[16:31]\n\t"
        "v_exp_f32 v72, v72\n\t"
        "v_exp_f32 v73, v73\n\t"
        "v_exp_f32 v74, v74\n\t"
        "v_exp_f32 v75, v75\n\t"
        "v_cvt_pk_bf16_f32 v146, v68, v69\n\t"
        "v_cvt_pk_bf16_f32 v147, v70, v71\n\t"
        "v_mfma_f32_32x32x16_bf16 v[16:31], v[220:223], v[156:159], v[16:31]\n\t"
        "v_exp_f32 v76, v76\n\t"
        "v_exp_f32 v77, v77\n\t"
        "v_exp_f32 v78, v78\n\t"
        "v_exp_f32 v79, v79\n\t"
        "v_cvt_pk_bf16_f32 v148, v72, v73\n\t"
        "v_cvt_pk_bf16_f32 v149, v74, v75\n\t"
        "v_cvt_pk_bf16_f32 v150, v76, v77\n\t"
        "v_cvt_pk_bf16_f32 v151, v78, v79\n\t"
        "s_waitcnt vmcnt(8)\n\t"
        "v_mfma_f32_32x32x16_bf16 v[64:79], v[160:163], v[112:115], v[96:111]\n\t"
        "v_exp_f32 v80, v80\n\t"
        "v_exp_f32 v81, v81\n\t"
        "v_exp_f32 v82, v82\n\t"
        "v_exp_f32 v83, v83\n\t"
        "v_mfma_f32_32x32x16_bf16 v[64:79], v[164:167], v[116:119], v[64:79]\n\t"
        "v_exp_f32 v84, v84\n\t"
        "v_exp_f32 v85, v85\n\t"
        "v_exp_f32 v86, v86\n\t"
        "v_exp_f32 v87, v87\n\t"
        "v_cvt_pk_bf16_f32 v152, v80, v81\n\t"
        "v_cvt_pk_bf16_f32 v153, v82, v83\n\t"
        "v_mfma_f32_32x32x16_bf16 v[32:47], v[216:219], v[144:147], v[32:47]\n\t"
        "v_exp_f32 v88, v88\n\t"
        "v_exp_f32 v89, v89\n\t"
        "v_exp_f32 v90, v90\n\t"
        "v_exp_f32 v91, v91\n\t"
        "v_cvt_pk_bf16_f32 v154, v84, v85\n\t"
        "v_cvt_pk_bf16_f32 v155, v86, v87\n\t"
        "v_mfma_f32_32x32x16_bf16 v[32:47], v[220:223], v[148:151], v[32:47]\n\t"
        "v_exp_f32 v92, v92\n\t"
        "v_exp_f32 v93, v93\n\t"
        "v_exp_f32 v94, v94\n\t"
        "v_exp_f32 v95, v95\n\t"
        "v_cvt_pk_bf16_f32 v156, v88, v89\n\t"
        "v_cvt_pk_bf16_f32 v157, v90, v91\n\t"
        "v_cvt_pk_bf16_f32 v158, v92, v93\n\t"
        "v_cvt_pk_bf16_f32 v159, v94, v95\n\t"
        "s_branch LOOP%=\n\t"
        "TAIL%=:\n\t"
        "v_mfma_f32_32x32x16_bf16 v[80:95], v[208:211], v[136:139], v[96:111]\n\t"
        "v_exp_f32 v64, v64\n\t"
        "v_exp_f32 v65, v65\n\t"
        "v_exp_f32 v66, v66\n\t"
        "v_exp_f32 v67, v67\n\t"
        "v_mfma_f32_32x32x16_bf16 v[80:95], v[212:215], v[140:143], v[80:95]\n\t"
        "ds_write_b128 v240, v[0:3] offset:0\n\t"
        "ds_write_b128 v240, v[4:7] offset:1024\n\t"
        "ds_write_b128 v240, v[8:11] offset:2048\n\t"
        "ds_write_b128 v240, v[12:15] offset:3072\n\t"
        "v_exp_f32 v68, v68\n\t"
        "v_exp_f32 v69, v69\n\t"
        "v_exp_f32 v70, v70\n\t"
        "v_exp_f32 v71, v71\n\t"
        "v_cvt_pk_bf16_f32 v144, v64, v65\n\t"
        "v_cvt_pk_bf16_f32 v145, v66, v67\n\t"
        "v_mfma_f32_32x32x16_bf16 v[16:31], v[216:219], v[152:155], v[16:31]\n\t"
        "v_exp_f32 v72, v72\n\t"
        "v_exp_f32 v73, v73\n\t"
        "v_exp_f32 v74, v74\n\t"
        "v_exp_f32 v75, v75\n\t"
        "v_cvt_pk_bf16_f32 v146, v68, v69\n\t"
        "v_cvt_pk_bf16_f32 v147, v70, v71\n\t"
        "v_mfma_f32_32x32x16_bf16 v[16:31], v[220:223], v[156:159], v[16:31]\n\t"
        "v_exp_f32 v76, v76\n\t"
        "v_exp_f32 v77, v77\n\t"
        "v_exp_f32 v78, v78\n\t"
        "v_exp_f32 v79, v79\n\t"
        "v_cvt_pk_bf16_f32 v148, v72, v73\n\t"
        "v_cvt_pk_bf16_f32 v149, v74, v75\n\t"
        "v_cvt_pk_bf16_f32 v150, v76, v77\n\t"
        "v_cvt_pk_bf16_f32 v151, v78, v79\n\t"
        "v_exp_f32 v80, v80\n\t"
        "v_exp_f32 v81, v81\n\t"
        "v_exp_f32 v82, v82\n\t"
        "v_exp_f32 v83, v83\n\t"
        "ds_write_b128 v240, v[16:19] offset:4096\n\t"
        "ds_write_b128 v240, v[20:23] offset:5120\n\t"
        "ds_write_b128 v240, v[24:27] offset:6144\n\t"
        "ds_write_b128 v240, v[28:31] offset:7168\n\t"
        "v_exp_f32 v84, v84\n\t"
        "v_exp_f32 v85, v85\n\t"
        "v_exp_f32 v86, v86\n\t"
        "v_exp_f32 v87, v87\n\t"
        "v_cvt_pk_bf16_f32 v152, v80, v81\n\t"
        "v_cvt_pk_bf16_f32 v153, v82, v83\n\t"
        "v_mfma_f32_32x32x16_bf16 v[32:47], v[216:219], v[144:147], v[32:47]\n\t"
        "v_exp_f32 v88, v88\n\t"
        "v_exp_f32 v89, v89\n\t"
        "v_exp_f32 v90, v90\n\t"
        "v_exp_f32 v91, v91\n\t"
        "v_cvt_pk_bf16_f32 v154, v84, v85\n\t"
        "v_cvt_pk_bf16_f32 v155, v86, v87\n\t"
        "v_mfma_f32_32x32x16_bf16 v[32:47], v[220:223], v[148:151], v[32:47]\n\t"
        "v_exp_f32 v92, v92\n\t"
        "v_exp_f32 v93, v93\n\t"
        "v_exp_f32 v94, v94\n\t"
        "v_exp_f32 v95, v95\n\t"
        "v_cvt_pk_bf16_f32 v156, v88, v89\n\t"
        "v_cvt_pk_bf16_f32 v157, v90, v91\n\t"
        "v_cvt_pk_bf16_f32 v158, v92, v93\n\t"
        "v_cvt_pk_bf16_f32 v159, v94, v95\n\t"
        "v_mfma_f32_32x32x16_bf16 v[48:63], v[216:219], v[152:155], v[48:63]\n\t"
        "v_mfma_f32_32x32x16_bf16 v[48:63], v[220:223], v[156:159], v[48:63]\n\t"
        "s_nop 3\n\t"
        "ds_write_b128 v240, v[32:35] offset:8192\n\t"
        "ds_write_b128 v240, v[36:39] offset:9216\n\t"
        "ds_write_b128 v240, v[40:43] offset:10240\n\t"
        "ds_write_b128 v240, v[44:47] offset:11264\n\t"
        "s_waitcnt vmcnt(0) lgkmcnt(0)\n\t"
        "s_barrier\n\t"
        "s_nop 7\n\t"
        : "={v[48:63]}"(acc), [sb] "+s"(sb), [cnt] "+s"(cnt), "+{v236}"(q2a), "+{v237}"(kba), "+{v238}"(ordn), "={v[136:139]}"(qo0), "={v[140:143]}"(qo1)
        : "{v224}"(vok + 0 * TS), "{v225}"(vov + 0 * TS), "{v226}"(vov + 2 * VROW + 0 * TS), "{v227}"(vok + 1 * TS), "{v228}"(vov + 1 * TS), "{v229}"(vov + 2 * VROW + 1 * TS), "{v230}"(vok + 2 * TS), "{v231}"(vov + 2 * TS), "{v232}"(vov + 2 * VROW + 2 * TS), "{v233}"(vok + 3 * TS), "{v234}"(vov + 3 * TS), "{v235}"(vov + 2 * VROW + 3 * TS),
          "{v240}"(puta), "{v241}"(qxa[0]), "{v242}"(qxa[1]), "{v243}"(qxa[2]), "{v244}"(qxa[3]), [rs] "s"(rsKV)
        : "v0", "v1", "v2", "v3", "v4", "v5", "v6", "v7", "v8", "v9", "v10", "v11", "v12", "v13", "v14", "v15", "v16", "v17", "v18", "v19", "v20", "v21", "v22", "v23", "v24", "v25", "v26", "v27", "v28", "v29", "v30", "v31", "v32", "v33", "v34", "v35", "v36", "v37", "v38", "v39", "v40", "v41", "v42", "v43", "v44", "v45", "v46", "v47", "v64", "v65", "v66", "v67", "v68", "v69", "v70", "v71", "v72", "v73", "v74", "v75", "v76", "v77", "v78", "v79", "v80", "v81", "v82", "v83", "v84", "v85", "v86", "v87", "v88", "v89", "v90", "v91", "v92", "v93", "v94", "v95", "v96", "v97", "v98", "v99", "v100", "v101", "v102", "v103", "v104", "v105", "v106", "v107", "v108", "v109", "v110", "v111", "v112", "v113", "v114", "v115", "v116", "v117", "v118", "v119", "v120", "v121", "v122", "v123", "v124", "v125", "v126", "v127", "v128", "v129", "v130", "v131", "v132", "v133", "v134", "v135", "v144", "v145", "v146", "v147", "v148", "v149", "v150", "v151", "v152", "v153", "v154", "v155", "v156", "v157", "v158", "v159", "v160", "v161", "v162", "v163", "v164", "v165", "v166", "v167", "v168", "v169", "v170", "v171", "v172", "v173", "v174", "v175", "v176", "v177", "v178", "v179", "v180", "v181", "v182", "v183", "v184", "v185", "v186", "v187", "v188", "v189", "v190", "v191", "v192", "v193", "v194", "v195", "v196", "v197", "v198", "v199", "v200", "v201", "v202", "v203", "v204", "v205", "v206", "v207", "v208", "v209", "v210", "v211", "v212", "v213", "v214", "v215", "v216", "v217", "v218", "v219", "v220", "v221", "v222", "v223", "v239", "scc", "memory");
    }
    STAMP(7);
#pragma unroll
    for (int s = 0; s < 3; ++s) {
        LAS const char* sp = lds + L_CB + (((w - 1 - s) & 3) * 3 + s) * 4096 + lane * 16;
#pragma unroll
        for (int g = 0; g < 4; ++g) {
            const f32x4 v = *(LAS const f32x4*)(sp + g * 1024);
            acc[4 * g] += v[0]; acc[4 * g + 1] += v[1]; acc[4 * g + 2] += v[2]; acc[4 * g + 3] += v[3];
        }
    }
    float l = half_hi(acc[12]);
    if (__builtin_expect(__any(!(l >= L_MIN && l <= L_MAX)), 0)) {
        const f32x16 zero = {};
        float m = -__builtin_inff();
#pragma unroll 1
        for (int t = 0; t < 64; ++t) {
            f32x16 s = MFMA(asbf(ld16_wt(rsKV, vok, t * TS)), asbf(qo0), zero);
            s = MFMA(asbf(ld16_wt(rsKV, vok + 1024, t * TS)), asbf(qo1), s);
#pragma unroll
            for (int i = 0; i < 16; ++i) m = __builtin_fmaxf(m, s[i]);
        }
        m = __builtin_fmaxf(half_lo(m), half_hi(m));
        acc = zero;
#pragma unroll 1
        for (int t = 0; t < 64; ++t) {
            f32x16 s = MFMA(asbf(ld16_wt(rsKV, vok, t * TS)), asbf(qo0), zero);
            s = MFMA(asbf(ld16_wt(rsKV, vok + 1024, t * TS)), asbf(qo1), s);
#pragma unroll
            for (int i = 0; i < 16; ++i) s[i] = __builtin_amdgcn_exp2f(s[i] - m);
            acc = MFMA(asbf(ld16_wt(rsKV, vov, t * TS)), asbf(pack8(s, 0)), acc);
            acc = MFMA(asbf(ld16_wt(rsKV, vov + 2 * VROW, t * TS)), asbf(pack8(s, 1)), acc);
        }
        l = half_hi(acc[12]);
    }
    {
        const float rl = __builtin_amdgcn_rcpf(l);
#pragma unroll
        for (int i = 0; i < 16; ++i) acc[i] = (i >= 12 && h) ? 0.f : acc[i] * rl;
        const unsigned so = (kt * NH + hd) * 2048;
        u32x4 p0 = pack8(acc, 0), p1 = pack8(acc, 1);
#pragma unroll
        for (int i = 0; i < 4; ++i) { p0[i] = min(p0[i], POISON - 1); p1[i] = min(p1[i], POISON - 1); }
        st16_wt(p0, rsOI, lane * 16, so);
        st16_wt(p1, rsOI, lane * 16 + 1024, so);
        if (lane < 4) __hip_atomic_store((GAS u64*)(wsg + WS_F + F_O + (qt * 16 + lane * 4 + w) * 2048 + hd * 128), (u64)tag, RLX_AGENT);
    }
    STAMP(8);
    BAR();

    bf16x8 wf[32];
    {
        LAS const char* wb = lds + L_WO + (4 * h + q4) * 256 + ((w ^ q4) * 64) + g16 * 32 + p4 * 8;
#pragma unroll
        for (int k = 0; k < 32; ++k) {
            const int ro = ((k >> 1) * DH + (k & 1) * 16) * 256;
            wf[k] = cat(vtr(wb + ro), vtr(wb + ro + 2048 - (k == 31 ? h * 1024 : 0)));
        }
    }
    STAMP(10);
    BAR();
    STAMP(11);
    if (cg < 3 || w < 2) {
        f32x16 c = {};
        bf16x8 of[3][4];
#define RDO(i) do { _Pragma("unroll") for (int j = 0; j < 4; ++j) of[(i) % 3][j] = *(LAS const bf16x8*)(lds + L_CB + ((i) * 4 + j) * 1024 + lane * 16); } while (0)
        RDO(0); RDO(1); SBAR();
#pragma unroll
        for (int i = 0; i < 8; ++i) {
            if (i + 2 < 8) RDO(i + 2);
            SBAR();
#pragma unroll
            for (int j = 0; j < 4; ++j) c = MFMA(wf[i * 4 + j], of[i % 3][j], c);
            SBAR();
        }
#undef RDO
        STAMP(12);
        char* orow = out + (size_t)(q32 * 32 + r) * (DM * 2) + cg * 256 + w * 64 + h * 16;
#pragma unroll
        for (int g = 0; g < 4; g += 2) {
            const unsigned x0 = cvtpk(c[4 * g], c[4 * g + 1]), x1 = cvtpk(c[4 * g + 2], c[4 * g + 3]);
            const unsigned y0 = cvtpk(c[4 * g + 4], c[4 * g + 5]), y1 = cvtpk(c[4 * g + 6], c[4 * g + 7]);
            auto s0 = __builtin_amdgcn_permlane32_swap(x0, y0, false, false);
            auto s1 = __builtin_amdgcn_permlane32_swap(x1, y1, false, false);
            *(u32x4*)(orow + g * 16) = (u32x4){s0[0], s1[0], s0[1], s1[1]};
        }
    }
    STAMP(13);
    STAMP_FLUSH();
}

extern "C" void kernel_launch(void* const* d_in, const int* in_sizes, int n_in, void* d_out, int out_size, void* d_ws, size_t ws_size,
                              hipStream_t stream) {
    (void)hipFuncSetAttribute((const void*)mha_kernel, hipFuncAttributeMaxDynamicSharedMemorySize, LDS_BYTES);
    mha_kernel<<<256, 512, LDS_BYTES, stream>>>((const char*)d_in[0], (const char*)d_in[1], (const char*)d_in[2], (char*)d_out, (char*)d_ws);
}
```

```cpp
#include <hip/hip_runtime.h>
#include <hip/hip_bf16.h>
#include <stdint.h>

typedef short bf16x8 __attribute__((ext_vector_type(8)));
typedef short s16x4 __attribute__((ext_vector_type(4)));
typedef float f32x16 __attribute__((ext_vector_type(16)));
typedef float f32x4 __attribute__((ext_vector_type(4)));
typedef float f32x2 __attribute__((ext_vector_type(2)));
typedef unsigned u32x4 __attribute__((ext_vector_type(4)));
typedef __bf16 bf16x2_t __attribute__((ext_vector_type(2)));
typedef short v4i16_t __attribute__((ext_vector_type(4)));
typedef unsigned long long u64;
#define LAS __attribute__((address_space(3)))
#define GAS __attribute__((address_space(1)))
#define DI __device__ __forceinline__
#define MFMA(a, b, c) __builtin_amdgcn_mfma_f32_32x32x16_bf16((a), (b), (c), 0, 0, 0)
#define SBAR() __builtin_amdgcn_sched_barrier(0)
#define RLX_AGENT __ATOMIC_RELAXED, __HIP_MEMORY_SCOPE_AGENT
#define BAR() asm volatile("s_waitcnt lgkmcnt(0)\n\ts_barrier" ::: "memory")

#define PRETOUCH 2
#define MAPPING 1
#define MAP_HD(b) (((b) & 7) * 2 + (((b) >> 3) & 1))
#define MAP_QT(b) ((b) >> 4)
constexpr int SEQ = 2048, DM = 448, NH = 16, DH = 28, N3 = 1344;
constexpr float QSCALE = 0.18898223650461362f * 1.4426950408889634f;
constexpr float L_MIN = 9.094947017729282e-13f, L_MAX = 1.099511627776e12f;
constexpr unsigned VROW = 464 * 16, TS = 4 * VROW;
constexpr unsigned WS_K = 0, WS_V = 2u << 20, WS_O = 4u << 20, WS_F = 6u << 20;
constexpr unsigned POISON = 0xffffffffu;
constexpr unsigned F_G = 0, F_R = 4096, F_KV = 131072, F_O = 262144;
constexpr unsigned L_WQ = 0, L_RING = 28672, STG = 24576;
constexpr unsigned L_QX = 131072, L_Q2 = L_QX + 8192, L_GB = L_Q2 + 512;
constexpr unsigned L_WO = 0, L_CB = 114688;
constexpr unsigned LDS_BYTES = 163840;

#define STAMP_DECL
#define STAMP(i)
#define STAMP_FLUSH()

DI unsigned rfl(unsigned x) { return __builtin_amdgcn_readfirstlane(x); }
DI unsigned cvtpk(float lo, float hi) { f32x2 v = {lo, hi}; bf16x2_t b = __builtin_convertvector(v, bf16x2_t); return __builtin_bit_cast(unsigned, b); }
DI u32x4 pack8(const f32x16& x, int s) {
    return (u32x4){cvtpk(x[8 * s], x[8 * s + 1]), cvtpk(x[8 * s + 2], x[8 * s + 3]), cvtpk(x[8 * s + 4], x[8 * s + 5]), cvtpk(x[8 * s + 6], x[8 * s + 7])};
}
DI bf16x8 asbf(u32x4 x) { return __builtin_bit_cast(bf16x8, x); }
DI s16x4 vtr(LAS const char* p) { return __builtin_bit_cast(s16x4, __builtin_amdgcn_ds_read_tr16_b64_v4i16((LAS v4i16_t*)p)); }
DI bf16x8 cat(s16x4 lo, s16x4 hi) { return __builtin_shufflevector(lo, hi, 0, 1, 2, 3, 4, 5, 6, 7); }
DI float half_lo(float x) { auto r = __builtin_amdgcn_permlane32_swap(__float_as_uint(x), __float_as_uint(x), false, false); return __uint_as_float(r[0]); }
DI float half_hi(float x) { auto r = __builtin_amdgcn_permlane32_swap(__float_as_uint(x), __float_as_uint(x), false, false); return __uint_as_float(r[1]); }
DI unsigned wave_max_u(unsigned x) {
#define DPPMAX(ctrl, rm) x = max(x, (unsigned)__builtin_amdgcn_update_dpp((int)x, (int)x, ctrl, rm, 0xf, false))
    DPPMAX(0x111, 0xf); DPPMAX(0x112, 0xf); DPPMAX(0x114, 0xf); DPPMAX(0x118, 0xf); DPPMAX(0x142, 0xa); DPPMAX(0x143, 0xc);
#undef DPPMAX
    return (unsigned)__builtin_amdgcn_readlane((int)x, 63);
}
DI void dma16(unsigned lds_dst, unsigned voff, __amdgpu_buffer_rsrc_t rs, unsigned soff) {
    asm volatile("s_mov_b32 m0, %0\n\ts_nop 0\n\tbuffer_load_dwordx4 %1, %2, %3 offen lds" ::"s"(lds_dst), "v"(voff), "s"(rs), "s"(soff) : "m0", "memory");
}
DI void st16_wt(u32x4 v, __amdgpu_buffer_rsrc_t rs, unsigned voff, unsigned soff) { __builtin_amdgcn_raw_buffer_store_b128(v, rs, voff, soff, 16); }
DI u32x4 ld16_wt(__amdgpu_buffer_rsrc_t rs, unsigned voff, unsigned soff) { return __builtin_amdgcn_raw_buffer_load_b128(rs, voff, soff, 16); }
DI __amdgpu_buffer_rsrc_t mkrs(const void* p, unsigned bytes) { return __builtin_amdgcn_make_buffer_rsrc((void*)p, (short)0, (int)bytes, 0x00020000); }
DI unsigned poll_tags(const GAS u64* g, unsigned tag) {
    u64 v = __hip_atomic_load(g, RLX_AGENT);
#pragma nounroll
    for (unsigned spins = 0; spins < (1u << 16); ++spins) {
        if (__all((unsigned)v == tag)) break;
        if ((unsigned)v != tag) v = __hip_atomic_load(g, RLX_AGENT);
    }
    __builtin_amdgcn_fence(__ATOMIC_ACQUIRE, "wavefront");
    return (unsigned)(v >> 32);
}
DI unsigned touch(const GAS char* p) { unsigned t; asm volatile("global_load_dword %0, %1, off" : "=v"(t) : "v"(p) : "memory"); return t; }

template <bool ISK>
DI void kv_gemm(LAS char* lds, unsigned w, int lane, bf16x8 (&xf)[7][4], f32x16& a0, f32x16& a1) {
    const int r = lane & 31, h = lane >> 5, i16 = lane & 15, q4 = i16 >> 2, p4 = i16 & 3, g16 = (lane >> 4) & 1;
    const int pc = ISK ? (((p4 & 1) << 1) | (p4 >> 1)) : p4;
    LAS const char* xb = lds + L_RING + w * 4096 + r * 128;
    LAS const char* wb0 = lds + L_RING + 16384 + (8 * h + q4) * 128 + ((q4 >> 1) * 64) + g16 * 32 + pc * 8;
    LAS const char* wb1 = lds + L_RING + 16384 + (8 * h + q4) * 128 + (((q4 >> 1) ^ 1) * 64) + g16 * 32 + pc * 8;
    const int xs = (r >> 1) & 7;
    bf16x8 wf[2][4][2];
#define RD(i) do { const int so = ((i) % 5) * STG; _Pragma("unroll") for (int ks = 0; ks < 4; ++ks) {                          \
        xf[i][ks] = *(LAS const bf16x8*)(xb + so + (((2 * ks + h) ^ xs) * 16));                                                \
        wf[(i) & 1][ks][0] = cat(vtr(wb0 + so + ks * 2048), vtr(wb0 + so + ks * 2048 + 512));                                  \
        wf[(i) & 1][ks][1] = cat(vtr(wb1 + so + ks * 2048), vtr(wb1 + so + ks * 2048 + 512)); } } while (0)
#define MM(i) do { _Pragma("unroll") for (int ks = 0; ks < 4; ++ks) {                                                          \
        if (ISK) { a0 = MFMA(wf[(i) & 1][ks][0], xf[i][ks], a0); a1 = MFMA(wf[(i) & 1][ks][1], xf[i][ks], a1); }               \
        else     { a0 = MFMA(xf[i][ks], wf[(i) & 1][ks][0], a0); a1 = MFMA(xf[i][ks], wf[(i) & 1][ks][1], a1); } } } while (0)
    BAR(); RD(0);
#pragma unroll
    for (int i = 0; i < 5; ++i) { BAR(); RD(i + 1); SBAR(); MM(i); SBAR(); }
    MM(5); SBAR();
    BAR(); RD(6); MM(6);
#undef RD
#undef MM
}

DI void stage_dma(unsigned mw, unsigned mx, unsigned sw, unsigned sx, unsigned wvo, unsigned xvo0, unsigned xvo1, __amdgpu_buffer_rsrc_t rsW,
                  __amdgpu_buffer_rsrc_t rsX) {
    unsigned t;
    asm volatile("s_mov_b32 m0, %1\n\ts_add_u32 %0, %3, 21504\n\tbuffer_load_dwordx4 %5, %8, %3 offen lds\n\t"
                 "s_add_u32 m0, m0, 1024\n\ts_nop 0\n\tbuffer_load_dwordx4 %5, %8, %0 offen lds\n\t"
                 "s_mov_b32 m0, %2\n\ts_add_u32 %0, %4, 7168\n\tbuffer_load_dwordx4 %6, %9, %4 offen lds\n\t"
                 "s_add_u32 m0, m0, 1024\n\ts_nop 0\n\tbuffer_load_dwordx4 %7, %9, %0 offen lds\n\t"
                 "s_add_u32 m0, m0, 1024\n\ts_add_u32 %0, %4, 14336\n\tbuffer_load_dwordx4 %6, %9, %0 offen lds\n\t"
                 "s_add_u32 m0, m0, 1024\n\ts_add_u32 %0, %4, 21504\n\tbuffer_load_dwordx4 %7, %9, %0 offen lds"
                 : "=&s"(t) : "s"(mw), "s"(mx), "s"(sw), "s"(sx), "v"(wvo), "v"(xvo0), "v"(xvo1), "s"(rsW), "s"(rsX) : "m0", "scc", "memory");
}

__global__ __launch_bounds__(512, 2) void mha_kernel(const char* __restrict__ x, const char* __restrict__ wqkv, const char* __restrict__ wo,
                                                     char* __restrict__ out, char* __restrict__ ws) {
    extern __shared__ __attribute__((aligned(16))) char lds_[];
    LAS char* lds = (LAS char*)lds_;
    STAMP_DECL STAMP(0);
    const unsigned wv = rfl(threadIdx.x >> 6), w = wv & 3;
    const unsigned lds0 = (unsigned)(uintptr_t)lds_;
    GAS char* wsg = (GAS char*)ws;
#define DECODE()                                                                                                              \
    unsigned b = blockIdx.x, lane = threadIdx.x & 63;                                                                        \
    asm volatile("" : "+s"(b)); asm volatile("" : "+v"(lane));                                                                \
    const unsigned hd = MAP_HD(b), qt = MAP_QT(b);                                                                            \
    const unsigned ch = hd < 14 ? hd : hd - 2;         \
    const unsigned rot = (hd + 2 * qt) % 7;
#define DECODE2()                                                                                                             \
    const bool isK = ch < 7;                                                                                                  \
    const unsigned g0 = (7 * hd) >> 1;                                                  \
    const unsigned cg = hd >> 2, q32 = qt * 4 + (hd & 3);                   \
    unsigned kbi[7];                                                                                                          \
    _Pragma("unroll") for (int i = 0; i < 7; ++i) { const unsigned k = rot + i; kbi[i] = k >= 7 ? k - 7 : k; }               \
    const __amdgpu_buffer_rsrc_t rsOI = mkrs(ws + WS_O, 2u << 20);

    if (__builtin_expect(wv >= 4, 1)) {
        DECODE();
        const unsigned xrow = qt * 128 + w * 32 + (lane >> 3);
        const unsigned xvo0 = xrow * (DM * 2) + (((lane & 7) ^ (lane >> 4)) * 16), xvo1 = xvo0 ^ 64;
        const unsigned wvo = (16 * w + (lane >> 3)) * (N3 * 2) + 896 + ch * 128 + (((lane & 7) ^ (((lane >> 4) & 1) << 2)) * 16);
        const __amdgpu_buffer_rsrc_t rsW = mkrs(wqkv, DM * N3 * 2), rsX = mkrs(x, SEQ * DM * 2);
        STAMP(14);
        stage_dma(lds0 + L_RING + 16384 + w * 2048, lds0 + L_RING + w * 4096, rot * (64 * N3 * 2), rot * 128, wvo, xvo0, xvo1, rsW, rsX);
        DECODE2();
        const __amdgpu_buffer_rsrc_t rsF = mkrs(ws + WS_F, 4096);
#define STAGE(i) stage_dma(lds0 + L_RING + ((i) % 5) * STG + 16384 + w * 2048, lds0 + L_RING + ((i) % 5) * STG + w * 4096, kbi[i] * (64 * N3 * 2), kbi[i] * 128, \
                           wvo, xvo0, xvo1, rsW, rsX)
#define VBAR(NV) asm volatile("s_waitcnt vmcnt(" #NV ")\n\ts_barrier" ::: "memory")
        STAGE(1); STAGE(2);
        STAMP(1);
        VBAR(12); STAGE(3);
        STAMP(2);
        VBAR(12); STAGE(4);
        VBAR(12); STAGE(5);
        VBAR(12); STAGE(6);
        VBAR(12);
        unsigned gen;
        { const unsigned vo = F_G; asm volatile("buffer_load_dword %0, %1, %2, 0 offen sc1" : "=v"(gen) : "v"(vo), "s"(rsF) : "memory"); }
        {
            const unsigned qvo = (16 * w + (lane >> 2)) * (N3 * 2) + g0 * 16 + (lane & 3) * 16;
#pragma unroll
            for (int j = 0; j < 7; ++j) dma16(lds0 + L_WQ + (16 * w + 64 * kbi[j]) * 64, qvo, rsW, kbi[j] * 64 * (N3 * 2));
        }
        VBAR(14); VBAR(8);
        STAMP(3);
        asm volatile("s_waitcnt vmcnt(7)" : "+v"(gen)::"memory");
        unsigned tag = rfl(gen);
        { const unsigned t = (tag ^ (tag >> 15)) * 0x2c1b3c6du + 0x9e3779b9u; tag = (t == tag) ? t + 1 : t; }
        const unsigned hh = lane & 15, wh = (lane >> 4) & 1;
        const unsigned c = isK ? (((7 * hh >> 1) + 3 * wh) >> 3) : 7 + ((28 * hh + 27 * wh) >> 6);
        GAS u64* fp = (GAS u64*)(wsg + WS_F + (lane < 32 ? F_KV + ((hh * 4 + (isK ? 0 : 2) + wh) * 16 + qt) * 128 : F_R + (qt * 16 + hd) * 8));
        const bool fon = w == 0 && ((lane < 32 && c == hd) || lane == 32);
        if (w == 0 && lane == 0) *(LAS unsigned*)(lds + L_GB + 20) = tag;
        asm volatile("s_waitcnt lgkmcnt(0)\n\ts_barrier" : "+v"(fp)::"memory");
        if (w == 0) {
            const u32x4 gb = *(LAS const u32x4*)(lds + L_GB);
            if (fon) __hip_atomic_store(fp, ((u64)max(max(gb[0], gb[1]), max(gb[2], gb[3])) << 32) | tag, RLX_AGENT);
            asm volatile("s_waitcnt vmcnt(1)" ::: "memory");
        } else {
            asm volatile("s_waitcnt vmcnt(0)" ::: "memory");
        }
        STAMP(4);
        asm volatile("s_waitcnt lgkmcnt(0)\n\ts_barrier" ::: "memory");
        LAS unsigned* ord = (LAS unsigned*)(lds + L_CB + w * 12288 + (w == 2 ? 2048 : 0));
        if (lane < 5) ord[lane] = lane == 4 ? 0x40000000u : 0xffffffffu;
#pragma nounroll
        for (unsigned j = 0; j < 4; ++j) {
            const unsigned tt = w + 4 * ((j + (qt >> 2)) & 3);
            const unsigned g = poll_tags((const GAS u64*)(wsg + WS_F + F_KV + hd * 8192 + ((lane & 3) * 16 + tt) * 128), tag);
            if (lane == 0) ord[j] = tt * (4 * TS);
            if (j == 0) {
                const unsigned gm = wave_max_u(g);
                if (tt == qt && lane == 0) *(LAS unsigned*)(lds + L_GB + 16) = gm;
                STAMP(5);
                BAR();
                STAMP(6);
                asm volatile("s_barrier" ::: "memory");
            }
        }
        if (w == 1 && b == 0) {
            const GAS u64* fr = (const GAS u64*)(wsg + WS_F + F_R) + lane;
#pragma nounroll
            for (unsigned spins = 0; spins < (1u << 16); ++spins) {
                const u64 v0 = __hip_atomic_load(fr, RLX_AGENT), v1 = __hip_atomic_load(fr + 64, RLX_AGENT);
                const u64 v2 = __hip_atomic_load(fr + 128, RLX_AGENT), v3 = __hip_atomic_load(fr + 192, RLX_AGENT);
                if (__all((unsigned)v0 == tag && (unsigned)v1 == tag && (unsigned)v2 == tag && (unsigned)v3 == tag)) break;
                __builtin_amdgcn_s_sleep(4);
            }
            if (lane == 0) __hip_atomic_store((GAS unsigned*)(wsg + WS_F + F_G), tag, RLX_AGENT);
        }
        {
            const __amdgpu_buffer_rsrc_t rsO = mkrs(wo, DM * DM * 2);
            const unsigned vow = (4 * w + (lane >> 4)) * (DM * 2) + cg * 256 + ((((lane >> 2) & 3) ^ (lane >> 4)) * 64) + (lane & 3) * 16;
            __builtin_amdgcn_s_sleep(8);
#pragma unroll 1
            for (unsigned j = 0; j < 28; ++j) { dma16(lds0 + L_WO + (4 * j + w) * 1024, vow, rsO, j * 14336); __builtin_amdgcn_s_sleep(1); }
        }
        st16_wt((u32x4){POISON, POISON, POISON, POISON}, rsOI, lane * 16, ((qt * 4 + w) * NH + hd) * 2048);
        st16_wt((u32x4){POISON, POISON, POISON, POISON}, rsOI, lane * 16 + 1024, ((qt * 4 + w) * NH + hd) * 2048);
        if (lane < 4) __hip_atomic_store((GAS u64*)(wsg + WS_F + F_O + (qt * 16 + lane * 4 + w) * 2048 + hd * 128), (u64)(tag ^ 1), RLX_AGENT);
        VBAR(0);
        STAMP(8);
        asm volatile("s_barrier" ::: "memory");
        STAMP(9);
        {
            u32x4 t[8];
            (void)poll_tags((const GAS u64*)(wsg + WS_F + F_O + (qt * 16 + hd) * 2048 + (w * 4 + (lane & 3)) * 128), tag);
            STAMP(7);
#pragma nounroll
            for (unsigned spins = 0; spins < (1u << 16); ++spins) {
                unsigned m = 0;
                asm volatile("" ::: "memory");
#pragma unroll
                for (int f = 0; f < 8; ++f) t[f] = ld16_wt(rsOI, lane * 16 + f * 1024, q32 * (NH * 2048) + w * 8192);
#pragma unroll
                for (int f = 0; f < 8; ++f) m = max(m, max(max(t[f][0], t[f][1]), max(t[f][2], t[f][3])));
                if (__all(m != POISON)) break;
            }
            STAMP(10);
#pragma unroll
            for (int f = 0; f < 8; ++f) *(LAS u32x4*)(lds + L_CB + (w * 8 + f) * 1024 + lane * 16) = t[f];
        }
        BAR();
        STAMP(11);
        STAMP_FLUSH();
        return;
    }

    DECODE(); DECODE2();
    const int r = lane & 31, h = lane >> 5, i16 = lane & 15, q4 = i16 >> 2, p4 = i16 & 3, g16 = (lane >> 4) & 1;
    f32x16 a0 = {}, a1 = {};
    bf16x8 xf[7][4];
    const unsigned kt = qt * 4 + w;
    unsigned gmx = 0;
    unsigned tch1 = 0;
    {
        unsigned long long pc0;
        asm volatile("s_getpc_b64 %0" : "=s"(pc0));
        tch1 = touch((const GAS char*)(pc0 + (((b >> 3) % 3) * 4 + w) * 1024 + (lane & 7) * 128));
    }
    if (isK) {
        kv_gemm<true>(lds, w, lane, xf, a0, a1);
        STAMP(3);
        {
            const __amdgpu_buffer_rsrc_t rs = mkrs(ws + WS_K, 64 * TS);
            const unsigned vo = (8 * ch + h) * 512 + r * 16, so = kt * TS;
            st16_wt(pack8(a0, 0), rs, vo, so); st16_wt(pack8(a0, 1), rs, vo + 1024, so);
            st16_wt(pack8(a1, 0), rs, vo + 2048, so); st16_wt(pack8(a1, 1), rs, vo + 3072, so);
            float s0 = 0.f, s1 = 0.f, s2 = 0.f, s3 = 0.f;
#pragma unroll
            for (int j = 0; j < 8; ++j) { s0 += a0[j] * a0[j]; s1 += a0[8 + j] * a0[8 + j]; s2 += a1[j] * a1[j]; s3 += a1[8 + j] * a1[8 + j]; }
            gmx = max(max(__float_as_uint(s0), __float_as_uint(s1)), max(__float_as_uint(s2), __float_as_uint(s3)));
            gmx = wave_max_u(gmx);
        }
    } else {
        kv_gemm<false>(lds, w, lane, xf, a0, a1);
        STAMP(3);
        if (hd < 14) {
        const __amdgpu_buffer_rsrc_t rs = mkrs(ws + WS_V, 64 * TS);
        const unsigned n0 = 64 * (ch - 7) + r, n1 = n0 + 32, so = kt * TS;
        const unsigned v0 = h * VROW + (n0 + (n0 * 2341 >> 16)) * 16, v1 = h * VROW + (n1 + (n1 * 2341 >> 16)) * 16;
        st16_wt(pack8(a0, 0), rs, v0, so); st16_wt(pack8(a0, 1), rs, v0 + 2 * VROW, so);
        st16_wt(pack8(a1, 0), rs, v1, so); st16_wt(pack8(a1, 1), rs, v1 + 2 * VROW, so);
        const unsigned hc = (64 * (ch - 7) * 2341 >> 16) + (lane >> 2), last = 28 * hc + 27;
        if (lane < 16 && (last >> 6) == ch - 7)
            st16_wt((u32x4){0x3f803f80u, 0x3f803f80u, 0x3f803f80u, 0x3f803f80u}, rs, (lane & 3) * VROW + (29 * hc + 28) * 16, so);
        }
    }
    if (lane == 0) *(LAS unsigned*)(lds + L_GB + w * 4) = gmx;
    unsigned vok = WS_K + (g0 + h) * 512 + r * 16, vov = WS_V + h * VROW + (29 * hd + r) * 16;
    const __amdgpu_buffer_rsrc_t rsKV = mkrs(ws, 4u << 20);
    unsigned q2a = lds0 + L_Q2 + r * 16, kba = lds0 + L_GB + 16, ordn = lds0 + L_CB + w * 12288 + (w == 2 ? 2048 : 0) + 4, puta = lds0 + L_CB + w * 12288 + lane * 16;
    unsigned qxa[4];
#pragma unroll
    for (int i = 0; i < 4; ++i) qxa[i] = lds0 + L_QX + ((w + 1 + i) & 3) * 2048 + lane * 16;
    LAS const char* qb = lds + L_WQ + (8 * h + q4) * 64 + g16 * 32 + (((p4 & 1) << 1) | (p4 >> 1)) * 8;
    asm volatile("" : "+v"(vok), "+v"(vov), "+v"(q2a), "+v"(kba), "+v"(ordn), "+v"(puta), "+v"(qxa[0]), "+v"(qxa[1]), "+v"(qxa[2]), "+v"(qxa[3]), "+v"(qb));
    STAMP(1);
    asm volatile("s_waitcnt vmcnt(0) lgkmcnt(0)" : "+v"(tch1)::"memory");
    STAMP(2);
    asm volatile("s_barrier" ::: "memory");
    STAMP(4);
    asm volatile("s_barrier" ::: "memory");
    STAMP(14);
    const unsigned tag = rfl(*(LAS const unsigned*)(lds + L_GB + 20));
    f32x16 aq = {};
    {
        bf16x8 wq[3][4];
#define RDQ(i) do { LAS const char* qk = qb + kbi[i] * 4096; _Pragma("unroll") for (int ks = 0; ks < 4; ++ks)                  \
            wq[(i) % 3][ks] = cat(vtr(qk + ks * 1024), vtr(qk + ks * 1024 + 256)); } while (0)
        RDQ(0); RDQ(1); SBAR();
#pragma unroll
        for (int i = 0; i < 7; ++i) {
            if (i + 2 < 7) RDQ(i + 2);
            SBAR();
#pragma unroll
            for (int ks = 0; ks < 4; ++ks) aq = MFMA(wq[i % 3][ks], xf[i][ks], aq);
            SBAR();
        }
#undef RDQ
    }
    asm volatile("" : "+v"(aq));
    STAMP(15);
    {
        const bool zlo = (hd & 1) && !h, zhi = !(hd & 1) && h;
        float qs[4] = {0.f, 0.f, 0.f, 0.f};
#pragma unroll
        for (int i = 0; i < 16; ++i) {
            aq[i] *= QSCALE;
            if (i < 4) aq[i] = zlo ? 0.f : aq[i];
            if (i >= 12) aq[i] = zhi ? 0.f : aq[i];
            qs[i & 3] += aq[i] * aq[i];
        }
        *(LAS u32x4*)(lds + (qxa[3] - lds0)) = pack8(aq, 0);
        *(LAS u32x4*)(lds + (qxa[3] - lds0) + 1024) = pack8(aq, 1);
        float q2 = (qs[0] + qs[1]) + (qs[2] + qs[3]);
        q2 = half_lo(q2) + half_hi(q2);
        *(LAS float*)(lds + (q2a - lds0) + w * 4) = q2;
    }
    STAMP(5);

    f32x16 acc;
    u32x4 qo0, qo1;
    {
        unsigned sb = (w + (qt & 12)) * (4 * TS), cnt = 4;
        asm volatile("s_setprio 2\n\ts_waitcnt lgkmcnt(0)\n\ts_barrier\n\t"
        "buffer_load_dwordx4 v[160:163], v224, %[rs], %[sb] offen sc1\n\t"
        "buffer_load_dwordx4 v[164:167], v224, %[rs], %[sb] offen offset:1024 sc1\n\t"
        "buffer_load_dwordx4 v[168:171], v225, %[rs], %[sb] offen sc1\n\t"
        "buffer_load_dwordx4 v[172:175], v226, %[rs], %[sb] offen sc1\n\t"
        "buffer_load_dwordx4 v[176:179], v227, %[rs], %[sb] offen sc1\n\t"
        "buffer_load_dwordx4 v[180:183], v227, %[rs], %[sb] offen offset:1024 sc1\n\t"
        "buffer_load_dwordx4 v[184:187], v228, %[rs], %[sb] offen sc1\n\t"
        "buffer_load_dwordx4 v[188:191], v229, %[rs], %[sb] offen sc1\n\t"
        "buffer_load_dwordx4 v[192:195], v230, %[rs], %[sb] offen sc1\n\t"
        "buffer_load_dwordx4 v[196:199], v230, %[rs], %[sb] offen offset:1024 sc1\n\t"
        "buffer_load_dwordx4 v[200:203], v231, %[rs], %[sb] offen sc1\n\t"
        "buffer_load_dwordx4 v[204:207], v232, %[rs], %[sb] offen sc1\n\t"
        "ds_read_b128 v[112:115], v241\n\t"
        "ds_read_b128 v[116:119], v241 offset:1024\n\t"
        "ds_read_b128 v[120:123], v242\n\t"
        "ds_read_b128 v[124:127], v242 offset:1024\n\t"
        "ds_read_b128 v[128:131], v243\n\t"
        "ds_read_b128 v[132:135], v243 offset:1024\n\t"
        "ds_read_b128 v[136:139], v244\n\t"
        "ds_read_b128 v[140:143], v244 offset:1024\n\t"
        "ds_read_b128 v[80:83], v236\n\t"
        "ds_read_b32 v237, v237\n\t"
        "v_mov_b32 v0, 0\n\t"
        "v_mov_b32 v1, 0\n\t"
        "v_mov_b32 v2, 0\n\t"
        "v_mov_b32 v3, 0\n\t"
        "v_mov_b32 v4, 0\n\t"
        "v_mov_b32 v5, 0\n\t"
        "v_mov_b32 v6, 0\n\t"
        "v_mov_b32 v7, 0\n\t"
        "v_mov_b32 v8, 0\n\t"
        "v_mov_b32 v9, 0\n\t"
        "v_mov_b32 v10, 0\n\t"
        "v_mov_b32 v11, 0\n\t"
        "v_mov_b32 v12, 0\n\t"
        "v_mov_b32 v13, 0\n\t"
        "v_mov_b32 v14, 0\n\t"
        "v_mov_b32 v15, 0\n\t"
        "v_mov_b32 v16, 0\n\t"
        "v_mov_b32 v17, 0\n\t"
        "v_mov_b32 v18, 0\n\t"
        "v_mov_b32 v19, 0\n\t"
        "v_mov_b32 v20, 0\n\t"
        "v_mov_b32 v21, 0\n\t"
        "v_mov_b32 v22, 0\n\t"
        "v_mov_b32 v23, 0\n\t"
        "v_mov_b32 v24, 0\n\t"
        "v_mov_b32 v25, 0\n\t"
        "v_mov_b32 v26, 0\n\t"
        "v_mov_b32 v27, 0\n\t"
        "v_mov_b32 v28, 0\n\t"
        "v_mov_b32 v29, 0\n\t"
        "v_mov_b32 v30, 0\n\t"
        "v_mov_b32 v31, 0\n\t"
        "v_mov_b32 v32, 0\n\t"
        "v_mov_b32 v33, 0\n\t"
        "v_mov_b32 v34, 0\n\t"
        "v_mov_b32 v35, 0\n\t"
        "v_mov_b32 v36, 0\n\t"
        "v_mov_b32 v37, 0\n\t"
        "v_mov_b32 v38, 0\n\t"
        "v_mov_b32 v39, 0\n\t"
        "v_mov_b32 v40, 0\n\t"
        "v_mov_b32 v41, 0\n\t"
        "v_mov_b32 v42, 0\n\t"
        "v_mov_b32 v43, 0\n\t"
        "v_mov_b32 v44, 0\n\t"
        "v_mov_b32 v45, 0\n\t"
        "v_mov_b32 v46, 0\n\t"
        "v_mov_b32 v47, 0\n\t"
        "v_mov_b32 v48, 0\n\t"
        "v_mov_b32 v49, 0\n\t"
        "v_mov_b32 v50, 0\n\t"
        "v_mov_b32 v51, 0\n\t"
        "v_mov_b32 v52, 0\n\t"
        "v_mov_b32 v53, 0\n\t"
        "v_mov_b32 v54, 0\n\t"
        "v_mov_b32 v55, 0\n\t"
        "v_mov_b32 v56, 0\n\t"
        "v_mov_b32 v57, 0\n\t"
        "v_mov_b32 v58, 0\n\t"
        "v_mov_b32 v59, 0\n\t"
        "v_mov_b32 v60, 0\n\t"
        "v_mov_b32 v61, 0\n\t"
        "v_mov_b32 v62, 0\n\t"
        "v_mov_b32 v63, 0\n\t"
        "v_mov_b32 v152, 0\n\t"
        "v_mov_b32 v153, 0\n\t"
        "v_mov_b32 v154, 0\n\t"
        "v_mov_b32 v155, 0\n\t"
        "v_mov_b32 v156, 0\n\t"
        "v_mov_b32 v157, 0\n\t"
        "v_mov_b32 v158, 0\n\t"
        "v_mov_b32 v159, 0\n\t"
        "v_mov_b32 v216, 0\n\t"
        "v_mov_b32 v217, 0\n\t"
        "v_mov_b32 v218, 0\n\t"
        "v_mov_b32 v219, 0\n\t"
        "v_mov_b32 v220, 0\n\t"
        "v_mov_b32 v221, 0\n\t"
        "v_mov_b32 v222, 0\n\t"
        "v_mov_b32 v223, 0\n\t"
        "s_waitcnt lgkmcnt(0)\n\t"
        "s_barrier\n\t"
        "v_max3_f32 v236, v80, v81, v82\n\t"
        "v_max_f32 v236, v236, v83\n\t"
        "v_mul_f32 v236, v236, v237\n\t"
        "v_sqrt_f32 v236, v236\n\t"
        "s_nop 0\n\t"
        "v_mul_f32 v96, 0xc0028f5c, v236\n\t"
        "v_mov_b32 v97, v96\n\t"
        "v_mov_b32 v98, v96\n\t"
        "v_mov_b32 v99, v96\n\t"
        "v_mov_b32 v100, v96\n\t"
        "v_mov_b32 v101, v96\n\t"
        "v_mov_b32 v102, v96\n\t"
        "v_mov_b32 v103, v96\n\t"
        "v_mov_b32 v104, v96\n\t"
        "v_mov_b32 v105, v96\n\t"
        "v_mov_b32 v106, v96\n\t"
        "v_mov_b32 v107, v96\n\t"
        "v_mov_b32 v108, v96\n\t"
        "v_mov_b32 v109, v96\n\t"
        "v_mov_b32 v110, v96\n\t"
        "v_mov_b32 v111, v96\n\t"
        "s_waitcnt vmcnt(8)\n\t"
        "v_mfma_f32_32x32x16_bf16 v[64:79], v[160:163], v[112:115], v[96:111]\n\t"
        "v_mfma_f32_32x32x16_bf16 v[64:79], v[164:167], v[116:119], v[64:79]\n\t"
        "s_nop 15\n\t"
        "s_nop 3\n\t"
        "LOOP%=:\n\t"
        "v_mfma_f32_32x32x16_bf16 v[80:95], v[160:163], v[120:123], v[96:111]\n\t"
        "v_exp_f32 v64, v64\n\t"
        "v_exp_f32 v65, v65\n\t"
        "v_exp_f32 v66, v66\n\t"
        "v_exp_f32 v67, v67\n\t"
        "v_mfma_f32_32x32x16_bf16 v[80:95], v[164:167], v[124:127], v[80:95]\n\t"
        "v_exp_f32 v68, v68\n\t"
        "v_exp_f32 v69, v69\n\t"
        "v_exp_f32 v70, v70\n\t"
        "v_exp_f32 v71, v71\n\t"
        "v_cvt_pk_bf16_f32 v144, v64, v65\n\t"
        "v_cvt_pk_bf16_f32 v145, v66, v67\n\t"
        "v_mfma_f32_32x32x16_bf16 v[48:63], v[216:219], v[152:155], v[48:63]\n\t"
        "v_exp_f32 v72, v72\n\t"
        "v_exp_f32 v73, v73\n\t"
        "v_exp_f32 v74, v74\n\t"
        "v_exp_f32 v75, v75\n\t"
        "v_cvt_pk_bf16_f32 v146, v68, v69\n\t"
        "v_cvt_pk_bf16_f32 v147, v70, v71\n\t"
        "v_mfma_f32_32x32x16_bf16 v[48:63], v[220:223], v[156:159], v[48:63]\n\t"
        "v_exp_f32 v76, v76\n\t"
        "v_exp_f32 v77, v77\n\t"
        "v_exp_f32 v78, v78\n\t"
        "v_exp_f32 v79, v79\n\t"
        "v_cvt_pk_bf16_f32 v148, v72, v73\n\t"
        "v_cvt_pk_bf16_f32 v149, v74, v75\n\t"
        "v_cvt_pk_bf16_f32 v150, v76, v77\n\t"
        "v_cvt_pk_bf16_f32 v151, v78, v79\n\t"
        "v_mfma_f32_32x32x16_bf16 v[64:79], v[160:163], v[128:131], v[96:111]\n\t"
        "buffer_load_dwordx4 v[208:211], v233, %[rs], %[sb] offen sc1\n\t"
        "buffer_load_dwordx4 v[212:215], v233, %[rs], %[sb] offen offset:1024 sc1\n\t"
        "buffer_load_dwordx4 v[216:219], v234, %[rs], %[sb] offen sc1\n\t"
        "buffer_load_dwordx4 v[220:223], v235, %[rs], %[sb] offen sc1\n\t"
        "v_exp_f32 v80, v80\n\t"
        "v_exp_f32 v81, v81\n\t"
        "v_exp_f32 v82, v82\n\t"
        "v_exp_f32 v83, v83\n\t"
        "v_mfma_f32_32x32x16_bf16 v[64:79], v[164:167], v[132:135], v[64:79]\n\t"
        "v_exp_f32 v84, v84\n\t"
        "v_exp_f32 v85, v85\n\t"
        "v_exp_f32 v86, v86\n\t"
        "v_exp_f32 v87, v87\n\t"
        "v_cvt_pk_bf16_f32 v152, v80, v81\n\t"
        "v_cvt_pk_bf16_f32 v153, v82, v83\n\t"
        "v_mfma_f32_32x32x16_bf16 v[0:15], v[168:171], v[144:147], v[0:15]\n\t"
        "v_exp_f32 v88, v88\n\t"
        "v_exp_f32 v89, v89\n\t"
        "v_exp_f32 v90, v90\n\t"
        "v_exp_f32 v91, v91\n\t"
        "v_cvt_pk_bf16_f32 v154, v84, v85\n\t"
        "v_cvt_pk_bf16_f32 v155, v86, v87\n\t"
        "v_mfma_f32_32x32x16_bf16 v[0:15], v[172:175], v[148:151], v[0:15]\n\t"
        "v_exp_f32 v92, v92\n\t"
        "v_exp_f32 v93, v93\n\t"
        "v_exp_f32 v94, v94\n\t"
        "v_exp_f32 v95, v95\n\t"
        "v_cvt_pk_bf16_f32 v156, v88, v89\n\t"
        "v_cvt_pk_bf16_f32 v157, v90, v91\n\t"
        "v_cvt_pk_bf16_f32 v158, v92, v93\n\t"
        "v_cvt_pk_bf16_f32 v159, v94, v95\n\t"
        "v_mfma_f32_32x32x16_bf16 v[80:95], v[160:163], v[136:139], v[96:111]\n\t"
        "ds_read_b32 v239, v238\n\t"
        "v_exp_f32 v64, v64\n\t"
        "v_exp_f32 v65, v65\n\t"
        "v_exp_f32 v66, v66\n\t"
        "v_exp_f32 v67, v67\n\t"
        "v_mfma_f32_32x32x16_bf16 v[80:95], v[164:167], v[140:143], v[80:95]\n\t"
        "v_exp_f32 v68, v68\n\t"
        "v_exp_f32 v69, v69\n\t"
        "v_exp_f32 v70, v70\n\t"
        "v_exp_f32 v71, v71\n\t"
        "v_cvt_pk_bf16_f32 v144, v64, v65\n\t"
        "v_cvt_pk_bf16_f32 v145, v66, v67\n\t"
        "v_mfma_f32_32x32x16_bf16 v[16:31], v[168:171], v[152:155], v[16:31]\n\t"
        "v_exp_f32 v72, v72\n\t"
        "v_exp_f32 v73, v73\n\t"
        "v_exp_f32 v74, v74\n\t"
        "v_exp_f32 v75, v75\n\t"
        "v_cvt_pk_bf16_f32 v146, v68, v69\n\t"
        "v_cvt_pk_bf16_f32 v147, v70, v71\n\t"
        "v_mfma_f32_32x32x16_bf16 v[16:31], v[172:175], v[156:159], v[16:31]\n\t"
        "v_exp_f32 v76, v76\n\t"
        "v_exp_f32 v77, v77\n\t"
        "v_exp_f32 v78, v78\n\t"
        "v_exp_f32 v79, v79\n\t"
        "v_cvt_pk_bf16_f32 v148, v72, v73\n\t"
        "v_cvt_pk_bf16_f32 v149, v74, v75\n\t"
        "v_cvt_pk_bf16_f32 v150, v76, v77\n\t"
        "v_cvt_pk_bf16_f32 v151, v78, v79\n\t"
        "s_waitcnt vmcnt(8)\n\t"
        "v_mfma_f32_32x32x16_bf16 v[64:79], v[176:179], v[112:115], v[96:111]\n\t"
        "v_exp_f32 v80, v80\n\t"
        "v_exp_f32 v81, v81\n\t"
        "v_exp_f32 v82, v82\n\t"
        "v_exp_f32 v83, v83\n\t"
        "v_mfma_f32_32x32x16_bf16 v[64:79], v[180:183], v[116:119], v[64:79]\n\t"
        "v_exp_f32 v84, v84\n\t"
        "v_exp_f32 v85, v85\n\t"
        "v_exp_f32 v86, v86\n\t"
        "v_exp_f32 v87, v87\n\t"
        "v_cvt_pk_bf16_f32 v152, v80, v81\n\t"
        "v_cvt_pk_bf16_f32 v153, v82, v83\n\t"
        "v_mfma_f32_32x32x16_bf16 v[32:47], v[168:171], v[144:147], v[32:47]\n\t"
        "v_exp_f32 v88, v88\n\t"
        "v_exp_f32 v89, v89\n\t"
        "v_exp_f32 v90, v90\n\t"
        "v_exp_f32 v91, v91\n\t"
        "v_cvt_pk_bf16_f32 v154, v84, v85\n\t"
        "v_cvt_pk_bf16_f32 v155, v86, v87\n\t"
        "v_mfma_f32_32x32x16_bf16 v[32:47], v[172:175], v[148:151], v[32:47]\n\t"
        "v_exp_f32 v92, v92\n\t"
        "v_exp_f32 v93, v93\n\t"
        "v_exp_f32 v94, v94\n\t"
        "v_exp_f32 v95, v95\n\t"
        "v_cvt_pk_bf16_f32 v156, v88, v89\n\t"
        "v_cvt_pk_bf16_f32 v157, v90, v91\n\t"
        "v_cvt_pk_bf16_f32 v158, v92, v93\n\t"
        "v_cvt_pk_bf16_f32 v159, v94, v95\n\t"
        "v_mfma_f32_32x32x16_bf16 v[80:95], v[176:179], v[120:123], v[96:111]\n\t"
        "CHK%=:\n\t"
        "s_waitcnt lgkmcnt(0)\n\t"
        "v_readfirstlane_b32 %[sb], v239\n\t"
        "ds_read_b32 v239, v238\n\t"
        "s_cmp_eq_u32 %[sb], -1\n\t"
        "s_cbranch_scc1 CHK%=\n\t"
        "v_add_u32 v238, 4, v238\n\t"
        "v_exp_f32 v64, v64\n\t"
        "v_exp_f32 v65, v65\n\t"
        "v_exp_f32 v66, v66\n\t"
        "v_exp_f32 v67, v67\n\t"
        "v_mfma_f32_32x32x16_bf16 v[80:95], v[180:183], v[124:127], v[80:95]\n\t"
        "v_exp_f32 v68, v68\n\t"
        "v_exp_f32 v69, v69\n\t"
        "v_exp_f32 v70, v70\n\t"
        "v_exp_f32 v71, v71\n\t"
        "v_cvt_pk_bf16_f32 v144, v64, v65\n\t"
        "v_cvt_pk_bf16_f32 v145, v66, v67\n\t"
        "v_mfma_f32_32x32x16_bf16 v[48:63], v[168:171], v[152:155], v[48:63]\n\t"
        "v_exp_f32 v72, v72\n\t"
        "v_exp_f32 v73, v73\n\t"
        "v_exp_f32 v74, v74\n\t"
        "v_exp_f32 v75, v75\n\t"
        "v_cvt_pk_bf16_f32 v146, v68, v69\n\t"
        "v_cvt_pk_bf16_f32 v147, v70, v71\n\t"
        "v_mfma_f32_32x32x16_bf16 v[48:63], v[172:175], v[156:159], v[48:63]\n\t"
        "v_exp_f32 v76, v76\n\t"
        "v_exp_f32 v77, v77\n\t"
        "v_exp_f32 v78, v78\n\t"
        "v_exp_f32 v79, v79\n\t"
        "v_cvt_pk_bf16_f32 v148, v72, v73\n\t"
        "v_cvt_pk_bf16_f32 v149, v74, v75\n\t"
        "v_cvt_pk_bf16_f32 v150, v76, v77\n\t"
        "v_cvt_pk_bf16_f32 v151, v78, v79\n\t"
        "v_mfma_f32_32x32x16_bf16 v[64:79], v[176:179], v[128:131], v[96:111]\n\t"
        "buffer_load_dwordx4 v[160:163], v224, %[rs], %[sb] offen sc1\n\t"
        "buffer_load_dwordx4 v[164:167], v224, %[rs], %[sb] offen offset:1024 sc1\n\t"
        "buffer_load_dwordx4 v[168:171], v225, %[rs], %[sb] offen sc1\n\t"
        "buffer_load_dwordx4 v[172:175], v226, %[rs], %[sb] offen sc1\n\t"
        "v_exp_f32 v80, v80\n\t"
        "v_exp_f32 v81, v81\n\t"
        "v_exp_f32 v82, v82\n\t"
        "v_exp_f32 v83, v83\n\t"
        "v_mfma_f32_32x32x16_bf16 v[64:79], v[180:183], v[132:135], v[64:79]\n\t"
        "v_exp_f32 v84, v84\n\t"
        "v_exp_f32 v85, v85\n\t"
        "v_exp_f32 v86, v86\n\t"
        "v_exp_f32 v87, v87\n\t"
        "v_cvt_pk_bf16_f32 v152, v80, v81\n\t"
        "v_cvt_pk_bf16_f32 v153, v82, v83\n\t"
        "v_mfma_f32_32x32x16_bf16 v[0:15], v[184:187], v[144:147], v[0:15]\n\t"
        "v_exp_f32 v88, v88\n\t"
        "v_exp_f32 v89, v89\n\t"
        "v_exp_f32 v90, v90\n\t"
        "v_exp_f32 v91, v91\n\t"
        "v_cvt_pk_bf16_f32 v154, v84, v85\n\t"
        "v_cvt_pk_bf16_f32 v155, v86, v87\n\t"
        "v_mfma_f32_32x32x16_bf16 v[0:15], v[188:191], v[148:151], v[0:15]\n\t"
        "v_exp_f32 v92, v92\n\t"
        "v_exp_f32 v93, v93\n\t"
        "v_exp_f32 v94, v94\n\t"
        "v_exp_f32 v95, v95\n\t"
        "v_cvt_pk_bf16_f32 v156, v88, v89\n\t"
        "v_cvt_pk_bf16_f32 v157, v90, v91\n\t"
        "v_cvt_pk_bf16_f32 v158, v92, v93\n\t"
        "v_cvt_pk_bf16_f32 v159, v94, v95\n\t"
        "v_mfma_f32_32x32x16_bf16 v[80:95], v[176:179], v[136:139], v[96:111]\n\t"
        "v_exp_f32 v64, v64\n\t"
        "v_exp_f32 v65, v65\n\t"
        "v_exp_f32 v66, v66\n\t"
        "v_exp_f32 v67, v67\n\t"
        "v_mfma_f32_32x32x16_bf16 v[80:95], v[180:183], v[140:143], v[80:95]\n\t"
        "v_exp_f32 v68, v68\n\t"
        "v_exp_f32 v69, v69\n\t"
        "v_exp_f32 v70, v70\n\t"
        "v_exp_f32 v71, v71\n\t"
        "v_cvt_pk_bf16_f32 v144, v64, v65\n\t"
        "v_cvt_pk_bf16_f32 v145, v66, v67\n\t"
        "v_mfma_f32_32x32x16_bf16 v[16:31], v[184:187], v[152:155], v[16:31]\n\t"
        "v_exp_f32 v72, v72\n\t"
        "v_exp_f32 v73, v73\n\t"
        "v_exp_f32 v74, v74\n\t"
        "v_exp_f32 v75, v75\n\t"
        "v_cvt_pk_bf16_f32 v146, v68, v69\n\t"
        "v_cvt_pk_bf16_f32 v147, v70, v71\n\t"
        "v_mfma_f32_32x32x16_bf16 v[16:31], v[188:191], v[156:159], v[16:31]\n\t"
        "v_exp_f32 v76, v76\n\t"
        "v_exp_f32 v77, v77\n\t"
        "v_exp_f32 v78, v78\n\t"
        "v_exp_f32 v79, v79\n\t"
        "v_cvt_pk_bf16_f32 v148, v72, v73\n\t"
        "v_cvt_pk_bf16_f32 v149, v74, v75\n\t"
        "v_cvt_pk_bf16_f32 v150, v76, v77\n\t"
        "v_cvt_pk_bf16_f32 v151, v78, v79\n\t"
        "s_waitcnt vmcnt(8)\n\t"
        "v_mfma_f32_32x32x16_bf16 v[64:79], v[192:195], v[112:115], v[96:111]\n\t"
        "v_exp_f32 v80, v80\n\t"
        "v_exp_f32 v81, v81\n\t"
        "v_exp_f32 v82, v82\n\t"
        "v_exp_f32 v83, v83\n\t"
        "v_mfma_f32_32x32x16_bf16 v[64:79], v[196:199], v[116:119], v[64:79]\n\t"
        "v_exp_f32 v84, v84\n\t"
        "v_exp_f32 v85, v85\n\t"
        "v_exp_f32 v86, v86\n\t"
        "v_exp_f32 v87, v87\n\t"
        "v_cvt_pk_bf16_f32 v152, v80, v81\n\t"
        "v_cvt_pk_bf16_f32 v153, v82, v83\n\t"
        "v_mfma_f32_32x32x16_bf16 v[32:47], v[184:187], v[144:147], v[32:47]\n\t"
        "v_exp_f32 v88, v88\n\t"
        "v_exp_f32 v89, v89\n\t"
        "v_exp_f32 v90, v90\n\t"
        "v_exp_f32 v91, v91\n\t"
        "v_cvt_pk_bf16_f32 v154, v84, v85\n\t"
        "v_cvt_pk_bf16_f32 v155, v86, v87\n\t"
        "v_mfma_f32_32x32x16_bf16 v[32:47], v[188:191], v[148:151], v[32:47]\n\t"
        "v_exp_f32 v92, v92\n\t"
        "v_exp_f32 v93, v93\n\t"
        "v_exp_f32 v94, v94\n\t"
        "v_exp_f32 v95, v95\n\t"
        "v_cvt_pk_bf16_f32 v156, v88, v89\n\t"
        "v_cvt_pk_bf16_f32 v157, v90, v91\n\t"
        "v_cvt_pk_bf16_f32 v158, v92, v93\n\t"
        "v_cvt_pk_bf16_f32 v159, v94, v95\n\t"
        "v_mfma_f32_32x32x16_bf16 v[80:95], v[192:195], v[120:123], v[96:111]\n\t"
        "v_exp_f32 v64, v64\n\t"
        "v_exp_f32 v65, v65\n\t"
        "v_exp_f32 v66, v66\n\t"
        "v_exp_f32 v67, v67\n\t"
        "v_mfma_f32_32x32x16_bf16 v[80:95], v[196:199], v[124:127], v[80:95]\n\t"
        "v_exp_f32 v68, v68\n\t"
        "v_exp_f32 v69, v69\n\t"
        "v_exp_f32 v70, v70\n\t"
        "v_exp_f32 v71, v71\n\t"
        "v_cvt_pk_bf16_f32 v144, v64, v65\n\t"
        "v_cvt_pk_bf16_f32 v145, v66, v67\n\t"
        "v_mfma_f32_32x32x16_bf16 v[48:63], v[184:187], v[152:155], v[48:63]\n\t"
        "v_exp_f32 v72, v72\n\t"
        "v_exp_f32 v73, v73\n\t"
        "v_exp_f32 v74, v74\n\t"
        "v_exp_f32 v75, v75\n\t"
        "v_cvt_pk_bf16_f32 v146, v68, v69\n\t"
        "v_cvt_pk_bf16_f32 v147, v70, v71\n\t"
        "v_mfma_f32_32x32x16_bf16 v[48:63], v[188:191], v[156:159], v[48:63]\n\t"
        "v_exp_f32 v76, v76\n\t"
        "v_exp_f32 v77, v77\n\t"
        "v_exp_f32 v78, v78\n\t"
        "v_exp_f32 v79, v79\n\t"
        "v_cvt_pk_bf16_f32 v148, v72, v73\n\t"
        "v_cvt_pk_bf16_f32 v149, v74, v75\n\t"
        "v_cvt_pk_bf16_f32 v150, v76, v77\n\t"
        "v_cvt_pk_bf16_f32 v151, v78, v79\n\t"
        "v_mfma_f32_32x32x16_bf16 v[64:79], v[192:195], v[128:131], v[96:111]\n\t"
        "buffer_load_dwordx4 v[176:179], v227, %[rs], %[sb] offen sc1\n\t"
        "buffer_load_dwordx4 v[180:183], v227, %[rs], %[sb] offen offset:1024 sc1\n\t"
        "buffer_load_dwordx4 v[184:187], v228, %[rs], %[sb] offen sc1\n\t"
        "buffer_load_dwordx4 v[188:191], v229, %[rs], %[sb] offen sc1\n\t"
        "v_exp_f32 v80, v80\n\t"
        "v_exp_f32 v81, v81\n\t"
        "v_exp_f32 v82, v82\n\t"
        "v_exp_f32 v83, v83\n\t"
        "v_mfma_f32_32x32x16_bf16 v[64:79], v[196:199], v[132:135], v[64:79]\n\t"
        "v_exp_f32 v84, v84\n\t"
        "v_exp_f32 v85, v85\n\t"
        "v_exp_f32 v86, v86\n\t"
        "v_exp_f32 v87, v87\n\t"
        "v_cvt_pk_bf16_f32 v152, v80, v81\n\t"
        "v_cvt_pk_bf16_f32 v153, v82, v83\n\t"
        "v_mfma_f32_32x32x16_bf16 v[0:15], v[200:203], v[144:147], v[0:15]\n\t"
        "v_exp_f32 v88, v88\n\t"
        "v_exp_f32 v89, v89\n\t"
        "v_exp_f32 v90, v90\n\t"
        "v_exp_f32 v91, v91\n\t"
        "v_cvt_pk_bf16_f32 v154, v84, v85\n\t"
        "v_cvt_pk_bf16_f32 v155, v86, v87\n\t"
        "v_mfma_f32_32x32x16_bf16 v[0:15], v[204:207], v[148:151], v[0:15]\n\t"
        "v_exp_f32 v92, v92\n\t"
        "v_exp_f32 v93, v93\n\t"
        "v_exp_f32 v94, v94\n\t"
        "v_exp_f32 v95, v95\n\t"
        "v_cvt_pk_bf16_f32 v156, v88, v89\n\t"
        "v_cvt_pk_bf16_f32 v157, v90, v91\n\t"
        "v_cvt_pk_bf16_f32 v158, v92, v93\n\t"
        "v_cvt_pk_bf16_f32 v159, v94, v95\n\t"
        "v_mfma_f32_32x32x16_bf16 v[80:95], v[192:195], v[136:139], v[96:111]\n\t"
        "v_exp_f32 v64, v64\n\t"
        "v_exp_f32 v65, v65\n\t"
        "v_exp_f32 v66, v66\n\t"
        "v_exp_f32 v67, v67\n\t"
        "v_mfma_f32_32x32x16_bf16 v[80:95], v[196:199], v[140:143], v[80:95]\n\t"
        "v_exp_f32 v68, v68\n\t"
        "v_exp_f32 v69, v69\n\t"
        "v_exp_f32 v70, v70\n\t"
        "v_exp_f32 v71, v71\n\t"
        "v_cvt_pk_bf16_f32 v144, v64, v65\n\t"
        "v_cvt_pk_bf16_f32 v145, v66, v67\n\t"
        "v_mfma_f32_32x32x16_bf16 v[16:31], v[200:203], v[152:155], v[16:31]\n\t"
        "v_exp_f32 v72, v72\n\t"
        "v_exp_f32 v73, v73\n\t"
        "v_exp_f32 v74, v74\n\t"
        "v_exp_f32 v75, v75\n\t"
        "v_cvt_pk_bf16_f32 v146, v68, v69\n\t"
        "v_cvt_pk_bf16_f32 v147, v70, v71\n\t"
        "v_mfma_f32_32x32x16_bf16 v[16:31], v[204:207], v[156:159], v[16:31]\n\t"
        "v_exp_f32 v76, v76\n\t"
        "v_exp_f32 v77, v77\n\t"
        "v_exp_f32 v78, v78\n\t"
        "v_exp_f32 v79, v79\n\t"
        "v_cvt_pk_bf16_f32 v148, v72, v73\n\t"
        "v_cvt_pk_bf16_f32 v149, v74, v75\n\t"
        "v_cvt_pk_bf16_f32 v150, v76, v77\n\t"
        "v_cvt_pk_bf16_f32 v151, v78, v79\n\t"
        "s_waitcnt vmcnt(8)\n\t"
        "v_mfma_f32_32x32x16_bf16 v[64:79], v[208:211], v[112:115], v[96:111]\n\t"
        "v_exp_f32 v80, v80\n\t"
        "v_exp_f32 v81, v81\n\t"
        "v_exp_f32 v82, v82\n\t"
        "v_exp_f32 v83, v83\n\t"
        "v_mfma_f32_32x32x16_bf16 v[64:79], v[212:215], v[116:119], v[64:79]\n\t"
        "v_exp_f32 v84, v84\n\t"
        "v_exp_f32 v85, v85\n\t"
        "v_exp_f32 v86, v86\n\t"
        "v_exp_f32 v87, v87\n\t"
        "v_cvt_pk_bf16_f32 v152, v80, v81\n\t"
        "v_cvt_pk_bf16_f32 v153, v82, v83\n\t"
        "v_mfma_f32_32x32x16_bf16 v[32:47], v[200:203], v[144:147], v[32:47]\n\t"
        "v_exp_f32 v88, v88\n\t"
        "v_exp_f32 v89, v89\n\t"
        "v_exp_f32 v90, v90\n\t"
        "v_exp_f32 v91, v91\n\t"
        "v_cvt_pk_bf16_f32 v154, v84, v85\n\t"
        "v_cvt_pk_bf16_f32 v155, v86, v87\n\t"
        "v_mfma_f32_32x32x16_bf16 v[32:47], v[204:207], v[148:151], v[32:47]\n\t"
        "v_exp_f32 v92, v92\n\t"
        "v_exp_f32 v93, v93\n\t"
        "v_exp_f32 v94, v94\n\t"
        "v_exp_f32 v95, v95\n\t"
        "v_cvt_pk_bf16_f32 v156, v88, v89\n\t"
        "v_cvt_pk_bf16_f32 v157, v90, v91\n\t"
        "v_cvt_pk_bf16_f32 v158, v92, v93\n\t"
        "v_cvt_pk_bf16_f32 v159, v94, v95\n\t"
        "v_mfma_f32_32x32x16_bf16 v[80:95], v[208:211], v[120:123], v[96:111]\n\t"
        "v_exp_f32 v64, v64\n\t"
        "v_exp_f32 v65, v65\n\t"
        "v_exp_f32 v66, v66\n\t"
        "v_exp_f32 v67, v67\n\t"
        "v_mfma_f32_32x32x16_bf16 v[80:95], v[212:215], v[124:127], v[80:95]\n\t"
        "v_exp_f32 v68, v68\n\t"
        "v_exp_f32 v69, v69\n\t"
        "v_exp_f32 v70, v70\n\t"
        "v_exp_f32 v71, v71\n\t"
        "v_cvt_pk_bf16_f32 v144, v64, v65\n\t"
        "v_cvt_pk_bf16_f32 v145, v66, v67\n\t"
        "v_mfma_f32_32x32x16_bf16 v[48:63], v[200:203], v[152:155], v[48:63]\n\t"
        "v_exp_f32 v72, v72\n\t"
        "v_exp_f32 v73, v73\n\t"
        "v_exp_f32 v74, v74\n\t"
        "v_exp_f32 v75, v75\n\t"
        "v_cvt_pk_bf16_f32 v146, v68, v69\n\t"
        "v_cvt_pk_bf16_f32 v147, v70, v71\n\t"
        "v_mfma_f32_32x32x16_bf16 v[48:63], v[204:207], v[156:159], v[48:63]\n\t"
        "v_exp_f32 v76, v76\n\t"
        "v_exp_f32 v77, v77\n\t"
        "v_exp_f32 v78, v78\n\t"
        "v_exp_f32 v79, v79\n\t"
        "v_cvt_pk_bf16_f32 v148, v72, v73\n\t"
        "v_cvt_pk_bf16_f32 v149, v74, v75\n\t"
        "v_cvt_pk_bf16_f32 v150, v76, v77\n\t"
        "v_cvt_pk_bf16_f32 v151, v78, v79\n\t"
        "v_mfma_f32_32x32x16_bf16 v[64:79], v[208:211], v[128:131], v[96:111]\n\t"
        "buffer_load_dwordx4 v[192:195], v230, %[rs], %[sb] offen sc1\n\t"
        "buffer_load_dwordx4 v[196:199], v230, %[rs], %[sb] offen offset:1024 sc1\n\t"
        "buffer_load_dwordx4 v[200:203], v231, %[rs], %[sb] offen sc1\n\t"
        "buffer_load_dwordx4 v[204:207], v232, %[rs], %[sb] offen sc1\n\t"
        "s_sub_u32 %[cnt], %[cnt], 1\n\t"
        "s_cmp_eq_u32 %[cnt], 0\n\t"
        "v_exp_f32 v80, v80\n\t"
        "v_exp_f32 v81, v81\n\t"
        "v_exp_f32 v82, v82\n\t"
        "v_exp_f32 v83, v83\n\t"
        "v_mfma_f32_32x32x16_bf16 v[64:79], v[212:215], v[132:135], v[64:79]\n\t"
        "v_exp_f32 v84, v84\n\t"
        "v_exp_f32 v85, v85\n\t"
        "v_exp_f32 v86, v86\n\t"
        "v_exp_f32 v87, v87\n\t"
        "v_cvt_pk_bf16_f32 v152, v80, v81\n\t"
        "v_cvt_pk_bf16_f32 v153, v82, v83\n\t"
        "v_mfma_f32_32x32x16_bf16 v[0:15], v[216:219], v[144:147], v[0:15]\n\t"
        "v_exp_f32 v88, v88\n\t"
        "v_exp_f32 v89, v89\n\t"
        "v_exp_f32 v90, v90\n\t"
        "v_exp_f32 v91, v91\n\t"
        "v_cvt_pk_bf16_f32 v154, v84, v85\n\t"
        "v_cvt_pk_bf16_f32 v155, v86, v87\n\t"
        "v_mfma_f32_32x32x16_bf16 v[0:15], v[220:223], v[148:151], v[0:15]\n\t"
        "v_exp_f32 v92, v92\n\t"
        "v_exp_f32 v93, v93\n\t"
        "v_exp_f32 v94, v94\n\t"
        "v_exp_f32 v95, v95\n\t"
        "v_cvt_pk_bf16_f32 v156, v88, v89\n\t"
        "v_cvt_pk_bf16_f32 v157, v90, v91\n\t"
        "v_cvt_pk_bf16_f32 v158, v92, v93\n\t"
        "v_cvt_pk_bf16_f32 v159, v94, v95\n\t"
        "s_cbranch_scc1 TAIL%=\n\t"
        "v_mfma_f32_32x32x16_bf16 v[80:95], v[208:211], v[136:139], v[96:111]\n\t"
        "v_exp_f32 v64, v64\n\t"
        "v_exp_f32 v65, v65\n\t"
        "v_exp_f32 v66, v66\n\t"
        "v_exp_f32 v67, v67\n\t"
        "v_mfma_f32_32x32x16_bf16 v[80:95], v[212:215], v[140:143], v[80:95]\n\t"
        "v_exp_f32 v68, v68\n\t"
        "v_exp_f32 v69, v69\n\t"
        "v_exp_f32 v70, v70\n\t"
        "v_exp_f32 v71, v71\n\t"
        "v_cvt_pk_bf16_f32 v144, v64, v65\n\t"
        "v_cvt_pk_bf16_f32 v145, v66, v67\n\t"
        "v_mfma_f32_32x32x16_bf16 v[16:31], v[216:219], v[152:155], v[16:31]\n\t"
        "v_exp_f32 v72, v72\n\t"
        "v_exp_f32 v73, v73\n\t"
        "v_exp_f32 v74, v74\n\t"
        "v_exp_f32 v75, v75\n\t"
        "v_cvt_pk_bf16_f32 v146, v68, v69\n\t"
        "v_cvt_pk_bf16_f32 v147, v70, v71\n\t"
        "v_mfma_f32_32x32x16_bf16 v[16:31], v[220:223], v[156:159], v[16:31]\n\t"
        "v_exp_f32 v76, v76\n\t"
        "v_exp_f32 v77, v77\n\t"
        "v_exp_f32 v78, v78\n\t"
        "v_exp_f32 v79, v79\n\t"
        "v_cvt_pk_bf16_f32 v148, v72, v73\n\t"
        "v_cvt_pk_bf16_f32 v149, v74, v75\n\t"
        "v_cvt_pk_bf16_f32 v150, v76, v77\n\t"
        "v_cvt_pk_bf16_f32 v151, v78, v79\n\t"
        "s_waitcnt vmcnt(8)\n\t"
        "v_mfma_f32_32x32x16_bf16 v[64:79], v[160:163], v[112:115], v[96:111]\n\t"
        "v_exp_f32 v80, v80\n\t"
        "v_exp_f32 v81, v81\n\t"
        "v_exp_f32 v82, v82\n\t"
        "v_exp_f32 v83, v83\n\t"
        "v_mfma_f32_32x32x16_bf16 v[64:79], v[164:167], v[116:119], v[64:79]\n\t"
        "v_exp_f32 v84, v84\n\t"
        "v_exp_f32 v85, v85\n\t"
        "v_exp_f32 v86, v86\n\t"
        "v_exp_f32 v87, v87\n\t"
        "v_cvt_pk_bf16_f32 v152, v80, v81\n\t"
        "v_cvt_pk_bf16_f32 v153, v82, v83\n\t"
        "v_mfma_f32_32x32x16_bf16 v[32:47], v[216:219], v[144:147], v[32:47]\n\t"
        "v_exp_f32 v88, v88\n\t"
        "v_exp_f32 v89, v89\n\t"
        "v_exp_f32 v90, v90\n\t"
        "v_exp_f32 v91, v91\n\t"
        "v_cvt_pk_bf16_f32 v154, v84, v85\n\t"
        "v_cvt_pk_bf16_f32 v155, v86, v87\n\t"
        "v_mfma_f32_32x32x16_bf16 v[32:47], v[220:223], v[148:151], v[32:47]\n\t"
        "v_exp_f32 v92, v92\n\t"
        "v_exp_f32 v93, v93\n\t"
        "v_exp_f32 v94, v94\n\t"
        "v_exp_f32 v95, v95\n\t"
        "v_cvt_pk_bf16_f32 v156, v88, v89\n\t"
        "v_cvt_pk_bf16_f32 v157, v90, v91\n\t"
        "v_cvt_pk_bf16_f32 v158, v92, v93\n\t"
        "v_cvt_pk_bf16_f32 v159, v94, v95\n\t"
        "s_branch LOOP%=\n\t"
        "TAIL%=:\n\t"
        "v_mfma_f32_32x32x16_bf16 v[80:95], v[208:211], v[136:139], v[96:111]\n\t"
        "v_exp_f32 v64, v64\n\t"
        "v_exp_f32 v65, v65\n\t"
        "v_exp_f32 v66, v66\n\t"
        "v_exp_f32 v67, v67\n\t"
        "v_mfma_f32_32x32x16_bf16 v[80:95], v[212:215], v[140:143], v[80:95]\n\t"
        "ds_write_b128 v240, v[0:3] offset:0\n\t"
        "ds_write_b128 v240, v[4:7] offset:1024\n\t"
        "ds_write_b128 v240, v[8:11] offset:2048\n\t"
        "ds_write_b128 v240, v[12:15] offset:3072\n\t"
        "v_exp_f32 v68, v68\n\t"
        "v_exp_f32 v69, v69\n\t"
        "v_exp_f32 v70, v70\n\t"
        "v_exp_f32 v71, v71\n\t"
        "v_cvt_pk_bf16_f32 v144, v64, v65\n\t"
        "v_cvt_pk_bf16_f32 v145, v66, v67\n\t"
        "v_mfma_f32_32x32x16_bf16 v[16:31], v[216:219], v[152:155], v[16:31]\n\t"
        "v_exp_f32 v72, v72\n\t"
        "v_exp_f32 v73, v73\n\t"
        "v_exp_f32 v74, v74\n\t"
        "v_exp_f32 v75, v75\n\t"
        "v_cvt_pk_bf16_f32 v146, v68, v69\n\t"
        "v_cvt_pk_bf16_f32 v147, v70, v71\n\t"
        "v_mfma_f32_32x32x16_bf16 v[16:31], v[220:223], v[156:159], v[16:31]\n\t"
        "v_exp_f32 v76, v76\n\t"
        "v_exp_f32 v77, v77\n\t"
        "v_exp_f32 v78, v78\n\t"
        "v_exp_f32 v79, v79\n\t"
        "v_cvt_pk_bf16_f32 v148, v72, v73\n\t"
        "v_cvt_pk_bf16_f32 v149, v74, v75\n\t"
        "v_cvt_pk_bf16_f32 v150, v76, v77\n\t"
        "v_cvt_pk_bf16_f32 v151, v78, v79\n\t"
        "v_exp_f32 v80, v80\n\t"
        "v_exp_f32 v81, v81\n\t"
        "v_exp_f32 v82, v82\n\t"
        "v_exp_f32 v83, v83\n\t"
        "ds_write_b128 v240, v[16:19] offset:4096\n\t"
        "ds_write_b128 v240, v[20:23] offset:5120\n\t"
        "ds_write_b128 v240, v[24:27] offset:6144\n\t"
        "ds_write_b128 v240, v[28:31] offset:7168\n\t"
        "v_exp_f32 v84, v84\n\t"
        "v_exp_f32 v85, v85\n\t"
        "v_exp_f32 v86, v86\n\t"
        "v_exp_f32 v87, v87\n\t"
        "v_cvt_pk_bf16_f32 v152, v80, v81\n\t"
        "v_cvt_pk_bf16_f32 v153, v82, v83\n\t"
        "v_mfma_f32_32x32x16_bf16 v[32:47], v[216:219], v[144:147], v[32:47]\n\t"
        "v_exp_f32 v88, v88\n\t"
        "v_exp_f32 v89, v89\n\t"
        "v_exp_f32 v90, v90\n\t"
        "v_exp_f32 v91, v91\n\t"
        "v_cvt_pk_bf16_f32 v154, v84, v85\n\t"
        "v_cvt_pk_bf16_f32 v155, v86, v87\n\t"
        "v_mfma_f32_32x32x16_bf16 v[32:47], v[220:223], v[148:151], v[32:47]\n\t"
        "v_exp_f32 v92, v92\n\t"
        "v_exp_f32 v93, v93\n\t"
        "v_exp_f32 v94, v94\n\t"
        "v_exp_f32 v95, v95\n\t"
        "v_cvt_pk_bf16_f32 v156, v88, v89\n\t"
        "v_cvt_pk_bf16_f32 v157, v90, v91\n\t"
        "v_cvt_pk_bf16_f32 v158, v92, v93\n\t"
        "v_cvt_pk_bf16_f32 v159, v94, v95\n\t"
        "v_mfma_f32_32x32x16_bf16 v[48:63], v[216:219], v[152:155], v[48:63]\n\t"
        "v_mfma_f32_32x32x16_bf16 v[48:63], v[220:223], v[156:159], v[48:63]\n\t"
        "s_nop 3\n\t"
        "ds_write_b128 v240, v[32:35] offset:8192\n\t"
        "ds_write_b128 v240, v[36:39] offset:9216\n\t"
        "ds_write_b128 v240, v[40:43] offset:10240\n\t"
        "ds_write_b128 v240, v[44:47] offset:11264\n\t"
        "s_waitcnt vmcnt(0) lgkmcnt(0)\n\t"
        "s_barrier\n\t"
        "s_nop 7\n\t"
        : "={v[48:63]}"(acc), [sb] "+s"(sb), [cnt] "+s"(cnt), "+{v236}"(q2a), "+{v237}"(kba), "+{v238}"(ordn), "={v[136:139]}"(qo0), "={v[140:143]}"(qo1)
        : "{v224}"(vok + 0 * TS), "{v225}"(vov + 0 * TS), "{v226}"(vov + 2 * VROW + 0 * TS), "{v227}"(vok + 1 * TS), "{v228}"(vov + 1 * TS), "{v229}"(vov + 2 * VROW + 1 * TS), "{v230}"(vok + 2 * TS), "{v231}"(vov + 2 * TS), "{v232}"(vov + 2 * VROW + 2 * TS), "{v233}"(vok + 3 * TS), "{v234}"(vov + 3 * TS), "{v235}"(vov + 2 * VROW + 3 * TS),
          "{v240}"(puta), "{v241}"(qxa[0]), "{v242}"(qxa[1]), "{v243}"(qxa[2]), "{v244}"(qxa[3]), [rs] "s"(rsKV)
        : "v0", "v1", "v2", "v3", "v4", "v5", "v6", "v7", "v8", "v9", "v10", "v11", "v12", "v13", "v14", "v15", "v16", "v17", "v18", "v19", "v20", "v21", "v22", "v23", "v24", "v25", "v26", "v27", "v28", "v29", "v30", "v31", "v32", "v33", "v34", "v35", "v36", "v37", "v38", "v39", "v40", "v41", "v42", "v43", "v44", "v45", "v46", "v47", "v64", "v65", "v66", "v67", "v68", "v69", "v70", "v71", "v72", "v73", "v74", "v75", "v76", "v77", "v78", "v79", "v80", "v81", "v82", "v83", "v84", "v85", "v86", "v87", "v88", "v89", "v90", "v91", "v92", "v93", "v94", "v95", "v96", "v97", "v98", "v99", "v100", "v101", "v102", "v103", "v104", "v105", "v106", "v107", "v108", "v109", "v110", "v111", "v112", "v113", "v114", "v115", "v116", "v117", "v118", "v119", "v120", "v121", "v122", "v123", "v124", "v125", "v126", "v127", "v128", "v129", "v130", "v131", "v132", "v133", "v134", "v135", "v144", "v145", "v146", "v147", "v148", "v149", "v150", "v151", "v152", "v153", "v154", "v155", "v156", "v157", "v158", "v159", "v160", "v161", "v162", "v163", "v164", "v165", "v166", "v167", "v168", "v169", "v170", "v171", "v172", "v173", "v174", "v175", "v176", "v177", "v178", "v179", "v180", "v181", "v182", "v183", "v184", "v185", "v186", "v187", "v188", "v189", "v190", "v191", "v192", "v193", "v194", "v195", "v196", "v197", "v198", "v199", "v200", "v201", "v202", "v203", "v204", "v205", "v206", "v207", "v208", "v209", "v210", "v211", "v212", "v213", "v214", "v215", "v216", "v217", "v218", "v219", "v220", "v221", "v222", "v223", "v239", "scc", "memory");
    }
    STAMP(7);
#pragma unroll
    for (int s = 0; s < 3; ++s) {
        LAS const char* sp = lds + L_CB + (((w - 1 - s) & 3) * 3 + s) * 4096 + lane * 16;
#pragma unroll
        for (int g = 0; g < 4; ++g) {
            const f32x4 v = *(LAS const f32x4*)(sp + g * 1024);
            acc[4 * g] += v[0]; acc[4 * g + 1] += v[1]; acc[4 * g + 2] += v[2]; acc[4 * g + 3] += v[3];
        }
    }
    float l = half_hi(acc[12]);
    if (__builtin_expect(__any(!(l >= L_MIN && l <= L_MAX)), 0)) {
        const f32x16 zero = {};
        float m = -__builtin_inff();
#pragma unroll 1
        for (int t = 0; t < 64; ++t) {
            f32x16 s = MFMA(asbf(ld16_wt(rsKV, vok, t * TS)), asbf(qo0), zero);
            s = MFMA(asbf(ld16_wt(rsKV, vok + 1024, t * TS)), asbf(qo1), s);
#pragma unroll
            for (int i = 0; i < 16; ++i) m = __builtin_fmaxf(m, s[i]);
        }
        m = __builtin_fmaxf(half_lo(m), half_hi(m));
        acc = zero;
#pragma unroll 1
        for (int t = 0; t < 64; ++t) {
            f32x16 s = MFMA(asbf(ld16_wt(rsKV, vok, t * TS)), asbf(qo0), zero);
            s = MFMA(asbf(ld16_wt(rsKV, vok + 1024, t * TS)), asbf(qo1), s);
#pragma unroll
            for (int i = 0; i < 16; ++i) s[i] = __builtin_amdgcn_exp2f(s[i] - m);
            acc = MFMA(asbf(ld16_wt(rsKV, vov, t * TS)), asbf(pack8(s, 0)), acc);
            acc = MFMA(asbf(ld16_wt(rsKV, vov + 2 * VROW, t * TS)), asbf(pack8(s, 1)), acc);
        }
        l = half_hi(acc[12]);
    }
    {
        const float rl = __builtin_amdgcn_rcpf(l);
#pragma unroll
        for (int i = 0; i < 16; ++i) acc[i] = (i >= 12 && h) ? 0.f : acc[i] * rl;
        const unsigned so = (kt * NH + hd) * 2048;
        u32x4 p0 = pack8(acc, 0), p1 = pack8(acc, 1);
#pragma unroll
        for (int i = 0; i < 4; ++i) { p0[i] = min(p0[i], POISON - 1); p1[i] = min(p1[i], POISON - 1); }
        st16_wt(p0, rsOI, lane * 16, so);
        st16_wt(p1, rsOI, lane * 16 + 1024, so);
        if (lane < 4) __hip_atomic_store((GAS u64*)(wsg + WS_F + F_O + (qt * 16 + lane * 4 + w) * 2048 + hd * 128), (u64)tag, RLX_AGENT);
    }
    STAMP(8);
    BAR();

    bf16x8 wf[32];
    {
        LAS const char* wb = lds + L_WO + (4 * h + q4) * 256 + ((w ^ q4) * 64) + g16 * 32 + p4 * 8;
#pragma unroll
        for (int k = 0; k < 32; ++k) {
            const int ro = ((k >> 1) * DH + (k & 1) * 16) * 256;
            wf[k] = cat(vtr(wb + ro), vtr(wb + ro + 2048 - (k == 31 ? h * 1024 : 0)));
        }
    }
    STAMP(10);
    BAR();
    STAMP(11);
    if (cg < 3 || w < 2) {
        f32x16 c = {};
        bf16x8 of[3][4];
#define RDO(i) do { _Pragma("unroll") for (int j = 0; j < 4; ++j) of[(i) % 3][j] = *(LAS const bf16x8*)(lds + L_CB + ((i) * 4 + j) * 1024 + lane * 16); } while (0)
        RDO(0); RDO(1); SBAR();
#pragma unroll
        for (int i = 0; i < 8; ++i) {
            if (i + 2 < 8) RDO(i + 2);
            SBAR();
#pragma unroll
            for (int j = 0; j < 4; ++j) c = MFMA(wf[i * 4 + j], of[i % 3][j], c);
            SBAR();
        }
#undef RDO
        STAMP(12);
        char* orow = out + (size_t)(q32 * 32 + r) * (DM * 2) + cg * 256 + w * 64 + h * 16;
#pragma unroll
        for (int g = 0; g < 4; g += 2) {
            const unsigned x0 = cvtpk(c[4 * g], c[4 * g + 1]), x1 = cvtpk(c[4 * g + 2], c[4 * g + 3]);
            const unsigned y0 = cvtpk(c[4 * g + 4], c[4 * g + 5]), y1 = cvtpk(c[4 * g + 6], c[4 * g + 7]);
            auto s0 = __builtin_amdgcn_permlane32_swap(x0, y0, false, false);
            auto s1 = __builtin_amdgcn_permlane32_swap(x1, y1, false, false);
            *(u32x4*)(orow + g * 16) = (u32x4){s0[0], s1[0], s0[1], s1[1]};
        }
    }
    STAMP(13);
    STAMP_FLUSH();
}

extern "C" void kernel_launch(void* const* d_in, const int* in_sizes, int n_in, void* d_out, int out_size, void* d_ws, size_t ws_size,
                              hipStream_t stream) {
    (void)hipFuncSetAttribute((const void*)mha_kernel, hipFuncAttributeMaxDynamicSharedMemorySize, LDS_BYTES);
    mha_kernel<<<256, 512, LDS_BYTES, stream>>>((const char*)d_in[0], (const char*)d_in[1], (const char*)d_in[2], (char*)d_out, (char*)d_ws + (size_t)64 * 1048576);
}
```

```cpp
#include <hip/hip_runtime.h>
#include <hip/hip_bf16.h>
#include <stdint.h>

typedef short bf16x8 __attribute__((ext_vector_type(8)));
typedef short s16x4 __attribute__((ext_vector_type(4)));
typedef float f32x16 __attribute__((ext_vector_type(16)));
typedef float f32x4 __attribute__((ext_vector_type(4)));
typedef float f32x2 __attribute__((ext_vector_type(2)));
typedef unsigned u32x4 __attribute__((ext_vector_type(4)));
typedef __bf16 bf16x2_t __attribute__((ext_vector_type(2)));
typedef short v4i16_t __attribute__((ext_vector_type(4)));
typedef unsigned long long u64;
#define LAS __attribute__((address_space(3)))
#define GAS __attribute__((address_space(1)))
#define DI __device__ __forceinline__
#define MFMA(a, b, c) __builtin_amdgcn_mfma_f32_32x32x16_bf16((a), (b), (c), 0, 0, 0)
#define SBAR() __builtin_amdgcn_sched_barrier(0)
#define RLX_AGENT __ATOMIC_RELAXED, __HIP_MEMORY_SCOPE_AGENT
#define BAR() asm volatile("s_waitcnt lgkmcnt(0)\n\ts_barrier" ::: "memory")

#define PRETOUCH 2
#define OFLAG 0
#define MAPPING 1
#define MAP_HD(b) (((b) & 7) * 2 + (((b) >> 3) & 1))
#define MAP_QT(b) ((b) >> 4)
constexpr int SEQ = 2048, DM = 448, NH = 16, DH = 28, N3 = 1344;
constexpr float QSCALE = 0.18898223650461362f * 1.4426950408889634f;
constexpr float L_MIN = 9.094947017729282e-13f, L_MAX = 1.099511627776e12f;
constexpr unsigned VROW = 464 * 16, TS = 4 * VROW;
constexpr unsigned WS_K = 0, WS_V = 2u << 20, WS_O = 4u << 20, WS_F = 6u << 20;
constexpr unsigned POISON = 0xffffffffu;
constexpr unsigned F_G = 0, F_R = 4096, F_P = 8192, F_KV = 131072, F_O = 262144;
constexpr unsigned L_WQ = 0, L_RING = 28672, STG = 24576;
constexpr unsigned L_QX = 131072, L_Q2 = L_QX + 8192, L_GB = L_Q2 + 512;
constexpr unsigned L_WO = 0, L_CB = 114688;
constexpr unsigned LDS_BYTES = 163840;

#define STAMP_DECL
#define STAMP(i)
#define STAMP_FLUSH()

DI unsigned rfl(unsigned x) { return __builtin_amdgcn_readfirstlane(x); }
DI unsigned cvtpk(float lo, float hi) { f32x2 v = {lo, hi}; bf16x2_t b = __builtin_convertvector(v, bf16x2_t); return __builtin_bit_cast(unsigned, b); }
DI u32x4 pack8(const f32x16& x, int s) {
    return (u32x4){cvtpk(x[8 * s], x[8 * s + 1]), cvtpk(x[8 * s + 2], x[8 * s + 3]), cvtpk(x[8 * s + 4], x[8 * s + 5]), cvtpk(x[8 * s + 6], x[8 * s + 7])};
}
DI bf16x8 asbf(u32x4 x) { return __builtin_bit_cast(bf16x8, x); }
DI s16x4 vtr(LAS const char* p) { return __builtin_bit_cast(s16x4, __builtin_amdgcn_ds_read_tr16_b64_v4i16((LAS v4i16_t*)p)); }
DI bf16x8 cat(s16x4 lo, s16x4 hi) { return __builtin_shufflevector(lo, hi, 0, 1, 2, 3, 4, 5, 6, 7); }
DI float half_lo(float x) { auto r = __builtin_amdgcn_permlane32_swap(__float_as_uint(x), __float_as_uint(x), false, false); return __uint_as_float(r[0]); }
DI float half_hi(float x) { auto r = __builtin_amdgcn_permlane32_swap(__float_as_uint(x), __float_as_uint(x), false, false); return __uint_as_float(r[1]); }
DI unsigned wave_max_u(unsigned x) {
#define DPPMAX(ctrl, rm) x = max(x, (unsigned)__builtin_amdgcn_update_dpp((int)x, (int)x, ctrl, rm, 0xf, false))
    DPPMAX(0x111, 0xf); DPPMAX(0x112, 0xf); DPPMAX(0x114, 0xf); DPPMAX(0x118, 0xf); DPPMAX(0x142, 0xa); DPPMAX(0x143, 0xc);
#undef DPPMAX
    return (unsigned)__builtin_amdgcn_readlane((int)x, 63);
}
DI void dma16(unsigned lds_dst, unsigned voff, __amdgpu_buffer_rsrc_t rs, unsigned soff) {
    asm volatile("s_mov_b32 m0, %0\n\ts_nop 0\n\tbuffer_load_dwordx4 %1, %2, %3 offen lds" ::"s"(lds_dst), "v"(voff), "s"(rs), "s"(soff) : "m0", "memory");
}
DI void st16_wt(u32x4 v, __amdgpu_buffer_rsrc_t rs, unsigned voff, unsigned soff) { __builtin_amdgcn_raw_buffer_store_b128(v, rs, voff, soff, 16); }
DI u32x4 ld16_wt(__amdgpu_buffer_rsrc_t rs, unsigned voff, unsigned soff) { return __builtin_amdgcn_raw_buffer_load_b128(rs, voff, soff, 16); }
DI __amdgpu_buffer_rsrc_t mkrs(const void* p, unsigned bytes) { return __builtin_amdgcn_make_buffer_rsrc((void*)p, (short)0, (int)bytes, 0x00020000); }
DI unsigned poll_tags(const GAS u64* g, unsigned tag) {
    u64 v = __hip_atomic_load(g, RLX_AGENT);
#pragma nounroll
    for (unsigned spins = 0; spins < (1u << 16); ++spins) {
        if (__all((unsigned)v == tag)) break;
        if ((unsigned)v != tag) v = __hip_atomic_load(g, RLX_AGENT);
    }
    __builtin_amdgcn_fence(__ATOMIC_ACQUIRE, "wavefront");
    return (unsigned)(v >> 32);
}
DI unsigned touch(const GAS char* p) { unsigned t; asm volatile("global_load_dword %0, %1, off" : "=v"(t) : "v"(p) : "memory"); return t; }

template <bool ISK>
DI void kv_gemm(LAS char* lds, unsigned w, int lane, bf16x8 (&xf)[7][4], f32x16& a0, f32x16& a1) {
    const int r = lane & 31, h = lane >> 5, i16 = lane & 15, q4 = i16 >> 2, p4 = i16 & 3, g16 = (lane >> 4) & 1;
    const int pc = ISK ? (((p4 & 1) << 1) | (p4 >> 1)) : p4;
    LAS const char* xb = lds + L_RING + w * 4096 + r * 128;
    LAS const char* wb0 = lds + L_RING + 16384 + (8 * h + q4) * 128 + ((q4 >> 1) * 64) + g16 * 32 + pc * 8;
    LAS const char* wb1 = lds + L_RING + 16384 + (8 * h + q4) * 128 + (((q4 >> 1) ^ 1) * 64) + g16 * 32 + pc * 8;
    const int xs = (r >> 1) & 7;
    bf16x8 wf[2][4][2];
#define RD(i) do { const int so = ((i) % 5) * STG; _Pragma("unroll") for (int ks = 0; ks < 4; ++ks) {                          \
        xf[i][ks] = *(LAS const bf16x8*)(xb + so + (((2 * ks + h) ^ xs) * 16));                                                \
        wf[(i) & 1][ks][0] = cat(vtr(wb0 + so + ks * 2048), vtr(wb0 + so + ks * 2048 + 512));                                  \
        wf[(i) & 1][ks][1] = cat(vtr(wb1 + so + ks * 2048), vtr(wb1 + so + ks * 2048 + 512)); } } while (0)
#define MM(i) do { _Pragma("unroll") for (int ks = 0; ks < 4; ++ks) {                                                          \
        if (ISK) { a0 = MFMA(wf[(i) & 1][ks][0], xf[i][ks], a0); a1 = MFMA(wf[(i) & 1][ks][1], xf[i][ks], a1); }               \
        else     { a0 = MFMA(xf[i][ks], wf[(i) & 1][ks][0], a0); a1 = MFMA(xf[i][ks], wf[(i) & 1][ks][1], a1); } } } while (0)
    BAR(); RD(0);
#pragma unroll
    for (int i = 0; i < 5; ++i) { BAR(); RD(i + 1); SBAR(); MM(i); SBAR(); }
    MM(5); SBAR();
    BAR(); RD(6); MM(6);
#undef RD
#undef MM
}

DI void stage_dma(unsigned mw, unsigned mx, unsigned sw, unsigned sx, unsigned wvo, unsigned xvo0, unsigned xvo1, __amdgpu_buffer_rsrc_t rsW,
                  __amdgpu_buffer_rsrc_t rsX) {
    unsigned t;
    asm volatile("s_mov_b32 m0, %1\n\ts_add_u32 %0, %3, 21504\n\tbuffer_load_dwordx4 %5, %8, %3 offen lds\n\t"
                 "s_add_u32 m0, m0, 1024\n\ts_nop 0\n\tbuffer_load_dwordx4 %5, %8, %0 offen lds\n\t"
                 "s_mov_b32 m0, %2\n\ts_add_u32 %0, %4, 7168\n\tbuffer_load_dwordx4 %6, %9, %4 offen lds\n\t"
                 "s_add_u32 m0, m0, 1024\n\ts_nop 0\n\tbuffer_load_dwordx4 %7, %9, %0 offen lds\n\t"
                 "s_add_u32 m0, m0, 1024\n\ts_add_u32 %0, %4, 14336\n\tbuffer_load_dwordx4 %6, %9, %0 offen lds\n\t"
                 "s_add_u32 m0, m0, 1024\n\ts_add_u32 %0, %4, 21504\n\tbuffer_load_dwordx4 %7, %9, %0 offen lds"
                 : "=&s"(t) : "s"(mw), "s"(mx), "s"(sw), "s"(sx), "v"(wvo), "v"(xvo0), "v"(xvo1), "s"(rsW), "s"(rsX) : "m0", "scc", "memory");
}

__global__ __launch_bounds__(512, 2) void mha_kernel(const char* __restrict__ x, const char* __restrict__ wqkv, const char* __restrict__ wo,
                                                     char* __restrict__ out, char* __restrict__ ws) {
    extern __shared__ __attribute__((aligned(16))) char lds_[];
    LAS char* lds = (LAS char*)lds_;
    STAMP_DECL STAMP(0);
    const unsigned wv = rfl(threadIdx.x >> 6), w = wv & 3;
    const unsigned lds0 = (unsigned)(uintptr_t)lds_;
    GAS char* wsg = (GAS char*)ws;
#define DECODE()                                                                                                              \
    unsigned b = blockIdx.x, lane = threadIdx.x & 63;                                                                        \
    asm volatile("" : "+s"(b)); asm volatile("" : "+v"(lane));                                                                \
    const unsigned hd = MAP_HD(b), qt = MAP_QT(b);                                                                            \
    const unsigned ch = hd < 14 ? hd : hd - 2;         \
    const unsigned rot = (hd + 2 * qt) % 7;
#define DECODE2()                                                                                                             \
    const bool isK = ch < 7;                                                                                                  \
    const unsigned g0 = (7 * hd) >> 1;                                                  \
    const unsigned cg = (b >> 3) & 3, q32 = (b & 7) * 8 + (b >> 5);     \
    unsigned kbi[7];                                                                                                          \
    _Pragma("unroll") for (int i = 0; i < 7; ++i) { const unsigned k = rot + i; kbi[i] = k >= 7 ? k - 7 : k; }               \
    const __amdgpu_buffer_rsrc_t rsOI = mkrs(ws + WS_O, 2u << 20);

    if (__builtin_expect(wv >= 4, 1)) {
        DECODE();
        const unsigned xrow = qt * 128 + w * 32 + (lane >> 3);
        const unsigned xvo0 = xrow * (DM * 2) + (((lane & 7) ^ (lane >> 4)) * 16), xvo1 = xvo0 ^ 64;
        const unsigned wvo = (16 * w + (lane >> 3)) * (N3 * 2) + 896 + ch * 128 + (((lane & 7) ^ (((lane >> 4) & 1) << 2)) * 16);
        const __amdgpu_buffer_rsrc_t rsW = mkrs(wqkv, DM * N3 * 2), rsX = mkrs(x, SEQ * DM * 2);
        STAMP(14);
        stage_dma(lds0 + L_RING + 16384 + w * 2048, lds0 + L_RING + w * 4096, rot * (64 * N3 * 2), rot * 128, wvo, xvo0, xvo1, rsW, rsX);
        DECODE2();
        const __amdgpu_buffer_rsrc_t rsF = mkrs(ws + WS_F, 4096);
#define STAGE(i) stage_dma(lds0 + L_RING + ((i) % 5) * STG + 16384 + w * 2048, lds0 + L_RING + ((i) % 5) * STG + w * 4096, kbi[i] * (64 * N3 * 2), kbi[i] * 128, \
                           wvo, xvo0, xvo1, rsW, rsX)
#define VBAR(NV) asm volatile("s_waitcnt vmcnt(" #NV ")\n\ts_barrier" ::: "memory")
        STAGE(1); STAGE(2);
        STAMP(1);
        VBAR(12); STAGE(3);
        STAMP(2);
        VBAR(12); STAGE(4);
        VBAR(12); STAGE(5);
        VBAR(12); STAGE(6);
        VBAR(12);
        unsigned gen;
        { const unsigned vo = F_G; asm volatile("buffer_load_dword %0, %1, %2, 0 offen sc1" : "=v"(gen) : "v"(vo), "s"(rsF) : "memory"); }
        {
            const unsigned qvo = (16 * w + (lane >> 2)) * (N3 * 2) + g0 * 16 + (lane & 3) * 16;
#pragma unroll
            for (int j = 0; j < 7; ++j) dma16(lds0 + L_WQ + (16 * w + 64 * kbi[j]) * 64, qvo, rsW, kbi[j] * 64 * (N3 * 2));
        }
        VBAR(14); VBAR(8);
        STAMP(3);
        asm volatile("s_waitcnt vmcnt(7)" : "+v"(gen)::"memory");
        unsigned tag = rfl(gen);
        { const unsigned t = (tag ^ (tag >> 15)) * 0x2c1b3c6du + 0x9e3779b9u; tag = (t == tag) ? t + 1 : t; }
        const unsigned hh = lane & 15, wh = (lane >> 4) & 1;
        const unsigned c = isK ? (((7 * hh >> 1) + 3 * wh) >> 3) : 7 + ((28 * hh + 27 * wh) >> 6);
        GAS u64* fp = (GAS u64*)(wsg + WS_F + (lane < 32 ? F_KV + ((hh * 4 + (isK ? 0 : 2) + wh) * 16 + qt) * 128 : F_R + (qt * 16 + hd) * 8));
        const bool fon = w == 0 && ((lane < 32 && c == hd) || lane == 32);
        if (w == 0 && lane == 0) *(LAS unsigned*)(lds + L_GB + 20) = tag;
        asm volatile("s_waitcnt lgkmcnt(0)\n\ts_barrier" : "+v"(fp)::"memory");
        if (w == 0) {
            const u32x4 gb = *(LAS const u32x4*)(lds + L_GB);
            if (fon) __hip_atomic_store(fp, ((u64)max(max(gb[0], gb[1]), max(gb[2], gb[3])) << 32) | tag, RLX_AGENT);
            asm volatile("s_waitcnt vmcnt(1)" ::: "memory");
        } else {
            asm volatile("s_waitcnt vmcnt(0)" ::: "memory");
        }
        STAMP(4);
        asm volatile("s_waitcnt lgkmcnt(0)\n\ts_barrier" ::: "memory");
        LAS unsigned* ord = (LAS unsigned*)(lds + L_CB + w * 12288 + (w == 2 ? 2048 : 0));
        if (lane < 5) ord[lane] = lane == 4 ? 0x40000000u : 0xffffffffu;
        {
            const unsigned tt = w + 4 * ((((lane >> 2) & 3) + (qt >> 2)) & 3);
            const GAS u64* fl = (const GAS u64*)(wsg + WS_F + F_KV + hd * 8192 + ((lane & 3) * 16 + tt) * 128);
            u64 v = __hip_atomic_load(fl, RLX_AGENT);
            unsigned done = 0;
#pragma nounroll
            for (unsigned spins = 0; done < 4; ++spins) {
                const unsigned miss = spins < (1u << 16) ? (unsigned)__ballot((unsigned)v != tag) & 0xffffu : 0u;
                const unsigned n = miss ? __builtin_ctz(miss) >> 2 : 4;
                if (n > done) {
                    __builtin_amdgcn_fence(__ATOMIC_ACQUIRE, "wavefront");
                    if (lane < 4 * n && (lane & 3) == 0) ord[lane >> 2] = tt * (4 * TS);
                    if (done == 0) {
                        const unsigned g = (unsigned)(v >> 32);
                        if (w == (qt & 3) && lane == 0) *(LAS unsigned*)(lds + L_GB + 16) = max(g, (unsigned)__builtin_amdgcn_readlane((int)g, 1));
                        STAMP(5);
                        BAR();
                        STAMP(6);
                        asm volatile("s_barrier" ::: "memory");
                    }
                    done = n;
                }
                if ((unsigned)v != tag) v = __hip_atomic_load(fl, RLX_AGENT);
            }
        }
        st16_wt((u32x4){POISON, POISON, POISON, POISON}, rsOI, lane * 16, ((qt * 4 + w) * NH + hd) * 2048);
        st16_wt((u32x4){POISON, POISON, POISON, POISON}, rsOI, lane * 16 + 1024, ((qt * 4 + w) * NH + hd) * 2048);
        asm volatile("s_waitcnt vmcnt(0)" ::: "memory");
        if (lane == 0) __hip_atomic_store((GAS u64*)(wsg + WS_F + F_P + ((qt * 4 + w) * 16 + hd) * 8), (u64)tag, RLX_AGENT);
        if (w == 1 && b == 0) {
            const GAS u64* fr = (const GAS u64*)(wsg + WS_F + F_R) + lane;
#pragma nounroll
            for (unsigned spins = 0; spins < (1u << 16); ++spins) {
                const u64 v0 = __hip_atomic_load(fr, RLX_AGENT), v1 = __hip_atomic_load(fr + 64, RLX_AGENT);
                const u64 v2 = __hip_atomic_load(fr + 128, RLX_AGENT), v3 = __hip_atomic_load(fr + 192, RLX_AGENT);
                if (__all((unsigned)v0 == tag && (unsigned)v1 == tag && (unsigned)v2 == tag && (unsigned)v3 == tag)) break;
                __builtin_amdgcn_s_sleep(4);
            }
            if (lane == 0) __hip_atomic_store((GAS unsigned*)(wsg + WS_F + F_G), tag, RLX_AGENT);
        }
        {
            const __amdgpu_buffer_rsrc_t rsO = mkrs(wo, DM * DM * 2);
            const unsigned vow = (4 * w + (lane >> 4)) * (DM * 2) + cg * 256 + ((((lane >> 2) & 3) ^ (lane >> 4)) * 64) + (lane & 3) * 16;
#pragma unroll 1
            for (unsigned j = 0; j < 28; ++j) { dma16(lds0 + L_WO + (4 * j + w) * 1024, vow, rsO, j * 14336); __builtin_amdgcn_s_sleep(1); }
        }
        (void)poll_tags((const GAS u64*)(wsg + WS_F + F_P + q32 * 128) + w * 4 + (lane & 3), tag);
        VBAR(0);
        STAMP(8);
        asm volatile("s_barrier" ::: "memory");
        STAMP(9);
        {
            unsigned pend = 15;
#pragma nounroll
            for (unsigned spins = 0; pend && spins < (1u << 16); ++spins) {
                u32x4 t[8];
                asm volatile("" ::: "memory");
#pragma unroll
                for (int j = 0; j < 4; ++j)
                    if (pend & (1u << j)) {
                        t[2 * j] = ld16_wt(rsOI, lane * 16 + j * 2048, q32 * (NH * 2048) + w * 8192);
                        t[2 * j + 1] = ld16_wt(rsOI, lane * 16 + j * 2048 + 1024, q32 * (NH * 2048) + w * 8192);
                    }
#pragma unroll
                for (int j = 0; j < 4; ++j)
                    if (pend & (1u << j)) {
                        const u32x4 u = t[2 * j], v = t[2 * j + 1];
                        *(LAS u32x4*)(lds + L_CB + (w * 8 + 2 * j) * 1024 + lane * 16) = u;
                        *(LAS u32x4*)(lds + L_CB + (w * 8 + 2 * j + 1) * 1024 + lane * 16) = v;
                        const unsigned m = max(max(max(u[0], u[1]), max(u[2], u[3])), max(max(v[0], v[1]), max(v[2], v[3])));
                        if (__all(m != POISON)) pend &= ~(1u << j);
                    }
            }
            STAMP(10);
        }
        BAR();
        STAMP(11);
        STAMP_FLUSH();
        return;
    }

    DECODE(); DECODE2();
    const int r = lane & 31, h = lane >> 5, i16 = lane & 15, q4 = i16 >> 2, p4 = i16 & 3, g16 = (lane >> 4) & 1;
    f32x16 a0 = {}, a1 = {};
    bf16x8 xf[7][4];
    const unsigned kt = qt * 4 + w;
    unsigned gmx = 0;
    unsigned tch1 = 0;
    {
        unsigned long long pc0;
        asm volatile("s_getpc_b64 %0" : "=s"(pc0));
        tch1 = touch((const GAS char*)(pc0 + (((b >> 3) % 3) * 4 + w) * 1024 + (lane & 7) * 128));
    }
    if (isK) {
        kv_gemm<true>(lds, w, lane, xf, a0, a1);
        STAMP(3);
        {
            const __amdgpu_buffer_rsrc_t rs = mkrs(ws + WS_K, 64 * TS);
            const unsigned vo = (8 * ch + h) * 512 + r * 16, so = kt * TS;
            st16_wt(pack8(a0, 0), rs, vo, so); st16_wt(pack8(a0, 1), rs, vo + 1024, so);
            st16_wt(pack8(a1, 0), rs, vo + 2048, so); st16_wt(pack8(a1, 1), rs, vo + 3072, so);
            float s0 = 0.f, s1 = 0.f, s2 = 0.f, s3 = 0.f;
#pragma unroll
            for (int j = 0; j < 8; ++j) { s0 += a0[j] * a0[j]; s1 += a0[8 + j] * a0[8 + j]; s2 += a1[j] * a1[j]; s3 += a1[8 + j] * a1[8 + j]; }
            gmx = max(max(__float_as_uint(s0), __float_as_uint(s1)), max(__float_as_uint(s2), __float_as_uint(s3)));
            gmx = wave_max_u(gmx);
        }
    } else {
        kv_gemm<false>(lds, w, lane, xf, a0, a1);
        STAMP(3);
        if (hd < 14) {
        const __amdgpu_buffer_rsrc_t rs = mkrs(ws + WS_V, 64 * TS);
        const unsigned n0 = 64 * (ch - 7) + r, n1 = n0 + 32, so = kt * TS;
        const unsigned v0 = h * VROW + (n0 + (n0 * 2341 >> 16)) * 16, v1 = h * VROW + (n1 + (n1 * 2341 >> 16)) * 16;
        st16_wt(pack8(a0, 0), rs, v0, so); st16_wt(pack8(a0, 1), rs, v0 + 2 * VROW, so);
        st16_wt(pack8(a1, 0), rs, v1, so); st16_wt(pack8(a1, 1), rs, v1 + 2 * VROW, so);
        const unsigned hc = (64 * (ch - 7) * 2341 >> 16) + (lane >> 2), last = 28 * hc + 27;
        if (lane < 16 && (last >> 6) == ch - 7)
            st16_wt((u32x4){0x3f803f80u, 0x3f803f80u, 0x3f803f80u, 0x3f803f80u}, rs, (lane & 3) * VROW + (29 * hc + 28) * 16, so);
        }
    }
    if (lane == 0) *(LAS unsigned*)(lds + L_GB + w * 4) = gmx;
    unsigned vok = WS_K + (g0 + h) * 512 + r * 16, vov = r <= DH ? WS_V + h * VROW + (29 * hd + r) * 16 : 0x70000000u;
    const __amdgpu_buffer_rsrc_t rsKV = mkrs(ws, 4u << 20);
    unsigned q2a = lds0 + L_Q2 + r * 16, kba = lds0 + L_GB + 16, ordn = lds0 + L_CB + w * 12288 + (w == 2 ? 2048 : 0) + 4, puta = lds0 + L_CB + w * 12288 + lane * 16;
    unsigned qxa[4];
#pragma unroll
    for (int i = 0; i < 4; ++i) qxa[i] = lds0 + L_QX + ((w + 1 + i) & 3) * 2048 + lane * 16;
    LAS const char* qb = lds + L_WQ + (8 * h + q4) * 64 + g16 * 32 + (((p4 & 1) << 1) | (p4 >> 1)) * 8;
    asm volatile("" : "+v"(vok), "+v"(vov), "+v"(q2a), "+v"(kba), "+v"(ordn), "+v"(puta), "+v"(qxa[0]), "+v"(qxa[1]), "+v"(qxa[2]), "+v"(qxa[3]), "+v"(qb));
    STAMP(1);
    asm volatile("s_waitcnt vmcnt(0) lgkmcnt(0)" : "+v"(tch1)::"memory");
    STAMP(2);
    asm volatile("s_barrier" ::: "memory");
    STAMP(4);
    asm volatile("s_barrier" ::: "memory");
    STAMP(14);
    const unsigned tag = rfl(*(LAS const unsigned*)(lds + L_GB + 20));
    f32x16 aq = {};
    {
        bf16x8 wq[3][4];
#define RDQ(i) do { LAS const char* qk = qb + kbi[i] * 4096; _Pragma("unroll") for (int ks = 0; ks < 4; ++ks)                  \
            wq[(i) % 3][ks] = cat(vtr(qk + ks * 1024), vtr(qk + ks * 1024 + 256)); } while (0)
        RDQ(0); RDQ(1); SBAR();
#pragma unroll
        for (int i = 0; i < 7; ++i) {
            if (i + 2 < 7) RDQ(i + 2);
            SBAR();
#pragma unroll
            for (int ks = 0; ks < 4; ++ks) aq = MFMA(wq[i % 3][ks], xf[i][ks], aq);
            SBAR();
        }
#undef RDQ
    }
    asm volatile("" : "+v"(aq));
    STAMP(15);
    {
        const bool zlo = (hd & 1) && !h, zhi = !(hd & 1) && h;
        float qs[4] = {0.f, 0.f, 0.f, 0.f};
#pragma unroll
        for (int i = 0; i < 16; ++i) {
            aq[i] *= QSCALE;
            if (i < 4) aq[i] = zlo ? 0.f : aq[i];
            if (i >= 12) aq[i] = zhi ? 0.f : aq[i];
            qs[i & 3] += aq[i] * aq[i];
        }
        *(LAS u32x4*)(lds + (qxa[3] - lds0)) = pack8(aq, 0);
        *(LAS u32x4*)(lds + (qxa[3] - lds0) + 1024) = pack8(aq, 1);
        float q2 = (qs[0] + qs[1]) + (qs[2] + qs[3]);
        q2 = half_lo(q2) + half_hi(q2);
        *(LAS float*)(lds + (q2a - lds0) + w * 4) = q2;
    }
    STAMP(5);

    f32x16 acc;
    u32x4 qo0, qo1;
    {
        unsigned sb = (w + (qt & 12)) * (4 * TS), cnt = 4;
        asm volatile(
        "s_setprio 2\n\t"
        "v_mov_b64 v[0:1], 0\n\t"
        "v_mov_b64 v[2:3], 0\n\t"
        "v_mov_b64 v[4:5], 0\n\t"
        "v_mov_b64 v[6:7], 0\n\t"
        "v_mov_b64 v[8:9], 0\n\t"
        "v_mov_b64 v[10:11], 0\n\t"
        "v_mov_b64 v[12:13], 0\n\t"
        "v_mov_b64 v[14:15], 0\n\t"
        "v_mov_b64 v[16:17], 0\n\t"
        "v_mov_b64 v[18:19], 0\n\t"
        "v_mov_b64 v[20:21], 0\n\t"
        "v_mov_b64 v[22:23], 0\n\t"
        "v_mov_b64 v[24:25], 0\n\t"
        "v_mov_b64 v[26:27], 0\n\t"
        "v_mov_b64 v[28:29], 0\n\t"
        "v_mov_b64 v[30:31], 0\n\t"
        "v_mov_b64 v[32:33], 0\n\t"
        "v_mov_b64 v[34:35], 0\n\t"
        "v_mov_b64 v[36:37], 0\n\t"
        "v_mov_b64 v[38:39], 0\n\t"
        "v_mov_b64 v[40:41], 0\n\t"
        "v_mov_b64 v[42:43], 0\n\t"
        "v_mov_b64 v[44:45], 0\n\t"
        "v_mov_b64 v[46:47], 0\n\t"
        "v_mov_b64 v[48:49], 0\n\t"
        "v_mov_b64 v[50:51], 0\n\t"
        "v_mov_b64 v[52:53], 0\n\t"
        "v_mov_b64 v[54:55], 0\n\t"
        "v_mov_b64 v[56:57], 0\n\t"
        "v_mov_b64 v[58:59], 0\n\t"
        "v_mov_b64 v[60:61], 0\n\t"
        "v_mov_b64 v[62:63], 0\n\t"
        "v_mov_b64 v[152:153], 0\n\t"
        "v_mov_b64 v[154:155], 0\n\t"
        "v_mov_b64 v[156:157], 0\n\t"
        "v_mov_b64 v[158:159], 0\n\t"
        "v_mov_b64 v[216:217], 0\n\t"
        "v_mov_b64 v[218:219], 0\n\t"
        "v_mov_b64 v[220:221], 0\n\t"
        "v_mov_b64 v[222:223], 0\n\t"
        "s_waitcnt lgkmcnt(0)\n\t"
        "s_barrier\n\t"
        "buffer_load_dwordx4 v[160:163], v224, %[rs], %[sb] offen sc1\n\t"
        "buffer_load_dwordx4 v[164:167], v224, %[rs], %[sb] offen offset:1024 sc1\n\t"
        "buffer_load_dwordx4 v[168:171], v225, %[rs], %[sb] offen sc1\n\t"
        "buffer_load_dwordx4 v[172:175], v226, %[rs], %[sb] offen sc1\n\t"
        "ds_read_b128 v[80:83], v236\n\t"
        "ds_read_b32 v237, v237\n\t"
        "ds_read_b128 v[112:115], v241\n\t"
        "ds_read_b128 v[116:119], v241 offset:1024\n\t"
        "ds_read_b128 v[120:123], v242\n\t"
        "ds_read_b128 v[124:127], v242 offset:1024\n\t"
        "ds_read_b128 v[128:131], v243\n\t"
        "ds_read_b128 v[132:135], v243 offset:1024\n\t"
        "ds_read_b128 v[136:139], v244\n\t"
        "ds_read_b128 v[140:143], v244 offset:1024\n\t"
        "s_waitcnt lgkmcnt(0)\n\t"
        "s_barrier\n\t"
        "v_max3_f32 v236, v80, v81, v82\n\t"
        "v_max_f32 v236, v236, v83\n\t"
        "v_mul_f32 v236, v236, v237\n\t"
        "v_sqrt_f32 v236, v236\n\t"
        "s_nop 0\n\t"
        "v_mul_f32 v96, 0xc0028f5c, v236\n\t"
        "v_mov_b32 v97, v96\n\t"
        "v_mov_b64 v[98:99], v[96:97]\n\t"
        "v_mov_b64 v[100:101], v[96:97]\n\t"
        "v_mov_b64 v[102:103], v[96:97]\n\t"
        "v_mov_b64 v[104:105], v[96:97]\n\t"
        "v_mov_b64 v[106:107], v[96:97]\n\t"
        "v_mov_b64 v[108:109], v[96:97]\n\t"
        "v_mov_b64 v[110:111], v[96:97]\n\t"
        "buffer_load_dwordx4 v[176:179], v227, %[rs], %[sb] offen sc1\n\t"
        "buffer_load_dwordx4 v[180:183], v227, %[rs], %[sb] offen offset:1024 sc1\n\t"
        "buffer_load_dwordx4 v[184:187], v228, %[rs], %[sb] offen sc1\n\t"
        "buffer_load_dwordx4 v[188:191], v229, %[rs], %[sb] offen sc1\n\t"
        "s_waitcnt vmcnt(4)\n\t"
        "v_mfma_f32_32x32x16_bf16 v[64:79], v[160:163], v[112:115], v[96:111]\n\t"
        "v_mfma_f32_32x32x16_bf16 v[64:79], v[164:167], v[116:119], v[64:79]\n\t"
        "buffer_load_dwordx4 v[192:195], v230, %[rs], %[sb] offen sc1\n\t"
        "buffer_load_dwordx4 v[196:199], v230, %[rs], %[sb] offen offset:1024 sc1\n\t"
        "buffer_load_dwordx4 v[200:203], v231, %[rs], %[sb] offen sc1\n\t"
        "buffer_load_dwordx4 v[204:207], v232, %[rs], %[sb] offen sc1\n\t"
        "s_nop 9\n\t"
        "LOOP%=:\n\t"
        "v_mfma_f32_32x32x16_bf16 v[80:95], v[160:163], v[120:123], v[96:111]\n\t"
        "v_exp_f32 v64, v64\n\t"
        "v_exp_f32 v65, v65\n\t"
        "v_exp_f32 v66, v66\n\t"
        "v_exp_f32 v67, v67\n\t"
        "v_mfma_f32_32x32x16_bf16 v[80:95], v[164:167], v[124:127], v[80:95]\n\t"
        "v_exp_f32 v68, v68\n\t"
        "v_exp_f32 v69, v69\n\t"
        "v_exp_f32 v70, v70\n\t"
        "v_exp_f32 v71, v71\n\t"
        "v_cvt_pk_bf16_f32 v144, v64, v65\n\t"
        "v_cvt_pk_bf16_f32 v145, v66, v67\n\t"
        "v_mfma_f32_32x32x16_bf16 v[48:63], v[216:219], v[152:155], v[48:63]\n\t"
        "v_exp_f32 v72, v72\n\t"
        "v_exp_f32 v73, v73\n\t"
        "v_exp_f32 v74, v74\n\t"
        "v_exp_f32 v75, v75\n\t"
        "v_cvt_pk_bf16_f32 v146, v68, v69\n\t"
        "v_cvt_pk_bf16_f32 v147, v70, v71\n\t"
        "v_mfma_f32_32x32x16_bf16 v[48:63], v[220:223], v[156:159], v[48:63]\n\t"
        "v_exp_f32 v76, v76\n\t"
        "v_exp_f32 v77, v77\n\t"
        "v_exp_f32 v78, v78\n\t"
        "v_exp_f32 v79, v79\n\t"
        "v_cvt_pk_bf16_f32 v148, v72, v73\n\t"
        "v_cvt_pk_bf16_f32 v149, v74, v75\n\t"
        "v_cvt_pk_bf16_f32 v150, v76, v77\n\t"
        "v_cvt_pk_bf16_f32 v151, v78, v79\n\t"
        "v_mfma_f32_32x32x16_bf16 v[64:79], v[160:163], v[128:131], v[96:111]\n\t"
        "buffer_load_dwordx4 v[208:211], v233, %[rs], %[sb] offen sc1\n\t"
        "buffer_load_dwordx4 v[212:215], v233, %[rs], %[sb] offen offset:1024 sc1\n\t"
        "buffer_load_dwordx4 v[216:219], v234, %[rs], %[sb] offen sc1\n\t"
        "buffer_load_dwordx4 v[220:223], v235, %[rs], %[sb] offen sc1\n\t"
        "v_exp_f32 v80, v80\n\t"
        "v_exp_f32 v81, v81\n\t"
        "v_exp_f32 v82, v82\n\t"
        "v_exp_f32 v83, v83\n\t"
        "v_mfma_f32_32x32x16_bf16 v[64:79], v[164:167], v[132:135], v[64:79]\n\t"
        "v_exp_f32 v84, v84\n\t"
        "v_exp_f32 v85, v85\n\t"
        "v_exp_f32 v86, v86\n\t"
        "v_exp_f32 v87, v87\n\t"
        "v_cvt_pk_bf16_f32 v152, v80, v81\n\t"
        "v_cvt_pk_bf16_f32 v153, v82, v83\n\t"
        "v_mfma_f32_32x32x16_bf16 v[0:15], v[168:171], v[144:147], v[0:15]\n\t"
        "v_exp_f32 v88, v88\n\t"
        "v_exp_f32 v89, v89\n\t"
        "v_exp_f32 v90, v90\n\t"
        "v_exp_f32 v91, v91\n\t"
        "v_cvt_pk_bf16_f32 v154, v84, v85\n\t"
        "v_cvt_pk_bf16_f32 v155, v86, v87\n\t"
        "v_mfma_f32_32x32x16_bf16 v[0:15], v[172:175], v[148:151], v[0:15]\n\t"
        "v_exp_f32 v92, v92\n\t"
        "v_exp_f32 v93, v93\n\t"
        "v_exp_f32 v94, v94\n\t"
        "v_exp_f32 v95, v95\n\t"
        "v_cvt_pk_bf16_f32 v156, v88, v89\n\t"
        "v_cvt_pk_bf16_f32 v157, v90, v91\n\t"
        "v_cvt_pk_bf16_f32 v158, v92, v93\n\t"
        "v_cvt_pk_bf16_f32 v159, v94, v95\n\t"
        "v_mfma_f32_32x32x16_bf16 v[80:95], v[160:163], v[136:139], v[96:111]\n\t"
        "ds_read_b32 v239, v238\n\t"
        "v_exp_f32 v64, v64\n\t"
        "v_exp_f32 v65, v65\n\t"
        "v_exp_f32 v66, v66\n\t"
        "v_exp_f32 v67, v67\n\t"
        "v_mfma_f32_32x32x16_bf16 v[80:95], v[164:167], v[140:143], v[80:95]\n\t"
        "v_exp_f32 v68, v68\n\t"
        "v_exp_f32 v69, v69\n\t"
        "v_exp_f32 v70, v70\n\t"
        "v_exp_f32 v71, v71\n\t"
        "v_cvt_pk_bf16_f32 v144, v64, v65\n\t"
        "v_cvt_pk_bf16_f32 v145, v66, v67\n\t"
        "v_mfma_f32_32x32x16_bf16 v[16:31], v[168:171], v[152:155], v[16:31]\n\t"
        "v_exp_f32 v72, v72\n\t"
        "v_exp_f32 v73, v73\n\t"
        "v_exp_f32 v74, v74\n\t"
        "v_exp_f32 v75, v75\n\t"
        "v_cvt_pk_bf16_f32 v146, v68, v69\n\t"
        "v_cvt_pk_bf16_f32 v147, v70, v71\n\t"
        "v_mfma_f32_32x32x16_bf16 v[16:31], v[172:175], v[156:159], v[16:31]\n\t"
        "v_exp_f32 v76, v76\n\t"
        "v_exp_f32 v77, v77\n\t"
        "v_exp_f32 v78, v78\n\t"
        "v_exp_f32 v79, v79\n\t"
        "v_cvt_pk_bf16_f32 v148, v72, v73\n\t"
        "v_cvt_pk_bf16_f32 v149, v74, v75\n\t"
        "v_cvt_pk_bf16_f32 v150, v76, v77\n\t"
        "v_cvt_pk_bf16_f32 v151, v78, v79\n\t"
        "s_waitcnt vmcnt(8)\n\t"
        "v_mfma_f32_32x32x16_bf16 v[64:79], v[176:179], v[112:115], v[96:111]\n\t"
        "v_exp_f32 v80, v80\n\t"
        "v_exp_f32 v81, v81\n\t"
        "v_exp_f32 v82, v82\n\t"
        "v_exp_f32 v83, v83\n\t"
        "v_mfma_f32_32x32x16_bf16 v[64:79], v[180:183], v[116:119], v[64:79]\n\t"
        "v_exp_f32 v84, v84\n\t"
        "v_exp_f32 v85, v85\n\t"
        "v_exp_f32 v86, v86\n\t"
        "v_exp_f32 v87, v87\n\t"
        "v_cvt_pk_bf16_f32 v152, v80, v81\n\t"
        "v_cvt_pk_bf16_f32 v153, v82, v83\n\t"
        "v_mfma_f32_32x32x16_bf16 v[32:47], v[168:171], v[144:147], v[32:47]\n\t"
        "v_exp_f32 v88, v88\n\t"
        "v_exp_f32 v89, v89\n\t"
        "v_exp_f32 v90, v90\n\t"
        "v_exp_f32 v91, v91\n\t"
        "v_cvt_pk_bf16_f32 v154, v84, v85\n\t"
        "v_cvt_pk_bf16_f32 v155, v86, v87\n\t"
        "v_mfma_f32_32x32x16_bf16 v[32:47], v[172:175], v[148:151], v[32:47]\n\t"
        "v_exp_f32 v92, v92\n\t"
        "v_exp_f32 v93, v93\n\t"
        "v_exp_f32 v94, v94\n\t"
        "v_exp_f32 v95, v95\n\t"
        "v_cvt_pk_bf16_f32 v156, v88, v89\n\t"
        "v_cvt_pk_bf16_f32 v157, v90, v91\n\t"
        "v_cvt_pk_bf16_f32 v158, v92, v93\n\t"
        "v_cvt_pk_bf16_f32 v159, v94, v95\n\t"
        "v_mfma_f32_32x32x16_bf16 v[80:95], v[176:179], v[120:123], v[96:111]\n\t"
        "CHK%=:\n\t"
        "s_waitcnt lgkmcnt(0)\n\t"
        "v_readfirstlane_b32 %[sb], v239\n\t"
        "ds_read_b32 v239, v238\n\t"
        "s_cmp_eq_u32 %[sb], -1\n\t"
        "s_cbranch_scc1 CHK%=\n\t"
        "v_add_u32 v238, 4, v238\n\t"
        "v_exp_f32 v64, v64\n\t"
        "v_exp_f32 v65, v65\n\t"
        "v_exp_f32 v66, v66\n\t"
        "v_exp_f32 v67, v67\n\t"
        "v_mfma_f32_32x32x16_bf16 v[80:95], v[180:183], v[124:127], v[80:95]\n\t"
        "v_exp_f32 v68, v68\n\t"
        "v_exp_f32 v69, v69\n\t"
        "v_exp_f32 v70, v70\n\t"
        "v_exp_f32 v71, v71\n\t"
        "v_cvt_pk_bf16_f32 v144, v64, v65\n\t"
        "v_cvt_pk_bf16_f32 v145, v66, v67\n\t"
        "v_mfma_f32_32x32x16_bf16 v[48:63], v[168:171], v[152:155], v[48:63]\n\t"
        "v_exp_f32 v72, v72\n\t"
        "v_exp_f32 v73, v73\n\t"
        "v_exp_f32 v74, v74\n\t"
        "v_exp_f32 v75, v75\n\t"
        "v_cvt_pk_bf16_f32 v146, v68, v69\n\t"
        "v_cvt_pk_bf16_f32 v147, v70, v71\n\t"
        "v_mfma_f32_32x32x16_bf16 v[48:63], v[172:175], v[156:159], v[48:63]\n\t"
        "v_exp_f32 v76, v76\n\t"
        "v_exp_f32 v77, v77\n\t"
        "v_exp_f32 v78, v78\n\t"
        "v_exp_f32 v79, v79\n\t"
        "v_cvt_pk_bf16_f32 v148, v72, v73\n\t"
        "v_cvt_pk_bf16_f32 v149, v74, v75\n\t"
        "v_cvt_pk_bf16_f32 v150, v76, v77\n\t"
        "v_cvt_pk_bf16_f32 v151, v78, v79\n\t"
        "v_mfma_f32_32x32x16_bf16 v[64:79], v[176:179], v[128:131], v[96:111]\n\t"
        "buffer_load_dwordx4 v[160:163], v224, %[rs], %[sb] offen sc1\n\t"
        "buffer_load_dwordx4 v[164:167], v224, %[rs], %[sb] offen offset:1024 sc1\n\t"
        "buffer_load_dwordx4 v[168:171], v225, %[rs], %[sb] offen sc1\n\t"
        "buffer_load_dwordx4 v[172:175], v226, %[rs], %[sb] offen sc1\n\t"
        "v_exp_f32 v80, v80\n\t"
        "v_exp_f32 v81, v81\n\t"
        "v_exp_f32 v82, v82\n\t"
        "v_exp_f32 v83, v83\n\t"
        "v_mfma_f32_32x32x16_bf16 v[64:79], v[180:183], v[132:135], v[64:79]\n\t"
        "v_exp_f32 v84, v84\n\t"
        "v_exp_f32 v85, v85\n\t"
        "v_exp_f32 v86, v86\n\t"
        "v_exp_f32 v87, v87\n\t"
        "v_cvt_pk_bf16_f32 v152, v80, v81\n\t"
        "v_cvt_pk_bf16_f32 v153, v82, v83\n\t"
        "v_mfma_f32_32x32x16_bf16 v[0:15], v[184:187], v[144:147], v[0:15]\n\t"
        "v_exp_f32 v88, v88\n\t"
        "v_exp_f32 v89, v89\n\t"
        "v_exp_f32 v90, v90\n\t"
        "v_exp_f32 v91, v91\n\t"
        "v_cvt_pk_bf16_f32 v154, v84, v85\n\t"
        "v_cvt_pk_bf16_f32 v155, v86, v87\n\t"
        "v_mfma_f32_32x32x16_bf16 v[0:15], v[188:191], v[148:151], v[0:15]\n\t"
        "v_exp_f32 v92, v92\n\t"
        "v_exp_f32 v93, v93\n\t"
        "v_exp_f32 v94, v94\n\t"
        "v_exp_f32 v95, v95\n\t"
        "v_cvt_pk_bf16_f32 v156, v88, v89\n\t"
        "v_cvt_pk_bf16_f32 v157, v90, v91\n\t"
        "v_cvt_pk_bf16_f32 v158, v92, v93\n\t"
        "v_cvt_pk_bf16_f32 v159, v94, v95\n\t"
        "v_mfma_f32_32x32x16_bf16 v[80:95], v[176:179], v[136:139], v[96:111]\n\t"
        "v_exp_f32 v64, v64\n\t"
        "v_exp_f32 v65, v65\n\t"
        "v_exp_f32 v66, v66\n\t"
        "v_exp_f32 v67, v67\n\t"
        "v_mfma_f32_32x32x16_bf16 v[80:95], v[180:183], v[140:143], v[80:95]\n\t"
        "v_exp_f32 v68, v68\n\t"
        "v_exp_f32 v69, v69\n\t"
        "v_exp_f32 v70, v70\n\t"
        "v_exp_f32 v71, v71\n\t"
        "v_cvt_pk_bf16_f32 v144, v64, v65\n\t"
        "v_cvt_pk_bf16_f32 v145, v66, v67\n\t"
        "v_mfma_f32_32x32x16_bf16 v[16:31], v[184:187], v[152:155], v[16:31]\n\t"
        "v_exp_f32 v72, v72\n\t"
        "v_exp_f32 v73, v73\n\t"
        "v_exp_f32 v74, v74\n\t"
        "v_exp_f32 v75, v75\n\t"
        "v_cvt_pk_bf16_f32 v146, v68, v69\n\t"
        "v_cvt_pk_bf16_f32 v147, v70, v71\n\t"
        "v_mfma_f32_32x32x16_bf16 v[16:31], v[188:191], v[156:159], v[16:31]\n\t"
        "v_exp_f32 v76, v76\n\t"
        "v_exp_f32 v77, v77\n\t"
        "v_exp_f32 v78, v78\n\t"
        "v_exp_f32 v79, v79\n\t"
        "v_cvt_pk_bf16_f32 v148, v72, v73\n\t"
        "v_cvt_pk_bf16_f32 v149, v74, v75\n\t"
        "v_cvt_pk_bf16_f32 v150, v76, v77\n\t"
        "v_cvt_pk_bf16_f32 v151, v78, v79\n\t"
        "s_waitcnt vmcnt(8)\n\t"
        "v_mfma_f32_32x32x16_bf16 v[64:79], v[192:195], v[112:115], v[96:111]\n\t"
        "v_exp_f32 v80, v80\n\t"
        "v_exp_f32 v81, v81\n\t"
        "v_exp_f32 v82, v82\n\t"
        "v_exp_f32 v83, v83\n\t"
        "v_mfma_f32_32x32x16_bf16 v[64:79], v[196:199], v[116:119], v[64:79]\n\t"
        "v_exp_f32 v84, v84\n\t"
        "v_exp_f32 v85, v85\n\t"
        "v_exp_f32 v86, v86\n\t"
        "v_exp_f32 v87, v87\n\t"
        "v_cvt_pk_bf16_f32 v152, v80, v81\n\t"
        "v_cvt_pk_bf16_f32 v153, v82, v83\n\t"
        "v_mfma_f32_32x32x16_bf16 v[32:47], v[184:187], v[144:147], v[32:47]\n\t"
        "v_exp_f32 v88, v88\n\t"
        "v_exp_f32 v89, v89\n\t"
        "v_exp_f32 v90, v90\n\t"
        "v_exp_f32 v91, v91\n\t"
        "v_cvt_pk_bf16_f32 v154, v84, v85\n\t"
        "v_cvt_pk_bf16_f32 v155, v86, v87\n\t"
        "v_mfma_f32_32x32x16_bf16 v[32:47], v[188:191], v[148:151], v[32:47]\n\t"
        "v_exp_f32 v92, v92\n\t"
        "v_exp_f32 v93, v93\n\t"
        "v_exp_f32 v94, v94\n\t"
        "v_exp_f32 v95, v95\n\t"
        "v_cvt_pk_bf16_f32 v156, v88, v89\n\t"
        "v_cvt_pk_bf16_f32 v157, v90, v91\n\t"
        "v_cvt_pk_bf16_f32 v158, v92, v93\n\t"
        "v_cvt_pk_bf16_f32 v159, v94, v95\n\t"
        "v_mfma_f32_32x32x16_bf16 v[80:95], v[192:195], v[120:123], v[96:111]\n\t"
        "v_exp_f32 v64, v64\n\t"
        "v_exp_f32 v65, v65\n\t"
        "v_exp_f32 v66, v66\n\t"
        "v_exp_f32 v67, v67\n\t"
        "v_mfma_f32_32x32x16_bf16 v[80:95], v[196:199], v[124:127], v[80:95]\n\t"
        "v_exp_f32 v68, v68\n\t"
        "v_exp_f32 v69, v69\n\t"
        "v_exp_f32 v70, v70\n\t"
        "v_exp_f32 v71, v71\n\t"
        "v_cvt_pk_bf16_f32 v144, v64, v65\n\t"
        "v_cvt_pk_bf16_f32 v145, v66, v67\n\t"
        "v_mfma_f32_32x32x16_bf16 v[48:63], v[184:187], v[152:155], v[48:63]\n\t"
        "v_exp_f32 v72, v72\n\t"
        "v_exp_f32 v73, v73\n\t"
        "v_exp_f32 v74, v74\n\t"
        "v_exp_f32 v75, v75\n\t"
        "v_cvt_pk_bf16_f32 v146, v68, v69\n\t"
        "v_cvt_pk_bf16_f32 v147, v70, v71\n\t"
        "v_mfma_f32_32x32x16_bf16 v[48:63], v[188:191], v[156:159], v[48:63]\n\t"
        "v_exp_f32 v76, v76\n\t"
        "v_exp_f32 v77, v77\n\t"
        "v_exp_f32 v78, v78\n\t"
        "v_exp_f32 v79, v79\n\t"
        "v_cvt_pk_bf16_f32 v148, v72, v73\n\t"
        "v_cvt_pk_bf16_f32 v149, v74, v75\n\t"
        "v_cvt_pk_bf16_f32 v150, v76, v77\n\t"
        "v_cvt_pk_bf16_f32 v151, v78, v79\n\t"
        "v_mfma_f32_32x32x16_bf16 v[64:79], v[192:195], v[128:131], v[96:111]\n\t"
        "buffer_load_dwordx4 v[176:179], v227, %[rs], %[sb] offen sc1\n\t"
        "buffer_load_dwordx4 v[180:183], v227, %[rs], %[sb] offen offset:1024 sc1\n\t"
        "buffer_load_dwordx4 v[184:187], v228, %[rs], %[sb] offen sc1\n\t"
        "buffer_load_dwordx4 v[188:191], v229, %[rs], %[sb] offen sc1\n\t"
        "v_exp_f32 v80, v80\n\t"
        "v_exp_f32 v81, v81\n\t"
        "v_exp_f32 v82, v82\n\t"
        "v_exp_f32 v83, v83\n\t"
        "v_mfma_f32_32x32x16_bf16 v[64:79], v[196:199], v[132:135], v[64:79]\n\t"
        "v_exp_f32 v84, v84\n\t"
        "v_exp_f32 v85, v85\n\t"
        "v_exp_f32 v86, v86\n\t"
        "v_exp_f32 v87, v87\n\t"
        "v_cvt_pk_bf16_f32 v152, v80, v81\n\t"
        "v_cvt_pk_bf16_f32 v153, v82, v83\n\t"
        "v_mfma_f32_32x32x16_bf16 v[0:15], v[200:203], v[144:147], v[0:15]\n\t"
        "v_exp_f32 v88, v88\n\t"
        "v_exp_f32 v89, v89\n\t"
        "v_exp_f32 v90, v90\n\t"
        "v_exp_f32 v91, v91\n\t"
        "v_cvt_pk_bf16_f32 v154, v84, v85\n\t"
        "v_cvt_pk_bf16_f32 v155, v86, v87\n\t"
        "v_mfma_f32_32x32x16_bf16 v[0:15], v[204:207], v[148:151], v[0:15]\n\t"
        "v_exp_f32 v92, v92\n\t"
        "v_exp_f32 v93, v93\n\t"
        "v_exp_f32 v94, v94\n\t"
        "v_exp_f32 v95, v95\n\t"
        "v_cvt_pk_bf16_f32 v156, v88, v89\n\t"
        "v_cvt_pk_bf16_f32 v157, v90, v91\n\t"
        "v_cvt_pk_bf16_f32 v158, v92, v93\n\t"
        "v_cvt_pk_bf16_f32 v159, v94, v95\n\t"
        "v_mfma_f32_32x32x16_bf16 v[80:95], v[192:195], v[136:139], v[96:111]\n\t"
        "v_exp_f32 v64, v64\n\t"
        "v_exp_f32 v65, v65\n\t"
        "v_exp_f32 v66, v66\n\t"
        "v_exp_f32 v67, v67\n\t"
        "v_mfma_f32_32x32x16_bf16 v[80:95], v[196:199], v[140:143], v[80:95]\n\t"
        "v_exp_f32 v68, v68\n\t"
        "v_exp_f32 v69, v69\n\t"
        "v_exp_f32 v70, v70\n\t"
        "v_exp_f32 v71, v71\n\t"
        "v_cvt_pk_bf16_f32 v144, v64, v65\n\t"
        "v_cvt_pk_bf16_f32 v145, v66, v67\n\t"
        "v_mfma_f32_32x32x16_bf16 v[16:31], v[200:203], v[152:155], v[16:31]\n\t"
        "v_exp_f32 v72, v72\n\t"
        "v_exp_f32 v73, v73\n\t"
        "v_exp_f32 v74, v74\n\t"
        "v_exp_f32 v75, v75\n\t"
        "v_cvt_pk_bf16_f32 v146, v68, v69\n\t"
        "v_cvt_pk_bf16_f32 v147, v70, v71\n\t"
        "v_mfma_f32_32x32x16_bf16 v[16:31], v[204:207], v[156:159], v[16:31]\n\t"
        "v_exp_f32 v76, v76\n\t"
        "v_exp_f32 v77, v77\n\t"
        "v_exp_f32 v78, v78\n\t"
        "v_exp_f32 v79, v79\n\t"
        "v_cvt_pk_bf16_f32 v148, v72, v73\n\t"
        "v_cvt_pk_bf16_f32 v149, v74, v75\n\t"
        "v_cvt_pk_bf16_f32 v150, v76, v77\n\t"
        "v_cvt_pk_bf16_f32 v151, v78, v79\n\t"
        "s_waitcnt vmcnt(8)\n\t"
        "v_mfma_f32_32x32x16_bf16 v[64:79], v[208:211], v[112:115], v[96:111]\n\t"
        "v_exp_f32 v80, v80\n\t"
        "v_exp_f32 v81, v81\n\t"
        "v_exp_f32 v82, v82\n\t"
        "v_exp_f32 v83, v83\n\t"
        "v_mfma_f32_32x32x16_bf16 v[64:79], v[212:215], v[116:119], v[64:79]\n\t"
        "v_exp_f32 v84, v84\n\t"
        "v_exp_f32 v85, v85\n\t"
        "v_exp_f32 v86, v86\n\t"
        "v_exp_f32 v87, v87\n\t"
        "v_cvt_pk_bf16_f32 v152, v80, v81\n\t"
        "v_cvt_pk_bf16_f32 v153, v82, v83\n\t"
        "v_mfma_f32_32x32x16_bf16 v[32:47], v[200:203], v[144:147], v[32:47]\n\t"
        "v_exp_f32 v88, v88\n\t"
        "v_exp_f32 v89, v89\n\t"
        "v_exp_f32 v90, v90\n\t"
        "v_exp_f32 v91, v91\n\t"
        "v_cvt_pk_bf16_f32 v154, v84, v85\n\t"
        "v_cvt_pk_bf16_f32 v155, v86, v87\n\t"
        "v_mfma_f32_32x32x16_bf16 v[32:47], v[204:207], v[148:151], v[32:47]\n\t"
        "v_exp_f32 v92, v92\n\t"
        "v_exp_f32 v93, v93\n\t"
        "v_exp_f32 v94, v94\n\t"
        "v_exp_f32 v95, v95\n\t"
        "v_cvt_pk_bf16_f32 v156, v88, v89\n\t"
        "v_cvt_pk_bf16_f32 v157, v90, v91\n\t"
        "v_cvt_pk_bf16_f32 v158, v92, v93\n\t"
        "v_cvt_pk_bf16_f32 v159, v94, v95\n\t"
        "v_mfma_f32_32x32x16_bf16 v[80:95], v[208:211], v[120:123], v[96:111]\n\t"
        "v_exp_f32 v64, v64\n\t"
        "v_exp_f32 v65, v65\n\t"
        "v_exp_f32 v66, v66\n\t"
        "v_exp_f32 v67, v67\n\t"
        "v_mfma_f32_32x32x16_bf16 v[80:95], v[212:215], v[124:127], v[80:95]\n\t"
        "v_exp_f32 v68, v68\n\t"
        "v_exp_f32 v69, v69\n\t"
        "v_exp_f32 v70, v70\n\t"
        "v_exp_f32 v71, v71\n\t"
        "v_cvt_pk_bf16_f32 v144, v64, v65\n\t"
        "v_cvt_pk_bf16_f32 v145, v66, v67\n\t"
        "v_mfma_f32_32x32x16_bf16 v[48:63], v[200:203], v[152:155], v[48:63]\n\t"
        "v_exp_f32 v72, v72\n\t"
        "v_exp_f32 v73, v73\n\t"
        "v_exp_f32 v74, v74\n\t"
        "v_exp_f32 v75, v75\n\t"
        "v_cvt_pk_bf16_f32 v146, v68, v69\n\t"
        "v_cvt_pk_bf16_f32 v147, v70, v71\n\t"
        "v_mfma_f32_32x32x16_bf16 v[48:63], v[204:207], v[156:159], v[48:63]\n\t"
        "v_exp_f32 v76, v76\n\t"
        "v_exp_f32 v77, v77\n\t"
        "v_exp_f32 v78, v78\n\t"
        "v_exp_f32 v79, v79\n\t"
        "v_cvt_pk_bf16_f32 v148, v72, v73\n\t"
        "v_cvt_pk_bf16_f32 v149, v74, v75\n\t"
        "v_cvt_pk_bf16_f32 v150, v76, v77\n\t"
        "v_cvt_pk_bf16_f32 v151, v78, v79\n\t"
        "v_mfma_f32_32x32x16_bf16 v[64:79], v[208:211], v[128:131], v[96:111]\n\t"
        "buffer_load_dwordx4 v[192:195], v230, %[rs], %[sb] offen sc1\n\t"
        "buffer_load_dwordx4 v[196:199], v230, %[rs], %[sb] offen offset:1024 sc1\n\t"
        "buffer_load_dwordx4 v[200:203], v231, %[rs], %[sb] offen sc1\n\t"
        "buffer_load_dwordx4 v[204:207], v232, %[rs], %[sb] offen sc1\n\t"
        "s_sub_u32 %[cnt], %[cnt], 1\n\t"
        "s_cmp_eq_u32 %[cnt], 0\n\t"
        "v_exp_f32 v80, v80\n\t"
        "v_exp_f32 v81, v81\n\t"
        "v_exp_f32 v82, v82\n\t"
        "v_exp_f32 v83, v83\n\t"
        "v_mfma_f32_32x32x16_bf16 v[64:79], v[212:215], v[132:135], v[64:79]\n\t"
        "v_exp_f32 v84, v84\n\t"
        "v_exp_f32 v85, v85\n\t"
        "v_exp_f32 v86, v86\n\t"
        "v_exp_f32 v87, v87\n\t"
        "v_cvt_pk_bf16_f32 v152, v80, v81\n\t"
        "v_cvt_pk_bf16_f32 v153, v82, v83\n\t"
        "v_mfma_f32_32x32x16_bf16 v[0:15], v[216:219], v[144:147], v[0:15]\n\t"
        "v_exp_f32 v88, v88\n\t"
        "v_exp_f32 v89, v89\n\t"
        "v_exp_f32 v90, v90\n\t"
        "v_exp_f32 v91, v91\n\t"
        "v_cvt_pk_bf16_f32 v154, v84, v85\n\t"
        "v_cvt_pk_bf16_f32 v155, v86, v87\n\t"
        "v_mfma_f32_32x32x16_bf16 v[0:15], v[220:223], v[148:151], v[0:15]\n\t"
        "v_exp_f32 v92, v92\n\t"
        "v_exp_f32 v93, v93\n\t"
        "v_exp_f32 v94, v94\n\t"
        "v_exp_f32 v95, v95\n\t"
        "v_cvt_pk_bf16_f32 v156, v88, v89\n\t"
        "v_cvt_pk_bf16_f32 v157, v90, v91\n\t"
        "v_cvt_pk_bf16_f32 v158, v92, v93\n\t"
        "v_cvt_pk_bf16_f32 v159, v94, v95\n\t"
        "s_cbranch_scc1 TAIL%=\n\t"
        "v_mfma_f32_32x32x16_bf16 v[80:95], v[208:211], v[136:139], v[96:111]\n\t"
        "v_exp_f32 v64, v64\n\t"
        "v_exp_f32 v65, v65\n\t"
        "v_exp_f32 v66, v66\n\t"
        "v_exp_f32 v67, v67\n\t"
        "v_mfma_f32_32x32x16_bf16 v[80:95], v[212:215], v[140:143], v[80:95]\n\t"
        "v_exp_f32 v68, v68\n\t"
        "v_exp_f32 v69, v69\n\t"
        "v_exp_f32 v70, v70\n\t"
        "v_exp_f32 v71, v71\n\t"
        "v_cvt_pk_bf16_f32 v144, v64, v65\n\t"
        "v_cvt_pk_bf16_f32 v145, v66, v67\n\t"
        "v_mfma_f32_32x32x16_bf16 v[16:31], v[216:219], v[152:155], v[16:31]\n\t"
        "v_exp_f32 v72, v72\n\t"
        "v_exp_f32 v73, v73\n\t"
        "v_exp_f32 v74, v74\n\t"
        "v_exp_f32 v75, v75\n\t"
        "v_cvt_pk_bf16_f32 v146, v68, v69\n\t"
        "v_cvt_pk_bf16_f32 v147, v70, v71\n\t"
        "v_mfma_f32_32x32x16_bf16 v[16:31], v[220:223], v[156:159], v[16:31]\n\t"
        "v_exp_f32 v76, v76\n\t"
        "v_exp_f32 v77, v77\n\t"
        "v_exp_f32 v78, v78\n\t"
        "v_exp_f32 v79, v79\n\t"
        "v_cvt_pk_bf16_f32 v148, v72, v73\n\t"
        "v_cvt_pk_bf16_f32 v149, v74, v75\n\t"
        "v_cvt_pk_bf16_f32 v150, v76, v77\n\t"
        "v_cvt_pk_bf16_f32 v151, v78, v79\n\t"
        "s_waitcnt vmcnt(8)\n\t"
        "v_mfma_f32_32x32x16_bf16 v[64:79], v[160:163], v[112:115], v[96:111]\n\t"
        "v_exp_f32 v80, v80\n\t"
        "v_exp_f32 v81, v81\n\t"
        "v_exp_f32 v82, v82\n\t"
        "v_exp_f32 v83, v83\n\t"
        "v_mfma_f32_32x32x16_bf16 v[64:79], v[164:167], v[116:119], v[64:79]\n\t"
        "v_exp_f32 v84, v84\n\t"
        "v_exp_f32 v85, v85\n\t"
        "v_exp_f32 v86, v86\n\t"
        "v_exp_f32 v87, v87\n\t"
        "v_cvt_pk_bf16_f32 v152, v80, v81\n\t"
        "v_cvt_pk_bf16_f32 v153, v82, v83\n\t"
        "v_mfma_f32_32x32x16_bf16 v[32:47], v[216:219], v[144:147], v[32:47]\n\t"
        "v_exp_f32 v88, v88\n\t"
        "v_exp_f32 v89, v89\n\t"
        "v_exp_f32 v90, v90\n\t"
        "v_exp_f32 v91, v91\n\t"
        "v_cvt_pk_bf16_f32 v154, v84, v85\n\t"
        "v_cvt_pk_bf16_f32 v155, v86, v87\n\t"
        "v_mfma_f32_32x32x16_bf16 v[32:47], v[220:223], v[148:151], v[32:47]\n\t"
        "v_exp_f32 v92, v92\n\t"
        "v_exp_f32 v93, v93\n\t"
        "v_exp_f32 v94, v94\n\t"
        "v_exp_f32 v95, v95\n\t"
        "v_cvt_pk_bf16_f32 v156, v88, v89\n\t"
        "v_cvt_pk_bf16_f32 v157, v90, v91\n\t"
        "v_cvt_pk_bf16_f32 v158, v92, v93\n\t"
        "v_cvt_pk_bf16_f32 v159, v94, v95\n\t"
        "s_branch LOOP%=\n\t"
        "TAIL%=:\n\t"
        "v_mfma_f32_32x32x16_bf16 v[80:95], v[208:211], v[136:139], v[96:111]\n\t"
        "v_exp_f32 v64, v64\n\t"
        "v_exp_f32 v65, v65\n\t"
        "v_exp_f32 v66, v66\n\t"
        "v_exp_f32 v67, v67\n\t"
        "v_mfma_f32_32x32x16_bf16 v[80:95], v[212:215], v[140:143], v[80:95]\n\t"
        "ds_write_b128 v240, v[0:3] offset:0\n\t"
        "ds_write_b128 v240, v[4:7] offset:1024\n\t"
        "ds_write_b128 v240, v[8:11] offset:2048\n\t"
        "ds_write_b128 v240, v[12:15] offset:3072\n\t"
        "v_exp_f32 v68, v68\n\t"
        "v_exp_f32 v69, v69\n\t"
        "v_exp_f32 v70, v70\n\t"
        "v_exp_f32 v71, v71\n\t"
        "v_cvt_pk_bf16_f32 v144, v64, v65\n\t"
        "v_cvt_pk_bf16_f32 v145, v66, v67\n\t"
        "v_mfma_f32_32x32x16_bf16 v[16:31], v[216:219], v[152:155], v[16:31]\n\t"
        "v_exp_f32 v72, v72\n\t"
        "v_exp_f32 v73, v73\n\t"
        "v_exp_f32 v74, v74\n\t"
        "v_exp_f32 v75, v75\n\t"
        "v_cvt_pk_bf16_f32 v146, v68, v69\n\t"
        "v_cvt_pk_bf16_f32 v147, v70, v71\n\t"
        "v_mfma_f32_32x32x16_bf16 v[16:31], v[220:223], v[156:159], v[16:31]\n\t"
        "v_exp_f32 v76, v76\n\t"
        "v_exp_f32 v77, v77\n\t"
        "v_exp_f32 v78, v78\n\t"
        "v_exp_f32 v79, v79\n\t"
        "v_cvt_pk_bf16_f32 v148, v72, v73\n\t"
        "v_cvt_pk_bf16_f32 v149, v74, v75\n\t"
        "v_cvt_pk_bf16_f32 v150, v76, v77\n\t"
        "v_cvt_pk_bf16_f32 v151, v78, v79\n\t"
        "v_exp_f32 v80, v80\n\t"
        "v_exp_f32 v81, v81\n\t"
        "v_exp_f32 v82, v82\n\t"
        "v_exp_f32 v83, v83\n\t"
        "v_exp_f32 v84, v84\n\t"
        "v_exp_f32 v85, v85\n\t"
        "v_exp_f32 v86, v86\n\t"
        "v_exp_f32 v87, v87\n\t"
        "v_cvt_pk_bf16_f32 v152, v80, v81\n\t"
        "v_cvt_pk_bf16_f32 v153, v82, v83\n\t"
        "v_mfma_f32_32x32x16_bf16 v[32:47], v[216:219], v[144:147], v[32:47]\n\t"
        "ds_write_b128 v240, v[16:19] offset:4096\n\t"
        "ds_write_b128 v240, v[20:23] offset:5120\n\t"
        "ds_write_b128 v240, v[24:27] offset:6144\n\t"
        "ds_write_b128 v240, v[28:31] offset:7168\n\t"
        "v_exp_f32 v88, v88\n\t"
        "v_exp_f32 v89, v89\n\t"
        "v_exp_f32 v90, v90\n\t"
        "v_exp_f32 v91, v91\n\t"
        "v_cvt_pk_bf16_f32 v154, v84, v85\n\t"
        "v_cvt_pk_bf16_f32 v155, v86, v87\n\t"
        "v_mfma_f32_32x32x16_bf16 v[32:47], v[220:223], v[148:151], v[32:47]\n\t"
        "v_exp_f32 v92, v92\n\t"
        "v_exp_f32 v93, v93\n\t"
        "v_exp_f32 v94, v94\n\t"
        "v_exp_f32 v95, v95\n\t"
        "v_cvt_pk_bf16_f32 v156, v88, v89\n\t"
        "v_cvt_pk_bf16_f32 v157, v90, v91\n\t"
        "v_cvt_pk_bf16_f32 v158, v92, v93\n\t"
        "v_cvt_pk_bf16_f32 v159, v94, v95\n\t"
        "v_mfma_f32_32x32x16_bf16 v[48:63], v[216:219], v[152:155], v[48:63]\n\t"
        "v_mfma_f32_32x32x16_bf16 v[48:63], v[220:223], v[156:159], v[48:63]\n\t"
        "s_nop 3\n\t"
        "ds_write_b128 v240, v[32:35] offset:8192\n\t"
        "ds_write_b128 v240, v[36:39] offset:9216\n\t"
        "ds_write_b128 v240, v[40:43] offset:10240\n\t"
        "ds_write_b128 v240, v[44:47] offset:11264\n\t"
        "s_waitcnt vmcnt(0) lgkmcnt(0)\n\t"
        "s_barrier\n\t"
        "s_nop 7\n\t"
        : "={v[48:63]}"(acc), [sb] "+s"(sb), [cnt] "+s"(cnt), "+{v236}"(q2a), "+{v237}"(kba), "+{v238}"(ordn), "={v[136:139]}"(qo0), "={v[140:143]}"(qo1)
        : "{v224}"(vok + 0 * TS), "{v225}"(vov + 0 * TS), "{v226}"(vov + 2 * VROW + 0 * TS), "{v227}"(vok + 1 * TS), "{v228}"(vov + 1 * TS), "{v229}"(vov + 2 * VROW + 1 * TS), "{v230}"(vok + 2 * TS), "{v231}"(vov + 2 * TS), "{v232}"(vov + 2 * VROW + 2 * TS), "{v233}"(vok + 3 * TS), "{v234}"(vov + 3 * TS), "{v235}"(vov + 2 * VROW + 3 * TS),
          "{v240}"(puta), "{v241}"(qxa[0]), "{v242}"(qxa[1]), "{v243}"(qxa[2]), "{v244}"(qxa[3]), [rs] "s"(rsKV)
        : "v0", "v1", "v2", "v3", "v4", "v5", "v6", "v7", "v8", "v9", "v10", "v11", "v12", "v13", "v14", "v15", "v16", "v17", "v18", "v19", "v20", "v21", "v22", "v23", "v24", "v25", "v26", "v27", "v28", "v29", "v30", "v31", "v32", "v33", "v34", "v35", "v36", "v37", "v38", "v39", "v40", "v41", "v42", "v43", "v44", "v45", "v46", "v47", "v64", "v65", "v66", "v67", "v68", "v69", "v70", "v71", "v72", "v73", "v74", "v75", "v76", "v77", "v78", "v79", "v80", "v81", "v82", "v83", "v84", "v85", "v86", "v87", "v88", "v89", "v90", "v91", "v92", "v93", "v94", "v95", "v96", "v97", "v98", "v99", "v100", "v101", "v102", "v103", "v104", "v105", "v106", "v107", "v108", "v109", "v110", "v111", "v112", "v113", "v114", "v115", "v116", "v117", "v118", "v119", "v120", "v121", "v122", "v123", "v124", "v125", "v126", "v127", "v128", "v129", "v130", "v131", "v132", "v133", "v134", "v135", "v144", "v145", "v146", "v147", "v148", "v149", "v150", "v151", "v152", "v153", "v154", "v155", "v156", "v157", "v158", "v159", "v160", "v161", "v162", "v163", "v164", "v165", "v166", "v167", "v168", "v169", "v170", "v171", "v172", "v173", "v174", "v175", "v176", "v177", "v178", "v179", "v180", "v181", "v182", "v183", "v184", "v185", "v186", "v187", "v188", "v189", "v190", "v191", "v192", "v193", "v194", "v195", "v196", "v197", "v198", "v199", "v200", "v201", "v202", "v203", "v204", "v205", "v206", "v207", "v208", "v209", "v210", "v211", "v212", "v213", "v214", "v215", "v216", "v217", "v218", "v219", "v220", "v221", "v222", "v223", "v239", "scc", "memory");
    }
    STAMP(7);
#pragma unroll
    for (int s = 0; s < 3; ++s) {
        LAS const char* sp = lds + L_CB + (((w - 1 - s) & 3) * 3 + s) * 4096 + lane * 16;
#pragma unroll
        for (int g = 0; g < 4; ++g) {
            const f32x4 v = *(LAS const f32x4*)(sp + g * 1024);
            acc[4 * g] += v[0]; acc[4 * g + 1] += v[1]; acc[4 * g + 2] += v[2]; acc[4 * g + 3] += v[3];
        }
    }
    float l = half_hi(acc[12]);
    if (__builtin_expect(__any(!(l >= L_MIN && l <= L_MAX)), 0)) {
        const f32x16 zero = {};
        float m = -__builtin_inff();
#pragma unroll 1
        for (int t = 0; t < 64; ++t) {
            f32x16 s = MFMA(asbf(ld16_wt(rsKV, vok, t * TS)), asbf(qo0), zero);
            s = MFMA(asbf(ld16_wt(rsKV, vok + 1024, t * TS)), asbf(qo1), s);
#pragma unroll
            for (int i = 0; i < 16; ++i) m = __builtin_fmaxf(m, s[i]);
        }
        m = __builtin_fmaxf(half_lo(m), half_hi(m));
        acc = zero;
#pragma unroll 1
        for (int t = 0; t < 64; ++t) {
            f32x16 s = MFMA(asbf(ld16_wt(rsKV, vok, t * TS)), asbf(qo0), zero);
            s = MFMA(asbf(ld16_wt(rsKV, vok + 1024, t * TS)), asbf(qo1), s);
#pragma unroll
            for (int i = 0; i < 16; ++i) s[i] = __builtin_amdgcn_exp2f(s[i] - m);
            acc = MFMA(asbf(ld16_wt(rsKV, vov, t * TS)), asbf(pack8(s, 0)), acc);
            acc = MFMA(asbf(ld16_wt(rsKV, vov + 2 * VROW, t * TS)), asbf(pack8(s, 1)), acc);
        }
        l = half_hi(acc[12]);
    }
    {
        const float rl = __builtin_amdgcn_rcpf(l);
#pragma unroll
        for (int i = 0; i < 16; ++i) acc[i] = (i >= 12 && h) ? 0.f : acc[i] * rl;
        const unsigned so = (kt * NH + hd) * 2048;
        u32x4 p0 = pack8(acc, 0), p1 = pack8(acc, 1);
#pragma unroll
        for (int i = 0; i < 4; ++i) { p0[i] = min(p0[i], POISON - 1); p1[i] = min(p1[i], POISON - 1); }
        st16_wt(p0, rsOI, lane * 16, so);
        st16_wt(p1, rsOI, lane * 16 + 1024, so);
    }
    STAMP(8);
    BAR();

    bf16x8 wf[32];
    {
        LAS const char* wb = lds + L_WO + (4 * h + q4) * 256 + ((w ^ q4) * 64) + g16 * 32 + p4 * 8;
#pragma unroll
        for (int k = 0; k < 32; ++k) {
            const int ro = ((k >> 1) * DH + (k & 1) * 16) * 256;
            wf[k] = cat(vtr(wb + ro), vtr(wb + ro + 2048 - (k == 31 ? h * 1024 : 0)));
        }
    }
    STAMP(10);
    BAR();
    STAMP(11);
    if (cg < 3 || w < 2) {
        f32x16 c = {};
        bf16x8 of[3][4];
#define RDO(i) do { _Pragma("unroll") for (int j = 0; j < 4; ++j) of[(i) % 3][j] = *(LAS const bf16x8*)(lds + L_CB + ((i) * 4 + j) * 1024 + lane * 16); } while (0)
        RDO(0); RDO(1); SBAR();
#pragma unroll
        for (int i = 0; i < 8; ++i) {
            if (i + 2 < 8) RDO(i + 2);
            SBAR();
#pragma unroll
            for (int j = 0; j < 4; ++j) c = MFMA(wf[i * 4 + j], of[i % 3][j], c);
            SBAR();
        }
#undef RDO
        STAMP(12);
        char* orow = out + (size_t)(q32 * 32 + r) * (DM * 2) + cg * 256 + w * 64 + h * 16;
#pragma unroll
        for (int g = 0; g < 4; g += 2) {
            const unsigned x0 = cvtpk(c[4 * g], c[4 * g + 1]), x1 = cvtpk(c[4 * g + 2], c[4 * g + 3]);
            const unsigned y0 = cvtpk(c[4 * g + 4], c[4 * g + 5]), y1 = cvtpk(c[4 * g + 6], c[4 * g + 7]);
            auto s0 = __builtin_amdgcn_permlane32_swap(x0, y0, false, false);
            auto s1 = __builtin_amdgcn_permlane32_swap(x1, y1, false, false);
            *(u32x4*)(orow + g * 16) = (u32x4){s0[0], s1[0], s0[1], s1[1]};
        }
    }
    STAMP(13);
    STAMP_FLUSH();
}

extern "C" void kernel_launch(void* const* d_in, const int* in_sizes, int n_in, void* d_out, int out_size, void* d_ws, size_t ws_size,
                              hipStream_t stream) {
    (void)hipFuncSetAttribute((const void*)mha_kernel, hipFuncAttributeMaxDynamicSharedMemorySize, LDS_BYTES);
    mha_kernel<<<256, 512, LDS_BYTES, stream>>>((const char*)d_in[0], (const char*)d_in[1], (const char*)d_in[2], (char*)d_out, (char*)d_ws);
}
```

```cpp
#include <hip/hip_runtime.h>
#include <hip/hip_bf16.h>
#include <stdint.h>

typedef short bf16x8 __attribute__((ext_vector_type(8)));
typedef short s16x4 __attribute__((ext_vector_type(4)));
typedef float f32x16 __attribute__((ext_vector_type(16)));
typedef float f32x4 __attribute__((ext_vector_type(4)));
typedef float f32x2 __attribute__((ext_vector_type(2)));
typedef unsigned u32x4 __attribute__((ext_vector_type(4)));
typedef __bf16 bf16x2_t __attribute__((ext_vector_type(2)));
typedef short v4i16_t __attribute__((ext_vector_type(4)));
typedef unsigned long long u64;
#define LAS __attribute__((address_space(3)))
#define GAS __attribute__((address_space(1)))
#define DI __device__ __forceinline__
#define MFMA(a, b, c) __builtin_amdgcn_mfma_f32_32x32x16_bf16((a), (b), (c), 0, 0, 0)
#define SBAR() __builtin_amdgcn_sched_barrier(0)
#define RLX_AGENT __ATOMIC_RELAXED, __HIP_MEMORY_SCOPE_AGENT
#define BAR() asm volatile("s_waitcnt lgkmcnt(0)\n\ts_barrier" ::: "memory")

#define PRETOUCH 2
#define OFLAG 0
#define MAPPING 1
#define MAP_HD(b) (((b) & 7) * 2 + (((b) >> 3) & 1))
#define MAP_QT(b) ((b) >> 4)
constexpr int SEQ = 2048, DM = 448, NH = 16, DH = 28, N3 = 1344;
constexpr float QSCALE = 0.18898223650461362f * 1.4426950408889634f;
constexpr float L_MIN = 9.094947017729282e-13f, L_MAX = 1.099511627776e12f;
constexpr unsigned VROW = 464 * 16, TS = 4 * VROW;
constexpr unsigned WS_K = 0, WS_V = 2u << 20, WS_O = 4u << 20, WS_F = 6u << 20;
constexpr unsigned POISON = 0xffffffffu;
constexpr unsigned F_G = 0, F_R = 4096, F_P = 8192, F_KV = 131072, F_O = 262144;
constexpr unsigned L_WQ = 0, L_RING = 28672, STG = 24576;
constexpr unsigned L_QX = 131072, L_Q2 = L_QX + 8192, L_GB = L_Q2 + 512;
constexpr unsigned L_WO = 0, L_CB = 114688;
constexpr unsigned LDS_BYTES = 163840;

#define STAMP_DECL
#define STAMP(i)
#define STAMP_FLUSH()

DI unsigned rfl(unsigned x) { return __builtin_amdgcn_readfirstlane(x); }
DI unsigned cvtpk(float lo, float hi) { f32x2 v = {lo, hi}; bf16x2_t b = __builtin_convertvector(v, bf16x2_t); return __builtin_bit_cast(unsigned, b); }
DI u32x4 pack8(const f32x16& x, int s) {
    return (u32x4){cvtpk(x[8 * s], x[8 * s + 1]), cvtpk(x[8 * s + 2], x[8 * s + 3]), cvtpk(x[8 * s + 4], x[8 * s + 5]), cvtpk(x[8 * s + 6], x[8 * s + 7])};
}
DI bf16x8 asbf(u32x4 x) { return __builtin_bit_cast(bf16x8, x); }
DI s16x4 vtr(LAS const char* p) { return __builtin_bit_cast(s16x4, __builtin_amdgcn_ds_read_tr16_b64_v4i16((LAS v4i16_t*)p)); }
DI bf16x8 cat(s16x4 lo, s16x4 hi) { return __builtin_shufflevector(lo, hi, 0, 1, 2, 3, 4, 5, 6, 7); }
DI float half_lo(float x) { auto r = __builtin_amdgcn_permlane32_swap(__float_as_uint(x), __float_as_uint(x), false, false); return __uint_as_float(r[0]); }
DI float half_hi(float x) { auto r = __builtin_amdgcn_permlane32_swap(__float_as_uint(x), __float_as_uint(x), false, false); return __uint_as_float(r[1]); }
DI unsigned wave_max_u(unsigned x) {
#define DPPMAX(ctrl, rm) x = max(x, (unsigned)__builtin_amdgcn_update_dpp((int)x, (int)x, ctrl, rm, 0xf, false))
    DPPMAX(0x111, 0xf); DPPMAX(0x112, 0xf); DPPMAX(0x114, 0xf); DPPMAX(0x118, 0xf); DPPMAX(0x142, 0xa); DPPMAX(0x143, 0xc);
#undef DPPMAX
    return (unsigned)__builtin_amdgcn_readlane((int)x, 63);
}
DI void dma16(unsigned lds_dst, unsigned voff, __amdgpu_buffer_rsrc_t rs, unsigned soff) {
    asm volatile("s_mov_b32 m0, %0\n\ts_nop 0\n\tbuffer_load_dwordx4 %1, %2, %3 offen lds" ::"s"(lds_dst), "v"(voff), "s"(rs), "s"(soff) : "m0", "memory");
}
DI void st16_wt(u32x4 v, __amdgpu_buffer_rsrc_t rs, unsigned voff, unsigned soff) { __builtin_amdgcn_raw_buffer_store_b128(v, rs, voff, soff, 16); }
DI u32x4 ld16_wt(__amdgpu_buffer_rsrc_t rs, unsigned voff, unsigned soff) { return __builtin_amdgcn_raw_buffer_load_b128(rs, voff, soff, 16); }
DI __amdgpu_buffer_rsrc_t mkrs(const void* p, unsigned bytes) { return __builtin_amdgcn_make_buffer_rsrc((void*)p, (short)0, (int)bytes, 0x00020000); }
DI unsigned poll_tags(const GAS u64* g, unsigned tag) {
    u64 v = __hip_atomic_load(g, RLX_AGENT);
#pragma nounroll
    for (unsigned spins = 0; spins < (1u << 16); ++spins) {
        if (__all((unsigned)v == tag)) break;
        if ((unsigned)v != tag) v = __hip_atomic_load(g, RLX_AGENT);
    }
    __builtin_amdgcn_fence(__ATOMIC_ACQUIRE, "wavefront");
    return (unsigned)(v >> 32);
}
DI unsigned touch(const GAS char* p) { unsigned t; asm volatile("global_load_dword %0, %1, off" : "=v"(t) : "v"(p) : "memory"); return t; }

template <bool ISK>
DI void kv_gemm(LAS char* lds, unsigned w, int lane, bf16x8 (&xf)[7][4], f32x16& a0, f32x16& a1) {
    const int r = lane & 31, h = lane >> 5, i16 = lane & 15, q4 = i16 >> 2, p4 = i16 & 3, g16 = (lane >> 4) & 1;
    const int pc = ISK ? (((p4 & 1) << 1) | (p4 >> 1)) : p4;
    LAS const char* xb = lds + L_RING + w * 4096 + r * 128;
    LAS const char* wb0 = lds + L_RING + 16384 + (8 * h + q4) * 128 + ((q4 >> 1) * 64) + g16 * 32 + pc * 8;
    LAS const char* wb1 = lds + L_RING + 16384 + (8 * h + q4) * 128 + (((q4 >> 1) ^ 1) * 64) + g16 * 32 + pc * 8;
    const int xs = (r >> 1) & 7;
    bf16x8 wf[2][4][2];
#define RD(i) do { const int so = ((i) % 5) * STG; _Pragma("unroll") for (int ks = 0; ks < 4; ++ks) {                          \
        xf[i][ks] = *(LAS const bf16x8*)(xb + so + (((2 * ks + h) ^ xs) * 16));                                                \
        wf[(i) & 1][ks][0] = cat(vtr(wb0 + so + ks * 2048), vtr(wb0 + so + ks * 2048 + 512));                                  \
        wf[(i) & 1][ks][1] = cat(vtr(wb1 + so + ks * 2048), vtr(wb1 + so + ks * 2048 + 512)); } } while (0)
#define MM(i) do { _Pragma("unroll") for (int ks = 0; ks < 4; ++ks) {                                                          \
        if (ISK) { a0 = MFMA(wf[(i) & 1][ks][0], xf[i][ks], a0); a1 = MFMA(wf[(i) & 1][ks][1], xf[i][ks], a1); }               \
        else     { a0 = MFMA(xf[i][ks], wf[(i) & 1][ks][0], a0); a1 = MFMA(xf[i][ks], wf[(i) & 1][ks][1], a1); } } } while (0)
    BAR(); RD(0);
#pragma unroll
    for (int i = 0; i < 5; ++i) { BAR(); RD(i + 1); SBAR(); MM(i); SBAR(); }
    MM(5); SBAR();
    BAR(); RD(6); SBAR();
#pragma unroll
    for (int ks = 0; ks < 4; ++ks) a0 = ISK ? MFMA(wf[0][ks][0], xf[6][ks], a0) : MFMA(xf[6][ks], wf[0][ks][0], a0);
#pragma unroll
    for (int ks = 0; ks < 4; ++ks) a1 = ISK ? MFMA(wf[0][ks][1], xf[6][ks], a1) : MFMA(xf[6][ks], wf[0][ks][1], a1);
    SBAR();
#undef RD
#undef MM
}

DI void stage_dma(unsigned mw, unsigned mx, unsigned sw, unsigned sx, unsigned wvo, unsigned xvo0, unsigned xvo1, __amdgpu_buffer_rsrc_t rsW,
                  __amdgpu_buffer_rsrc_t rsX) {
    unsigned t;
    asm volatile("s_mov_b32 m0, %1\n\ts_add_u32 %0, %3, 21504\n\tbuffer_load_dwordx4 %5, %8, %3 offen lds\n\t"
                 "s_add_u32 m0, m0, 1024\n\ts_nop 0\n\tbuffer_load_dwordx4 %5, %8, %0 offen lds\n\t"
                 "s_mov_b32 m0, %2\n\ts_add_u32 %0, %4, 7168\n\tbuffer_load_dwordx4 %6, %9, %4 offen lds\n\t"
                 "s_add_u32 m0, m0, 1024\n\ts_nop 0\n\tbuffer_load_dwordx4 %7, %9, %0 offen lds\n\t"
                 "s_add_u32 m0, m0, 1024\n\ts_add_u32 %0, %4, 14336\n\tbuffer_load_dwordx4 %6, %9, %0 offen lds\n\t"
                 "s_add_u32 m0, m0, 1024\n\ts_add_u32 %0, %4, 21504\n\tbuffer_load_dwordx4 %7, %9, %0 offen lds"
                 : "=&s"(t) : "s"(mw), "s"(mx), "s"(sw), "s"(sx), "v"(wvo), "v"(xvo0), "v"(xvo1), "s"(rsW), "s"(rsX) : "m0", "scc", "memory");
}

__global__ __launch_bounds__(512, 2) void mha_kernel(const char* __restrict__ x, const char* __restrict__ wqkv, const char* __restrict__ wo,
                                                     char* __restrict__ out, char* __restrict__ ws) {
    extern __shared__ __attribute__((aligned(16))) char lds_[];
    LAS char* lds = (LAS char*)lds_;
    STAMP_DECL STAMP(0);
    const unsigned wv = rfl(threadIdx.x >> 6), w = wv & 3;
    const unsigned lds0 = (unsigned)(uintptr_t)lds_;
    GAS char* wsg = (GAS char*)ws;
#define DECODE()                                                                                                              \
    unsigned b = blockIdx.x, lane = threadIdx.x & 63;                                                                        \
    asm volatile("" : "+s"(b)); asm volatile("" : "+v"(lane));                                                                \
    const unsigned hd = MAP_HD(b), qt = MAP_QT(b);                                                                            \
    const unsigned ch = hd < 14 ? hd : hd - 2;         \
    const unsigned rot = (hd + 2 * qt) % 7;
#define DECODE2()                                                                                                             \
    const bool isK = ch < 7;                                                                                                  \
    const unsigned g0 = (7 * hd) >> 1;                                                  \
    const unsigned cg = (b >> 3) & 3, q32 = (b & 7) * 8 + (b >> 5);     \
    unsigned kbi[7];                                                                                                          \
    _Pragma("unroll") for (int i = 0; i < 7; ++i) { const unsigned k = rot + i; kbi[i] = k >= 7 ? k - 7 : k; }               \
    const __amdgpu_buffer_rsrc_t rsOI = mkrs(ws + WS_O, 2u << 20);

    if (__builtin_expect(wv >= 4, 1)) {
        DECODE();
        const unsigned xrow = qt * 128 + w * 32 + (lane >> 3);
        const unsigned xvo0 = xrow * (DM * 2) + (((lane & 7) ^ (lane >> 4)) * 16), xvo1 = xvo0 ^ 64;
        const unsigned wvo = (16 * w + (lane >> 3)) * (N3 * 2) + 896 + ch * 128 + (((lane & 7) ^ (((lane >> 4) & 1) << 2)) * 16);
        const __amdgpu_buffer_rsrc_t rsW = mkrs(wqkv, DM * N3 * 2), rsX = mkrs(x, SEQ * DM * 2);
        STAMP(14);
        stage_dma(lds0 + L_RING + 16384 + w * 2048, lds0 + L_RING + w * 4096, rot * (64 * N3 * 2), rot * 128, wvo, xvo0, xvo1, rsW, rsX);
        DECODE2();
        const __amdgpu_buffer_rsrc_t rsF = mkrs(ws + WS_F, 4096);
#define STAGE(i) stage_dma(lds0 + L_RING + ((i) % 5) * STG + 16384 + w * 2048, lds0 + L_RING + ((i) % 5) * STG + w * 4096, kbi[i] * (64 * N3 * 2), kbi[i] * 128, \
                           wvo, xvo0, xvo1, rsW, rsX)
#define VBAR(NV) asm volatile("s_waitcnt vmcnt(" #NV ")\n\ts_barrier" ::: "memory")
        STAGE(1); STAGE(2);
        STAMP(1);
        VBAR(12); STAGE(3);
        STAMP(2);
        VBAR(12); STAGE(4);
        VBAR(12); STAGE(5);
        VBAR(12); STAGE(6);
        VBAR(12);
        unsigned gen;
        { const unsigned vo = F_G; asm volatile("buffer_load_dword %0, %1, %2, 0 offen sc1" : "=v"(gen) : "v"(vo), "s"(rsF) : "memory"); }
        {
            const unsigned qvo = (16 * w + (lane >> 2)) * (N3 * 2) + g0 * 16 + (lane & 3) * 16;
#pragma unroll
            for (int j = 0; j < 7; ++j) dma16(lds0 + L_WQ + (16 * w + 64 * kbi[j]) * 64, qvo, rsW, kbi[j] * 64 * (N3 * 2));
        }
        VBAR(14); VBAR(8);
        STAMP(3);
        asm volatile("s_waitcnt vmcnt(7)" : "+v"(gen)::"memory");
        unsigned tag = rfl(gen);
        { const unsigned t = (tag ^ (tag >> 15)) * 0x2c1b3c6du + 0x9e3779b9u; tag = (t == tag) ? t + 1 : t; }
        const unsigned hh = lane & 15, wh = (lane >> 4) & 1;
        const unsigned c = isK ? (((7 * hh >> 1) + 3 * wh) >> 3) : 7 + ((28 * hh + 27 * wh) >> 6);
        GAS u64* fp = (GAS u64*)(wsg + WS_F + (lane < 32 ? F_KV + ((hh * 4 + (isK ? 0 : 2) + wh) * 16 + qt) * 128 : F_R + (qt * 16 + hd) * 8));
        const bool fon = w == 0 && ((lane < 32 && c == hd) || lane == 32);
        if (w == 0 && lane == 0) *(LAS unsigned*)(lds + L_GB + 20) = tag;
        asm volatile("s_waitcnt lgkmcnt(0)\n\ts_barrier" : "+v"(fp)::"memory");
        if (w == 0) {
            const u32x4 gb = *(LAS const u32x4*)(lds + L_GB);
            if (fon) __hip_atomic_store(fp, ((u64)max(max(gb[0], gb[1]), max(gb[2], gb[3])) << 32) | tag, RLX_AGENT);
            asm volatile("s_waitcnt vmcnt(1)" ::: "memory");
        } else {
            asm volatile("s_waitcnt vmcnt(0)" ::: "memory");
        }
        STAMP(4);
        asm volatile("s_waitcnt lgkmcnt(0)\n\ts_barrier" ::: "memory");
        LAS unsigned* ord = (LAS unsigned*)(lds + L_CB + w * 12288 + (w == 2 ? 2048 : 0));
        if (lane < 5) ord[lane] = lane == 4 ? 0x40000000u : 0xffffffffu;
        {
            const unsigned tt = w + 4 * ((((lane >> 2) & 3) + (qt >> 2)) & 3);
            const GAS u64* fl = (const GAS u64*)(wsg + WS_F + F_KV + hd * 8192 + ((lane & 3) * 16 + tt) * 128);
            u64 v = __hip_atomic_load(fl, RLX_AGENT);
            unsigned done = 0;
#pragma nounroll
            for (unsigned spins = 0; done < 4; ++spins) {
                const unsigned miss = spins < (1u << 16) ? (unsigned)__ballot((unsigned)v != tag) & 0xffffu : 0u;
                const unsigned n = miss ? __builtin_ctz(miss) >> 2 : 4;
                if (n > done) {
                    __builtin_amdgcn_fence(__ATOMIC_ACQUIRE, "wavefront");
                    if (lane < 4 * n && (lane & 3) == 0) ord[lane >> 2] = tt * (4 * TS);
                    if (done == 0) {
                        const unsigned g = (unsigned)(v >> 32);
                        if (w == (qt & 3) && lane == 0) *(LAS unsigned*)(lds + L_GB + 16) = max(g, (unsigned)__builtin_amdgcn_readlane((int)g, 1));
                        STAMP(5);
                        BAR();
                        STAMP(6);
                        asm volatile("s_barrier" ::: "memory");
                    }
                    done = n;
                }
                if ((unsigned)v != tag) v = __hip_atomic_load(fl, RLX_AGENT);
            }
        }
        st16_wt((u32x4){POISON, POISON, POISON, POISON}, rsOI, lane * 16, ((qt * 4 + w) * NH + hd) * 2048);
        st16_wt((u32x4){POISON, POISON, POISON, POISON}, rsOI, lane * 16 + 1024, ((qt * 4 + w) * NH + hd) * 2048);
        asm volatile("s_waitcnt vmcnt(0)" ::: "memory");
        if (lane == 0) __hip_atomic_store((GAS u64*)(wsg + WS_F + F_P + ((qt * 4 + w) * 16 + hd) * 8), (u64)tag, RLX_AGENT);
        if (w == 1 && b == 0) {
            const GAS u64* fr = (const GAS u64*)(wsg + WS_F + F_R) + lane;
#pragma nounroll
            for (unsigned spins = 0; spins < (1u << 16); ++spins) {
                const u64 v0 = __hip_atomic_load(fr, RLX_AGENT), v1 = __hip_atomic_load(fr + 64, RLX_AGENT);
                const u64 v2 = __hip_atomic_load(fr + 128, RLX_AGENT), v3 = __hip_atomic_load(fr + 192, RLX_AGENT);
                if (__all((unsigned)v0 == tag && (unsigned)v1 == tag && (unsigned)v2 == tag && (unsigned)v3 == tag)) break;
                __builtin_amdgcn_s_sleep(4);
            }
            if (lane == 0) __hip_atomic_store((GAS unsigned*)(wsg + WS_F + F_G), tag, RLX_AGENT);
        }
        {
            const __amdgpu_buffer_rsrc_t rsO = mkrs(wo, DM * DM * 2);
            const unsigned vow = (4 * w + (lane >> 4)) * (DM * 2) + cg * 256 + ((((lane >> 2) & 3) ^ (lane >> 4)) * 64) + (lane & 3) * 16;
#pragma unroll 1
            for (unsigned j = 0; j < 28; ++j) { dma16(lds0 + L_WO + (4 * j + w) * 1024, vow, rsO, j * 14336); __builtin_amdgcn_s_sleep(1); }
        }
        (void)poll_tags((const GAS u64*)(wsg + WS_F + F_P + q32 * 128) + w * 4 + (lane & 3), tag);
        VBAR(0);
        STAMP(8);
        asm volatile("s_barrier" ::: "memory");
        STAMP(9);
        {
            unsigned pend = 15;
#pragma nounroll
            for (unsigned spins = 0; pend && spins < (1u << 16); ++spins) {
                u32x4 t[8];
                asm volatile("" ::: "memory");
#pragma unroll
                for (int j = 0; j < 4; ++j)
                    if (pend & (1u << j)) {
                        t[2 * j] = ld16_wt(rsOI, lane * 16 + j * 2048, q32 * (NH * 2048) + w * 8192);
                        t[2 * j + 1] = ld16_wt(rsOI, lane * 16 + j * 2048 + 1024, q32 * (NH * 2048) + w * 8192);
                    }
#pragma unroll
                for (int j = 0; j < 4; ++j)
                    if (pend & (1u << j)) {
                        const u32x4 u = t[2 * j], v = t[2 * j + 1];
                        *(LAS u32x4*)(lds + L_CB + (w * 8 + 2 * j) * 1024 + lane * 16) = u;
                        *(LAS u32x4*)(lds + L_CB + (w * 8 + 2 * j + 1) * 1024 + lane * 16) = v;
                        const unsigned m = max(max(max(u[0], u[1]), max(u[2], u[3])), max(max(v[0], v[1]), max(v[2], v[3])));
                        if (__all(m != POISON)) pend &= ~(1u << j);
                    }
            }
            STAMP(10);
        }
        BAR();
        STAMP(11);
        STAMP_FLUSH();
        return;
    }

    DECODE(); DECODE2();
    const int r = lane & 31, h = lane >> 5, i16 = lane & 15, q4 = i16 >> 2, p4 = i16 & 3, g16 = (lane >> 4) & 1;
    f32x16 a0 = {}, a1 = {};
    bf16x8 xf[7][4];
    const unsigned kt = qt * 4 + w;
    unsigned gmx = 0;
    unsigned tch1 = 0;
    {
        unsigned long long pc0;
        asm volatile("s_getpc_b64 %0" : "=s"(pc0));
        tch1 = touch((const GAS char*)(pc0 + (((b >> 3) % 3) * 4 + w) * 1024 + (lane & 7) * 128));
    }
    const __amdgpu_buffer_rsrc_t rsKV = mkrs(ws, hd < 14 ? 4u << 20 : 0u);
    if (isK) {
        unsigned vo = WS_K + (8 * ch + h) * 512 + r * 16;
        asm volatile("" : "+v"(vo));
        kv_gemm<true>(lds, w, lane, xf, a0, a1);
        STAMP(3);
        st16_wt(pack8(a0, 0), rsKV, vo, kt * TS); st16_wt(pack8(a0, 1), rsKV, vo + 1024, kt * TS);
        st16_wt(pack8(a1, 0), rsKV, vo + 2048, kt * TS); st16_wt(pack8(a1, 1), rsKV, vo + 3072, kt * TS);
        SBAR();
        float s0 = 0.f, s1 = 0.f, s2 = 0.f, s3 = 0.f;
#pragma unroll
        for (int j = 0; j < 8; ++j) { s0 += a0[j] * a0[j]; s1 += a0[8 + j] * a0[8 + j]; s2 += a1[j] * a1[j]; s3 += a1[8 + j] * a1[8 + j]; }
        gmx = max(max(__float_as_uint(s0), __float_as_uint(s1)), max(__float_as_uint(s2), __float_as_uint(s3)));
        gmx = wave_max_u(gmx);
    } else {
        const unsigned n0 = 64 * (ch - 7) + r, n1 = n0 + 32;
        unsigned v0 = WS_V + h * VROW + (n0 + (n0 * 2341 >> 16)) * 16, v1 = WS_V + h * VROW + (n1 + (n1 * 2341 >> 16)) * 16;
        unsigned v2 = v0 + 2 * VROW, v3 = v1 + 2 * VROW;
        const unsigned hc = (64 * (ch - 7) * 2341 >> 16) + (lane >> 2), last = 28 * hc + 27;
        if (lane < 16 && (last >> 6) == ch - 7)
            st16_wt((u32x4){0x3f803f80u, 0x3f803f80u, 0x3f803f80u, 0x3f803f80u}, rsKV, WS_V + (lane & 3) * VROW + (29 * hc + 28) * 16, kt * TS);
        asm volatile("" : "+v"(v0), "+v"(v1), "+v"(v2), "+v"(v3));
        kv_gemm<false>(lds, w, lane, xf, a0, a1);
        STAMP(3);
        st16_wt(pack8(a0, 0), rsKV, v0, kt * TS); st16_wt(pack8(a0, 1), rsKV, v2, kt * TS);
        st16_wt(pack8(a1, 0), rsKV, v1, kt * TS); st16_wt(pack8(a1, 1), rsKV, v3, kt * TS);
        SBAR();
    }
    if (lane == 0) *(LAS unsigned*)(lds + L_GB + w * 4) = gmx;
    unsigned vok = WS_K + (g0 + h) * 512 + r * 16, vov = r <= DH ? WS_V + h * VROW + (29 * hd + r) * 16 : 0x70000000u;
    const __amdgpu_buffer_rsrc_t rsKVr = mkrs(ws, 4u << 20);
    unsigned q2a = lds0 + L_Q2 + r * 16, kba = lds0 + L_GB + 16, ordn = lds0 + L_CB + w * 12288 + (w == 2 ? 2048 : 0) + 4, puta = lds0 + L_CB + w * 12288 + lane * 16;
    unsigned qxa[4];
#pragma unroll
    for (int i = 0; i < 4; ++i) qxa[i] = lds0 + L_QX + ((w + 1 + i) & 3) * 2048 + lane * 16;
    LAS const char* qb = lds + L_WQ + (8 * h + q4) * 64 + g16 * 32 + (((p4 & 1) << 1) | (p4 >> 1)) * 8;
    asm volatile("" : "+v"(vok), "+v"(vov), "+v"(q2a), "+v"(kba), "+v"(ordn), "+v"(puta), "+v"(qxa[0]), "+v"(qxa[1]), "+v"(qxa[2]), "+v"(qxa[3]), "+v"(qb));
    STAMP(1);
    asm volatile("s_waitcnt vmcnt(0) lgkmcnt(0)" : "+v"(tch1)::"memory");
    STAMP(2);
    asm volatile("s_barrier" ::: "memory");
    STAMP(4);
    asm volatile("s_barrier" ::: "memory");
    STAMP(14);
    const unsigned tag = rfl(*(LAS const unsigned*)(lds + L_GB + 20));
    f32x16 aq = {};
    {
        bf16x8 wq[3][4];
#define RDQ(i) do { LAS const char* qk = qb + kbi[i] * 4096; _Pragma("unroll") for (int ks = 0; ks < 4; ++ks)                  \
            wq[(i) % 3][ks] = cat(vtr(qk + ks * 1024), vtr(qk + ks * 1024 + 256)); } while (0)
        RDQ(0); RDQ(1); SBAR();
#pragma unroll
        for (int i = 0; i < 7; ++i) {
            if (i + 2 < 7) RDQ(i + 2);
            SBAR();
#pragma unroll
            for (int ks = 0; ks < 4; ++ks) aq = MFMA(wq[i % 3][ks], xf[i][ks], aq);
            SBAR();
        }
#undef RDQ
    }
    asm volatile("" : "+v"(aq));
    STAMP(15);
    {
        const bool zlo = (hd & 1) && !h, zhi = !(hd & 1) && h;
        float qs[4] = {0.f, 0.f, 0.f, 0.f};
#pragma unroll
        for (int i = 0; i < 16; ++i) {
            aq[i] *= QSCALE;
            if (i < 4) aq[i] = zlo ? 0.f : aq[i];
            if (i >= 12) aq[i] = zhi ? 0.f : aq[i];
            qs[i & 3] += aq[i] * aq[i];
        }
        *(LAS u32x4*)(lds + (qxa[3] - lds0)) = pack8(aq, 0);
        *(LAS u32x4*)(lds + (qxa[3] - lds0) + 1024) = pack8(aq, 1);
        float q2 = (qs[0] + qs[1]) + (qs[2] + qs[3]);
        q2 = half_lo(q2) + half_hi(q2);
        *(LAS float*)(lds + (q2a - lds0) + w * 4) = q2;
    }
    STAMP(5);

    f32x16 acc;
    u32x4 qo0, qo1;
    {
        unsigned sb = (w + (qt & 12)) * (4 * TS), cnt = 4;
        asm volatile(
        "s_setprio 2\n\t"
        "v_mov_b64 v[0:1], 0\n\t"
        "v_mov_b64 v[2:3], 0\n\t"
        "v_mov_b64 v[4:5], 0\n\t"
        "v_mov_b64 v[6:7], 0\n\t"
        "v_mov_b64 v[8:9], 0\n\t"
        "v_mov_b64 v[10:11], 0\n\t"
        "v_mov_b64 v[12:13], 0\n\t"
        "v_mov_b64 v[14:15], 0\n\t"
        "v_mov_b64 v[16:17], 0\n\t"
        "v_mov_b64 v[18:19], 0\n\t"
        "v_mov_b64 v[20:21], 0\n\t"
        "v_mov_b64 v[22:23], 0\n\t"
        "v_mov_b64 v[24:25], 0\n\t"
        "v_mov_b64 v[26:27], 0\n\t"
        "v_mov_b64 v[28:29], 0\n\t"
        "v_mov_b64 v[30:31], 0\n\t"
        "v_mov_b64 v[32:33], 0\n\t"
        "v_mov_b64 v[34:35], 0\n\t"
        "v_mov_b64 v[36:37], 0\n\t"
        "v_mov_b64 v[38:39], 0\n\t"
        "v_mov_b64 v[40:41], 0\n\t"
        "v_mov_b64 v[42:43], 0\n\t"
        "v_mov_b64 v[44:45], 0\n\t"
        "v_mov_b64 v[46:47], 0\n\t"
        "v_mov_b64 v[48:49], 0\n\t"
        "v_mov_b64 v[50:51], 0\n\t"
        "v_mov_b64 v[52:53], 0\n\t"
        "v_mov_b64 v[54:55], 0\n\t"
        "v_mov_b64 v[56:57], 0\n\t"
        "v_mov_b64 v[58:59], 0\n\t"
        "v_mov_b64 v[60:61], 0\n\t"
        "v_mov_b64 v[62:63], 0\n\t"
        "v_mov_b64 v[152:153], 0\n\t"
        "v_mov_b64 v[154:155], 0\n\t"
        "v_mov_b64 v[156:157], 0\n\t"
        "v_mov_b64 v[158:159], 0\n\t"
        "v_mov_b64 v[216:217], 0\n\t"
        "v_mov_b64 v[218:219], 0\n\t"
        "v_mov_b64 v[220:221], 0\n\t"
        "v_mov_b64 v[222:223], 0\n\t"
        "s_waitcnt lgkmcnt(0)\n\t"
        "s_barrier\n\t"
        "buffer_load_dwordx4 v[160:163], v224, %[rs], %[sb] offen sc1\n\t"
        "buffer_load_dwordx4 v[164:167], v224, %[rs], %[sb] offen offset:1024 sc1\n\t"
        "buffer_load_dwordx4 v[168:171], v225, %[rs], %[sb] offen sc1\n\t"
        "buffer_load_dwordx4 v[172:175], v226, %[rs], %[sb] offen sc1\n\t"
        "ds_read_b128 v[80:83], v236\n\t"
        "ds_read_b32 v237, v237\n\t"
        "ds_read_b128 v[112:115], v241\n\t"
        "ds_read_b128 v[116:119], v241 offset:1024\n\t"
        "ds_read_b128 v[120:123], v242\n\t"
        "ds_read_b128 v[124:127], v242 offset:1024\n\t"
        "ds_read_b128 v[128:131], v243\n\t"
        "ds_read_b128 v[132:135], v243 offset:1024\n\t"
        "ds_read_b128 v[136:139], v244\n\t"
        "ds_read_b128 v[140:143], v244 offset:1024\n\t"
        "s_waitcnt lgkmcnt(0)\n\t"
        "s_barrier\n\t"
        "v_max3_f32 v236, v80, v81, v82\n\t"
        "v_max_f32 v236, v236, v83\n\t"
        "v_mul_f32 v236, v236, v237\n\t"
        "v_sqrt_f32 v236, v236\n\t"
        "s_nop 0\n\t"
        "v_mul_f32 v96, 0xc0028f5c, v236\n\t"
        "v_mov_b32 v97, v96\n\t"
        "v_mov_b64 v[98:99], v[96:97]\n\t"
        "v_mov_b64 v[100:101], v[96:97]\n\t"
        "v_mov_b64 v[102:103], v[96:97]\n\t"
        "v_mov_b64 v[104:105], v[96:97]\n\t"
        "v_mov_b64 v[106:107], v[96:97]\n\t"
        "v_mov_b64 v[108:109], v[96:97]\n\t"
        "v_mov_b64 v[110:111], v[96:97]\n\t"
        "buffer_load_dwordx4 v[176:179], v227, %[rs], %[sb] offen sc1\n\t"
        "buffer_load_dwordx4 v[180:183], v227, %[rs], %[sb] offen offset:1024 sc1\n\t"
        "buffer_load_dwordx4 v[184:187], v228, %[rs], %[sb] offen sc1\n\t"
        "buffer_load_dwordx4 v[188:191], v229, %[rs], %[sb] offen sc1\n\t"
        "s_waitcnt vmcnt(4)\n\t"
        "v_mfma_f32_32x32x16_bf16 v[64:79], v[160:163], v[112:115], v[96:111]\n\t"
        "v_mfma_f32_32x32x16_bf16 v[64:79], v[164:167], v[116:119], v[64:79]\n\t"
        "buffer_load_dwordx4 v[192:195], v230, %[rs], %[sb] offen sc1\n\t"
        "buffer_load_dwordx4 v[196:199], v230, %[rs], %[sb] offen offset:1024 sc1\n\t"
        "buffer_load_dwordx4 v[200:203], v231, %[rs], %[sb] offen sc1\n\t"
        "buffer_load_dwordx4 v[204:207], v232, %[rs], %[sb] offen sc1\n\t"
        "s_nop 9\n\t"
        "LOOP%=:\n\t"
        "v_mfma_f32_32x32x16_bf16 v[80:95], v[160:163], v[120:123], v[96:111]\n\t"
        "v_exp_f32 v64, v64\n\t"
        "v_exp_f32 v65, v65\n\t"
        "v_exp_f32 v66, v66\n\t"
        "v_exp_f32 v67, v67\n\t"
        "v_mfma_f32_32x32x16_bf16 v[80:95], v[164:167], v[124:127], v[80:95]\n\t"
        "v_exp_f32 v68, v68\n\t"
        "v_exp_f32 v69, v69\n\t"
        "v_exp_f32 v70, v70\n\t"
        "v_exp_f32 v71, v71\n\t"
        "v_cvt_pk_bf16_f32 v144, v64, v65\n\t"
        "v_cvt_pk_bf16_f32 v145, v66, v67\n\t"
        "v_mfma_f32_32x32x16_bf16 v[48:63], v[216:219], v[152:155], v[48:63]\n\t"
        "v_exp_f32 v72, v72\n\t"
        "v_exp_f32 v73, v73\n\t"
        "v_exp_f32 v74, v74\n\t"
        "v_exp_f32 v75, v75\n\t"
        "v_cvt_pk_bf16_f32 v146, v68, v69\n\t"
        "v_cvt_pk_bf16_f32 v147, v70, v71\n\t"
        "v_mfma_f32_32x32x16_bf16 v[48:63], v[220:223], v[156:159], v[48:63]\n\t"
        "v_exp_f32 v76, v76\n\t"
        "v_exp_f32 v77, v77\n\t"
        "v_exp_f32 v78, v78\n\t"
        "v_exp_f32 v79, v79\n\t"
        "v_cvt_pk_bf16_f32 v148, v72, v73\n\t"
        "v_cvt_pk_bf16_f32 v149, v74, v75\n\t"
        "v_cvt_pk_bf16_f32 v150, v76, v77\n\t"
        "v_cvt_pk_bf16_f32 v151, v78, v79\n\t"
        "v_mfma_f32_32x32x16_bf16 v[64:79], v[160:163], v[128:131], v[96:111]\n\t"
        "buffer_load_dwordx4 v[208:211], v233, %[rs], %[sb] offen sc1\n\t"
        "buffer_load_dwordx4 v[212:215], v233, %[rs], %[sb] offen offset:1024 sc1\n\t"
        "buffer_load_dwordx4 v[216:219], v234, %[rs], %[sb] offen sc1\n\t"
        "buffer_load_dwordx4 v[220:223], v235, %[rs], %[sb] offen sc1\n\t"
        "v_exp_f32 v80, v80\n\t"
        "v_exp_f32 v81, v81\n\t"
        "v_exp_f32 v82, v82\n\t"
        "v_exp_f32 v83, v83\n\t"
        "v_mfma_f32_32x32x16_bf16 v[64:79], v[164:167], v[132:135], v[64:79]\n\t"
        "v_exp_f32 v84, v84\n\t"
        "v_exp_f32 v85, v85\n\t"
        "v_exp_f32 v86, v86\n\t"
        "v_exp_f32 v87, v87\n\t"
        "v_cvt_pk_bf16_f32 v152, v80, v81\n\t"
        "v_cvt_pk_bf16_f32 v153, v82, v83\n\t"
        "v_mfma_f32_32x32x16_bf16 v[0:15], v[168:171], v[144:147], v[0:15]\n\t"
        "v_exp_f32 v88, v88\n\t"
        "v_exp_f32 v89, v89\n\t"
        "v_exp_f32 v90, v90\n\t"
        "v_exp_f32 v91, v91\n\t"
        "v_cvt_pk_bf16_f32 v154, v84, v85\n\t"
        "v_cvt_pk_bf16_f32 v155, v86, v87\n\t"
        "v_mfma_f32_32x32x16_bf16 v[0:15], v[172:175], v[148:151], v[0:15]\n\t"
        "v_exp_f32 v92, v92\n\t"
        "v_exp_f32 v93, v93\n\t"
        "v_exp_f32 v94, v94\n\t"
        "v_exp_f32 v95, v95\n\t"
        "v_cvt_pk_bf16_f32 v156, v88, v89\n\t"
        "v_cvt_pk_bf16_f32 v157, v90, v91\n\t"
        "v_cvt_pk_bf16_f32 v158, v92, v93\n\t"
        "v_cvt_pk_bf16_f32 v159, v94, v95\n\t"
        "v_mfma_f32_32x32x16_bf16 v[80:95], v[160:163], v[136:139], v[96:111]\n\t"
        "ds_read_b32 v239, v238\n\t"
        "v_exp_f32 v64, v64\n\t"
        "v_exp_f32 v65, v65\n\t"
        "v_exp_f32 v66, v66\n\t"
        "v_exp_f32 v67, v67\n\t"
        "v_mfma_f32_32x32x16_bf16 v[80:95], v[164:167], v[140:143], v[80:95]\n\t"
        "v_exp_f32 v68, v68\n\t"
        "v_exp_f32 v69, v69\n\t"
        "v_exp_f32 v70, v70\n\t"
        "v_exp_f32 v71, v71\n\t"
        "v_cvt_pk_bf16_f32 v144, v64, v65\n\t"
        "v_cvt_pk_bf16_f32 v145, v66, v67\n\t"
        "v_mfma_f32_32x32x16_bf16 v[16:31], v[168:171], v[152:155], v[16:31]\n\t"
        "v_exp_f32 v72, v72\n\t"
        "v_exp_f32 v73, v73\n\t"
        "v_exp_f32 v74, v74\n\t"
        "v_exp_f32 v75, v75\n\t"
        "v_cvt_pk_bf16_f32 v146, v68, v69\n\t"
        "v_cvt_pk_bf16_f32 v147, v70, v71\n\t"
        "v_mfma_f32_32x32x16_bf16 v[16:31], v[172:175], v[156:159], v[16:31]\n\t"
        "v_exp_f32 v76, v76\n\t"
        "v_exp_f32 v77, v77\n\t"
        "v_exp_f32 v78, v78\n\t"
        "v_exp_f32 v79, v79\n\t"
        "v_cvt_pk_bf16_f32 v148, v72, v73\n\t"
        "v_cvt_pk_bf16_f32 v149, v74, v75\n\t"
        "v_cvt_pk_bf16_f32 v150, v76, v77\n\t"
        "v_cvt_pk_bf16_f32 v151, v78, v79\n\t"
        "s_waitcnt vmcnt(8)\n\t"
        "v_mfma_f32_32x32x16_bf16 v[64:79], v[176:179], v[112:115], v[96:111]\n\t"
        "v_exp_f32 v80, v80\n\t"
        "v_exp_f32 v81, v81\n\t"
        "v_exp_f32 v82, v82\n\t"
        "v_exp_f32 v83, v83\n\t"
        "v_mfma_f32_32x32x16_bf16 v[64:79], v[180:183], v[116:119], v[64:79]\n\t"
        "v_exp_f32 v84, v84\n\t"
        "v_exp_f32 v85, v85\n\t"
        "v_exp_f32 v86, v86\n\t"
        "v_exp_f32 v87, v87\n\t"
        "v_cvt_pk_bf16_f32 v152, v80, v81\n\t"
        "v_cvt_pk_bf16_f32 v153, v82, v83\n\t"
        "v_mfma_f32_32x32x16_bf16 v[32:47], v[168:171], v[144:147], v[32:47]\n\t"
        "v_exp_f32 v88, v88\n\t"
        "v_exp_f32 v89, v89\n\t"
        "v_exp_f32 v90, v90\n\t"
        "v_exp_f32 v91, v91\n\t"
        "v_cvt_pk_bf16_f32 v154, v84, v85\n\t"
        "v_cvt_pk_bf16_f32 v155, v86, v87\n\t"
        "v_mfma_f32_32x32x16_bf16 v[32:47], v[172:175], v[148:151], v[32:47]\n\t"
        "v_exp_f32 v92, v92\n\t"
        "v_exp_f32 v93, v93\n\t"
        "v_exp_f32 v94, v94\n\t"
        "v_exp_f32 v95, v95\n\t"
        "v_cvt_pk_bf16_f32 v156, v88, v89\n\t"
        "v_cvt_pk_bf16_f32 v157, v90, v91\n\t"
        "v_cvt_pk_bf16_f32 v158, v92, v93\n\t"
        "v_cvt_pk_bf16_f32 v159, v94, v95\n\t"
        "v_mfma_f32_32x32x16_bf16 v[80:95], v[176:179], v[120:123], v[96:111]\n\t"
        "CHK%=:\n\t"
        "s_waitcnt lgkmcnt(0)\n\t"
        "v_readfirstlane_b32 %[sb], v239\n\t"
        "ds_read_b32 v239, v238\n\t"
        "s_cmp_eq_u32 %[sb], -1\n\t"
        "s_cbranch_scc1 CHK%=\n\t"
        "v_add_u32 v238, 4, v238\n\t"
        "v_exp_f32 v64, v64\n\t"
        "v_exp_f32 v65, v65\n\t"
        "v_exp_f32 v66, v66\n\t"
        "v_exp_f32 v67, v67\n\t"
        "v_mfma_f32_32x32x16_bf16 v[80:95], v[180:183], v[124:127], v[80:95]\n\t"
        "v_exp_f32 v68, v68\n\t"
        "v_exp_f32 v69, v69\n\t"
        "v_exp_f32 v70, v70\n\t"
        "v_exp_f32 v71, v71\n\t"
        "v_cvt_pk_bf16_f32 v144, v64, v65\n\t"
        "v_cvt_pk_bf16_f32 v145, v66, v67\n\t"
        "v_mfma_f32_32x32x16_bf16 v[48:63], v[168:171], v[152:155], v[48:63]\n\t"
        "v_exp_f32 v72, v72\n\t"
        "v_exp_f32 v73, v73\n\t"
        "v_exp_f32 v74, v74\n\t"
        "v_exp_f32 v75, v75\n\t"
        "v_cvt_pk_bf16_f32 v146, v68, v69\n\t"
        "v_cvt_pk_bf16_f32 v147, v70, v71\n\t"
        "v_mfma_f32_32x32x16_bf16 v[48:63], v[172:175], v[156:159], v[48:63]\n\t"
        "v_exp_f32 v76, v76\n\t"
        "v_exp_f32 v77, v77\n\t"
        "v_exp_f32 v78, v78\n\t"
        "v_exp_f32 v79, v79\n\t"
        "v_cvt_pk_bf16_f32 v148, v72, v73\n\t"
        "v_cvt_pk_bf16_f32 v149, v74, v75\n\t"
        "v_cvt_pk_bf16_f32 v150, v76, v77\n\t"
        "v_cvt_pk_bf16_f32 v151, v78, v79\n\t"
        "v_mfma_f32_32x32x16_bf16 v[64:79], v[176:179], v[128:131], v[96:111]\n\t"
        "buffer_load_dwordx4 v[160:163], v224, %[rs], %[sb] offen sc1\n\t"
        "buffer_load_dwordx4 v[164:167], v224, %[rs], %[sb] offen offset:1024 sc1\n\t"
        "buffer_load_dwordx4 v[168:171], v225, %[rs], %[sb] offen sc1\n\t"
        "buffer_load_dwordx4 v[172:175], v226, %[rs], %[sb] offen sc1\n\t"
        "v_exp_f32 v80, v80\n\t"
        "v_exp_f32 v81, v81\n\t"
        "v_exp_f32 v82, v82\n\t"
        "v_exp_f32 v83, v83\n\t"
        "v_mfma_f32_32x32x16_bf16 v[64:79], v[180:183], v[132:135], v[64:79]\n\t"
        "v_exp_f32 v84, v84\n\t"
        "v_exp_f32 v85, v85\n\t"
        "v_exp_f32 v86, v86\n\t"
        "v_exp_f32 v87, v87\n\t"
        "v_cvt_pk_bf16_f32 v152, v80, v81\n\t"
        "v_cvt_pk_bf16_f32 v153, v82, v83\n\t"
        "v_mfma_f32_32x32x16_bf16 v[0:15], v[184:187], v[144:147], v[0:15]\n\t"
        "v_exp_f32 v88, v88\n\t"
        "v_exp_f32 v89, v89\n\t"
        "v_exp_f32 v90, v90\n\t"
        "v_exp_f32 v91, v91\n\t"
        "v_cvt_pk_bf16_f32 v154, v84, v85\n\t"
        "v_cvt_pk_bf16_f32 v155, v86, v87\n\t"
        "v_mfma_f32_32x32x16_bf16 v[0:15], v[188:191], v[148:151], v[0:15]\n\t"
        "v_exp_f32 v92, v92\n\t"
        "v_exp_f32 v93, v93\n\t"
        "v_exp_f32 v94, v94\n\t"
        "v_exp_f32 v95, v95\n\t"
        "v_cvt_pk_bf16_f32 v156, v88, v89\n\t"
        "v_cvt_pk_bf16_f32 v157, v90, v91\n\t"
        "v_cvt_pk_bf16_f32 v158, v92, v93\n\t"
        "v_cvt_pk_bf16_f32 v159, v94, v95\n\t"
        "v_mfma_f32_32x32x16_bf16 v[80:95], v[176:179], v[136:139], v[96:111]\n\t"
        "v_exp_f32 v64, v64\n\t"
        "v_exp_f32 v65, v65\n\t"
        "v_exp_f32 v66, v66\n\t"
        "v_exp_f32 v67, v67\n\t"
        "v_mfma_f32_32x32x16_bf16 v[80:95], v[180:183], v[140:143], v[80:95]\n\t"
        "v_exp_f32 v68, v68\n\t"
        "v_exp_f32 v69, v69\n\t"
        "v_exp_f32 v70, v70\n\t"
        "v_exp_f32 v71, v71\n\t"
        "v_cvt_pk_bf16_f32 v144, v64, v65\n\t"
        "v_cvt_pk_bf16_f32 v145, v66, v67\n\t"
        "v_mfma_f32_32x32x16_bf16 v[16:31], v[184:187], v[152:155], v[16:31]\n\t"
        "v_exp_f32 v72, v72\n\t"
        "v_exp_f32 v73, v73\n\t"
        "v_exp_f32 v74, v74\n\t"
        "v_exp_f32 v75, v75\n\t"
        "v_cvt_pk_bf16_f32 v146, v68, v69\n\t"
        "v_cvt_pk_bf16_f32 v147, v70, v71\n\t"
        "v_mfma_f32_32x32x16_bf16 v[16:31], v[188:191], v[156:159], v[16:31]\n\t"
        "v_exp_f32 v76, v76\n\t"
        "v_exp_f32 v77, v77\n\t"
        "v_exp_f32 v78, v78\n\t"
        "v_exp_f32 v79, v79\n\t"
        "v_cvt_pk_bf16_f32 v148, v72, v73\n\t"
        "v_cvt_pk_bf16_f32 v149, v74, v75\n\t"
        "v_cvt_pk_bf16_f32 v150, v76, v77\n\t"
        "v_cvt_pk_bf16_f32 v151, v78, v79\n\t"
        "s_waitcnt vmcnt(8)\n\t"
        "v_mfma_f32_32x32x16_bf16 v[64:79], v[192:195], v[112:115], v[96:111]\n\t"
        "v_exp_f32 v80, v80\n\t"
        "v_exp_f32 v81, v81\n\t"
        "v_exp_f32 v82, v82\n\t"
        "v_exp_f32 v83, v83\n\t"
        "v_mfma_f32_32x32x16_bf16 v[64:79], v[196:199], v[116:119], v[64:79]\n\t"
        "v_exp_f32 v84, v84\n\t"
        "v_exp_f32 v85, v85\n\t"
        "v_exp_f32 v86, v86\n\t"
        "v_exp_f32 v87, v87\n\t"
        "v_cvt_pk_bf16_f32 v152, v80, v81\n\t"
        "v_cvt_pk_bf16_f32 v153, v82, v83\n\t"
        "v_mfma_f32_32x32x16_bf16 v[32:47], v[184:187], v[144:147], v[32:47]\n\t"
        "v_exp_f32 v88, v88\n\t"
        "v_exp_f32 v89, v89\n\t"
        "v_exp_f32 v90, v90\n\t"
        "v_exp_f32 v91, v91\n\t"
        "v_cvt_pk_bf16_f32 v154, v84, v85\n\t"
        "v_cvt_pk_bf16_f32 v155, v86, v87\n\t"
        "v_mfma_f32_32x32x16_bf16 v[32:47], v[188:191], v[148:151], v[32:47]\n\t"
        "v_exp_f32 v92, v92\n\t"
        "v_exp_f32 v93, v93\n\t"
        "v_exp_f32 v94, v94\n\t"
        "v_exp_f32 v95, v95\n\t"
        "v_cvt_pk_bf16_f32 v156, v88, v89\n\t"
        "v_cvt_pk_bf16_f32 v157, v90, v91\n\t"
        "v_cvt_pk_bf16_f32 v158, v92, v93\n\t"
        "v_cvt_pk_bf16_f32 v159, v94, v95\n\t"
        "v_mfma_f32_32x32x16_bf16 v[80:95], v[192:195], v[120:123], v[96:111]\n\t"
        "v_exp_f32 v64, v64\n\t"
        "v_exp_f32 v65, v65\n\t"
        "v_exp_f32 v66, v66\n\t"
        "v_exp_f32 v67, v67\n\t"
        "v_mfma_f32_32x32x16_bf16 v[80:95], v[196:199], v[124:127], v[80:95]\n\t"
        "v_exp_f32 v68, v68\n\t"
        "v_exp_f32 v69, v69\n\t"
        "v_exp_f32 v70, v70\n\t"
        "v_exp_f32 v71, v71\n\t"
        "v_cvt_pk_bf16_f32 v144, v64, v65\n\t"
        "v_cvt_pk_bf16_f32 v145, v66, v67\n\t"
        "v_mfma_f32_32x32x16_bf16 v[48:63], v[184:187], v[152:155], v[48:63]\n\t"
        "v_exp_f32 v72, v72\n\t"
        "v_exp_f32 v73, v73\n\t"
        "v_exp_f32 v74, v74\n\t"
        "v_exp_f32 v75, v75\n\t"
        "v_cvt_pk_bf16_f32 v146, v68, v69\n\t"
        "v_cvt_pk_bf16_f32 v147, v70, v71\n\t"
        "v_mfma_f32_32x32x16_bf16 v[48:63], v[188:191], v[156:159], v[48:63]\n\t"
        "v_exp_f32 v76, v76\n\t"
        "v_exp_f32 v77, v77\n\t"
        "v_exp_f32 v78, v78\n\t"
        "v_exp_f32 v79, v79\n\t"
        "v_cvt_pk_bf16_f32 v148, v72, v73\n\t"
        "v_cvt_pk_bf16_f32 v149, v74, v75\n\t"
        "v_cvt_pk_bf16_f32 v150, v76, v77\n\t"
        "v_cvt_pk_bf16_f32 v151, v78, v79\n\t"
        "v_mfma_f32_32x32x16_bf16 v[64:79], v[192:195], v[128:131], v[96:111]\n\t"
        "buffer_load_dwordx4 v[176:179], v227, %[rs], %[sb] offen sc1\n\t"
        "buffer_load_dwordx4 v[180:183], v227, %[rs], %[sb] offen offset:1024 sc1\n\t"
        "buffer_load_dwordx4 v[184:187], v228, %[rs], %[sb] offen sc1\n\t"
        "buffer_load_dwordx4 v[188:191], v229, %[rs], %[sb] offen sc1\n\t"
        "v_exp_f32 v80, v80\n\t"
        "v_exp_f32 v81, v81\n\t"
        "v_exp_f32 v82, v82\n\t"
        "v_exp_f32 v83, v83\n\t"
        "v_mfma_f32_32x32x16_bf16 v[64:79], v[196:199], v[132:135], v[64:79]\n\t"
        "v_exp_f32 v84, v84\n\t"
        "v_exp_f32 v85, v85\n\t"
        "v_exp_f32 v86, v86\n\t"
        "v_exp_f32 v87, v87\n\t"
        "v_cvt_pk_bf16_f32 v152, v80, v81\n\t"
        "v_cvt_pk_bf16_f32 v153, v82, v83\n\t"
        "v_mfma_f32_32x32x16_bf16 v[0:15], v[200:203], v[144:147], v[0:15]\n\t"
        "v_exp_f32 v88, v88\n\t"
        "v_exp_f32 v89, v89\n\t"
        "v_exp_f32 v90, v90\n\t"
        "v_exp_f32 v91, v91\n\t"
        "v_cvt_pk_bf16_f32 v154, v84, v85\n\t"
        "v_cvt_pk_bf16_f32 v155, v86, v87\n\t"
        "v_mfma_f32_32x32x16_bf16 v[0:15], v[204:207], v[148:151], v[0:15]\n\t"
        "v_exp_f32 v92, v92\n\t"
        "v_exp_f32 v93, v93\n\t"
        "v_exp_f32 v94, v94\n\t"
        "v_exp_f32 v95, v95\n\t"
        "v_cvt_pk_bf16_f32 v156, v88, v89\n\t"
        "v_cvt_pk_bf16_f32 v157, v90, v91\n\t"
        "v_cvt_pk_bf16_f32 v158, v92, v93\n\t"
        "v_cvt_pk_bf16_f32 v159, v94, v95\n\t"
        "v_mfma_f32_32x32x16_bf16 v[80:95], v[192:195], v[136:139], v[96:111]\n\t"
        "v_exp_f32 v64, v64\n\t"
        "v_exp_f32 v65, v65\n\t"
        "v_exp_f32 v66, v66\n\t"
        "v_exp_f32 v67, v67\n\t"
        "v_mfma_f32_32x32x16_bf16 v[80:95], v[196:199], v[140:143], v[80:95]\n\t"
        "v_exp_f32 v68, v68\n\t"
        "v_exp_f32 v69, v69\n\t"
        "v_exp_f32 v70, v70\n\t"
        "v_exp_f32 v71, v71\n\t"
        "v_cvt_pk_bf16_f32 v144, v64, v65\n\t"
        "v_cvt_pk_bf16_f32 v145, v66, v67\n\t"
        "v_mfma_f32_32x32x16_bf16 v[16:31], v[200:203], v[152:155], v[16:31]\n\t"
        "v_exp_f32 v72, v72\n\t"
        "v_exp_f32 v73, v73\n\t"
        "v_exp_f32 v74, v74\n\t"
        "v_exp_f32 v75, v75\n\t"
        "v_cvt_pk_bf16_f32 v146, v68, v69\n\t"
        "v_cvt_pk_bf16_f32 v147, v70, v71\n\t"
        "v_mfma_f32_32x32x16_bf16 v[16:31], v[204:207], v[156:159], v[16:31]\n\t"
        "v_exp_f32 v76, v76\n\t"
        "v_exp_f32 v77, v77\n\t"
        "v_exp_f32 v78, v78\n\t"
        "v_exp_f32 v79, v79\n\t"
        "v_cvt_pk_bf16_f32 v148, v72, v73\n\t"
        "v_cvt_pk_bf16_f32 v149, v74, v75\n\t"
        "v_cvt_pk_bf16_f32 v150, v76, v77\n\t"
        "v_cvt_pk_bf16_f32 v151, v78, v79\n\t"
        "s_waitcnt vmcnt(8)\n\t"
        "v_mfma_f32_32x32x16_bf16 v[64:79], v[208:211], v[112:115], v[96:111]\n\t"
        "v_exp_f32 v80, v80\n\t"
        "v_exp_f32 v81, v81\n\t"
        "v_exp_f32 v82, v82\n\t"
        "v_exp_f32 v83, v83\n\t"
        "v_mfma_f32_32x32x16_bf16 v[64:79], v[212:215], v[116:119], v[64:79]\n\t"
        "v_exp_f32 v84, v84\n\t"
        "v_exp_f32 v85, v85\n\t"
        "v_exp_f32 v86, v86\n\t"
        "v_exp_f32 v87, v87\n\t"
        "v_cvt_pk_bf16_f32 v152, v80, v81\n\t"
        "v_cvt_pk_bf16_f32 v153, v82, v83\n\t"
        "v_mfma_f32_32x32x16_bf16 v[32:47], v[200:203], v[144:147], v[32:47]\n\t"
        "v_exp_f32 v88, v88\n\t"
        "v_exp_f32 v89, v89\n\t"
        "v_exp_f32 v90, v90\n\t"
        "v_exp_f32 v91, v91\n\t"
        "v_cvt_pk_bf16_f32 v154, v84, v85\n\t"
        "v_cvt_pk_bf16_f32 v155, v86, v87\n\t"
        "v_mfma_f32_32x32x16_bf16 v[32:47], v[204:207], v[148:151], v[32:47]\n\t"
        "v_exp_f32 v92, v92\n\t"
        "v_exp_f32 v93, v93\n\t"
        "v_exp_f32 v94, v94\n\t"
        "v_exp_f32 v95, v95\n\t"
        "v_cvt_pk_bf16_f32 v156, v88, v89\n\t"
        "v_cvt_pk_bf16_f32 v157, v90, v91\n\t"
        "v_cvt_pk_bf16_f32 v158, v92, v93\n\t"
        "v_cvt_pk_bf16_f32 v159, v94, v95\n\t"
        "v_mfma_f32_32x32x16_bf16 v[80:95], v[208:211], v[120:123], v[96:111]\n\t"
        "v_exp_f32 v64, v64\n\t"
        "v_exp_f32 v65, v65\n\t"
        "v_exp_f32 v66, v66\n\t"
        "v_exp_f32 v67, v67\n\t"
        "v_mfma_f32_32x32x16_bf16 v[80:95], v[212:215], v[124:127], v[80:95]\n\t"
        "v_exp_f32 v68, v68\n\t"
        "v_exp_f32 v69, v69\n\t"
        "v_exp_f32 v70, v70\n\t"
        "v_exp_f32 v71, v71\n\t"
        "v_cvt_pk_bf16_f32 v144, v64, v65\n\t"
        "v_cvt_pk_bf16_f32 v145, v66, v67\n\t"
        "v_mfma_f32_32x32x16_bf16 v[48:63], v[200:203], v[152:155], v[48:63]\n\t"
        "v_exp_f32 v72, v72\n\t"
        "v_exp_f32 v73, v73\n\t"
        "v_exp_f32 v74, v74\n\t"
        "v_exp_f32 v75, v75\n\t"
        "v_cvt_pk_bf16_f32 v146, v68, v69\n\t"
        "v_cvt_pk_bf16_f32 v147, v70, v71\n\t"
        "v_mfma_f32_32x32x16_bf16 v[48:63], v[204:207], v[156:159], v[48:63]\n\t"
        "v_exp_f32 v76, v76\n\t"
        "v_exp_f32 v77, v77\n\t"
        "v_exp_f32 v78, v78\n\t"
        "v_exp_f32 v79, v79\n\t"
        "v_cvt_pk_bf16_f32 v148, v72, v73\n\t"
        "v_cvt_pk_bf16_f32 v149, v74, v75\n\t"
        "v_cvt_pk_bf16_f32 v150, v76, v77\n\t"
        "v_cvt_pk_bf16_f32 v151, v78, v79\n\t"
        "v_mfma_f32_32x32x16_bf16 v[64:79], v[208:211], v[128:131], v[96:111]\n\t"
        "buffer_load_dwordx4 v[192:195], v230, %[rs], %[sb] offen sc1\n\t"
        "buffer_load_dwordx4 v[196:199], v230, %[rs], %[sb] offen offset:1024 sc1\n\t"
        "buffer_load_dwordx4 v[200:203], v231, %[rs], %[sb] offen sc1\n\t"
        "buffer_load_dwordx4 v[204:207], v232, %[rs], %[sb] offen sc1\n\t"
        "s_sub_u32 %[cnt], %[cnt], 1\n\t"
        "s_cmp_eq_u32 %[cnt], 0\n\t"
        "v_exp_f32 v80, v80\n\t"
        "v_exp_f32 v81, v81\n\t"
        "v_exp_f32 v82, v82\n\t"
        "v_exp_f32 v83, v83\n\t"
        "v_mfma_f32_32x32x16_bf16 v[64:79], v[212:215], v[132:135], v[64:79]\n\t"
        "v_exp_f32 v84, v84\n\t"
        "v_exp_f32 v85, v85\n\t"
        "v_exp_f32 v86, v86\n\t"
        "v_exp_f32 v87, v87\n\t"
        "v_cvt_pk_bf16_f32 v152, v80, v81\n\t"
        "v_cvt_pk_bf16_f32 v153, v82, v83\n\t"
        "v_mfma_f32_32x32x16_bf16 v[0:15], v[216:219], v[144:147], v[0:15]\n\t"
        "v_exp_f32 v88, v88\n\t"
        "v_exp_f32 v89, v89\n\t"
        "v_exp_f32 v90, v90\n\t"
        "v_exp_f32 v91, v91\n\t"
        "v_cvt_pk_bf16_f32 v154, v84, v85\n\t"
        "v_cvt_pk_bf16_f32 v155, v86, v87\n\t"
        "v_mfma_f32_32x32x16_bf16 v[0:15], v[220:223], v[148:151], v[0:15]\n\t"
        "v_exp_f32 v92, v92\n\t"
        "v_exp_f32 v93, v93\n\t"
        "v_exp_f32 v94, v94\n\t"
        "v_exp_f32 v95, v95\n\t"
        "v_cvt_pk_bf16_f32 v156, v88, v89\n\t"
        "v_cvt_pk_bf16_f32 v157, v90, v91\n\t"
        "v_cvt_pk_bf16_f32 v158, v92, v93\n\t"
        "v_cvt_pk_bf16_f32 v159, v94, v95\n\t"
        "s_cbranch_scc1 TAIL%=\n\t"
        "v_mfma_f32_32x32x16_bf16 v[80:95], v[208:211], v[136:139], v[96:111]\n\t"
        "v_exp_f32 v64, v64\n\t"
        "v_exp_f32 v65, v65\n\t"
        "v_exp_f32 v66, v66\n\t"
        "v_exp_f32 v67, v67\n\t"
        "v_mfma_f32_32x32x16_bf16 v[80:95], v[212:215], v[140:143], v[80:95]\n\t"
        "v_exp_f32 v68, v68\n\t"
        "v_exp_f32 v69, v69\n\t"
        "v_exp_f32 v70, v70\n\t"
        "v_exp_f32 v71, v71\n\t"
        "v_cvt_pk_bf16_f32 v144, v64, v65\n\t"
        "v_cvt_pk_bf16_f32 v145, v66, v67\n\t"
        "v_mfma_f32_32x32x16_bf16 v[16:31], v[216:219], v[152:155], v[16:31]\n\t"
        "v_exp_f32 v72, v72\n\t"
        "v_exp_f32 v73, v73\n\t"
        "v_exp_f32 v74, v74\n\t"
        "v_exp_f32 v75, v75\n\t"
        "v_cvt_pk_bf16_f32 v146, v68, v69\n\t"
        "v_cvt_pk_bf16_f32 v147, v70, v71\n\t"
        "v_mfma_f32_32x32x16_bf16 v[16:31], v[220:223], v[156:159], v[16:31]\n\t"
        "v_exp_f32 v76, v76\n\t"
        "v_exp_f32 v77, v77\n\t"
        "v_exp_f32 v78, v78\n\t"
        "v_exp_f32 v79, v79\n\t"
        "v_cvt_pk_bf16_f32 v148, v72, v73\n\t"
        "v_cvt_pk_bf16_f32 v149, v74, v75\n\t"
        "v_cvt_pk_bf16_f32 v150, v76, v77\n\t"
        "v_cvt_pk_bf16_f32 v151, v78, v79\n\t"
        "s_waitcnt vmcnt(8)\n\t"
        "v_mfma_f32_32x32x16_bf16 v[64:79], v[160:163], v[112:115], v[96:111]\n\t"
        "v_exp_f32 v80, v80\n\t"
        "v_exp_f32 v81, v81\n\t"
        "v_exp_f32 v82, v82\n\t"
        "v_exp_f32 v83, v83\n\t"
        "v_mfma_f32_32x32x16_bf16 v[64:79], v[164:167], v[116:119], v[64:79]\n\t"
        "v_exp_f32 v84, v84\n\t"
        "v_exp_f32 v85, v85\n\t"
        "v_exp_f32 v86, v86\n\t"
        "v_exp_f32 v87, v87\n\t"
        "v_cvt_pk_bf16_f32 v152, v80, v81\n\t"
        "v_cvt_pk_bf16_f32 v153, v82, v83\n\t"
        "v_mfma_f32_32x32x16_bf16 v[32:47], v[216:219], v[144:147], v[32:47]\n\t"
        "v_exp_f32 v88, v88\n\t"
        "v_exp_f32 v89, v89\n\t"
        "v_exp_f32 v90, v90\n\t"
        "v_exp_f32 v91, v91\n\t"
        "v_cvt_pk_bf16_f32 v154, v84, v85\n\t"
        "v_cvt_pk_bf16_f32 v155, v86, v87\n\t"
        "v_mfma_f32_32x32x16_bf16 v[32:47], v[220:223], v[148:151], v[32:47]\n\t"
        "v_exp_f32 v92, v92\n\t"
        "v_exp_f32 v93, v93\n\t"
        "v_exp_f32 v94, v94\n\t"
        "v_exp_f32 v95, v95\n\t"
        "v_cvt_pk_bf16_f32 v156, v88, v89\n\t"
        "v_cvt_pk_bf16_f32 v157, v90, v91\n\t"
        "v_cvt_pk_bf16_f32 v158, v92, v93\n\t"
        "v_cvt_pk_bf16_f32 v159, v94, v95\n\t"
        "s_branch LOOP%=\n\t"
        "TAIL%=:\n\t"
        "v_mfma_f32_32x32x16_bf16 v[80:95], v[208:211], v[136:139], v[96:111]\n\t"
        "v_exp_f32 v64, v64\n\t"
        "v_exp_f32 v65, v65\n\t"
        "v_exp_f32 v66, v66\n\t"
        "v_exp_f32 v67, v67\n\t"
        "v_mfma_f32_32x32x16_bf16 v[80:95], v[212:215], v[140:143], v[80:95]\n\t"
        "ds_write_b128 v240, v[0:3] offset:0\n\t"
        "ds_write_b128 v240, v[4:7] offset:1024\n\t"
        "ds_write_b128 v240, v[8:11] offset:2048\n\t"
        "ds_write_b128 v240, v[12:15] offset:3072\n\t"
        "v_exp_f32 v68, v68\n\t"
        "v_exp_f32 v69, v69\n\t"
        "v_exp_f32 v70, v70\n\t"
        "v_exp_f32 v71, v71\n\t"
        "v_cvt_pk_bf16_f32 v144, v64, v65\n\t"
        "v_cvt_pk_bf16_f32 v145, v66, v67\n\t"
        "v_mfma_f32_32x32x16_bf16 v[16:31], v[216:219], v[152:155], v[16:31]\n\t"
        "v_exp_f32 v72, v72\n\t"
        "v_exp_f32 v73, v73\n\t"
        "v_exp_f32 v74, v74\n\t"
        "v_exp_f32 v75, v75\n\t"
        "v_cvt_pk_bf16_f32 v146, v68, v69\n\t"
        "v_cvt_pk_bf16_f32 v147, v70, v71\n\t"
        "v_mfma_f32_32x32x16_bf16 v[16:31], v[220:223], v[156:159], v[16:31]\n\t"
        "v_exp_f32 v76, v76\n\t"
        "v_exp_f32 v77, v77\n\t"
        "v_exp_f32 v78, v78\n\t"
        "v_exp_f32 v79, v79\n\t"
        "v_cvt_pk_bf16_f32 v148, v72, v73\n\t"
        "v_cvt_pk_bf16_f32 v149, v74, v75\n\t"
        "v_cvt_pk_bf16_f32 v150, v76, v77\n\t"
        "v_cvt_pk_bf16_f32 v151, v78, v79\n\t"
        "v_exp_f32 v80, v80\n\t"
        "v_exp_f32 v81, v81\n\t"
        "v_exp_f32 v82, v82\n\t"
        "v_exp_f32 v83, v83\n\t"
        "v_exp_f32 v84, v84\n\t"
        "v_exp_f32 v85, v85\n\t"
        "v_exp_f32 v86, v86\n\t"
        "v_exp_f32 v87, v87\n\t"
        "v_cvt_pk_bf16_f32 v152, v80, v81\n\t"
        "v_cvt_pk_bf16_f32 v153, v82, v83\n\t"
        "v_mfma_f32_32x32x16_bf16 v[32:47], v[216:219], v[144:147], v[32:47]\n\t"
        "ds_write_b128 v240, v[16:19] offset:4096\n\t"
        "ds_write_b128 v240, v[20:23] offset:5120\n\t"
        "ds_write_b128 v240, v[24:27] offset:6144\n\t"
        "ds_write_b128 v240, v[28:31] offset:7168\n\t"
        "v_exp_f32 v88, v88\n\t"
        "v_exp_f32 v89, v89\n\t"
        "v_exp_f32 v90, v90\n\t"
        "v_exp_f32 v91, v91\n\t"
        "v_cvt_pk_bf16_f32 v154, v84, v85\n\t"
        "v_cvt_pk_bf16_f32 v155, v86, v87\n\t"
        "v_mfma_f32_32x32x16_bf16 v[32:47], v[220:223], v[148:151], v[32:47]\n\t"
        "v_exp_f32 v92, v92\n\t"
        "v_exp_f32 v93, v93\n\t"
        "v_exp_f32 v94, v94\n\t"
        "v_exp_f32 v95, v95\n\t"
        "v_cvt_pk_bf16_f32 v156, v88, v89\n\t"
        "v_cvt_pk_bf16_f32 v157, v90, v91\n\t"
        "v_cvt_pk_bf16_f32 v158, v92, v93\n\t"
        "v_cvt_pk_bf16_f32 v159, v94, v95\n\t"
        "v_mfma_f32_32x32x16_bf16 v[48:63], v[216:219], v[152:155], v[48:63]\n\t"
        "v_mfma_f32_32x32x16_bf16 v[48:63], v[220:223], v[156:159], v[48:63]\n\t"
        "s_nop 3\n\t"
        "ds_write_b128 v240, v[32:35] offset:8192\n\t"
        "ds_write_b128 v240, v[36:39] offset:9216\n\t"
        "ds_write_b128 v240, v[40:43] offset:10240\n\t"
        "ds_write_b128 v240, v[44:47] offset:11264\n\t"
        "s_waitcnt vmcnt(0) lgkmcnt(0)\n\t"
        "s_barrier\n\t"
        "s_nop 7\n\t"
        : "={v[48:63]}"(acc), [sb] "+s"(sb), [cnt] "+s"(cnt), "+{v236}"(q2a), "+{v237}"(kba), "+{v238}"(ordn), "={v[136:139]}"(qo0), "={v[140:143]}"(qo1)
        : "{v224}"(vok + 0 * TS), "{v225}"(vov + 0 * TS), "{v226}"(vov + 2 * VROW + 0 * TS), "{v227}"(vok + 1 * TS), "{v228}"(vov + 1 * TS), "{v229}"(vov + 2 * VROW + 1 * TS), "{v230}"(vok + 2 * TS), "{v231}"(vov + 2 * TS), "{v232}"(vov + 2 * VROW + 2 * TS), "{v233}"(vok + 3 * TS), "{v234}"(vov + 3 * TS), "{v235}"(vov + 2 * VROW + 3 * TS),
          "{v240}"(puta), "{v241}"(qxa[0]), "{v242}"(qxa[1]), "{v243}"(qxa[2]), "{v244}"(qxa[3]), [rs] "s"(rsKVr)
        : "v0", "v1", "v2", "v3", "v4", "v5", "v6", "v7", "v8", "v9", "v10", "v11", "v12", "v13", "v14", "v15", "v16", "v17", "v18", "v19", "v20", "v21", "v22", "v23", "v24", "v25", "v26", "v27", "v28", "v29", "v30", "v31", "v32", "v33", "v34", "v35", "v36", "v37", "v38", "v39", "v40", "v41", "v42", "v43", "v44", "v45", "v46", "v47", "v64", "v65", "v66", "v67", "v68", "v69", "v70", "v71", "v72", "v73", "v74", "v75", "v76", "v77", "v78", "v79", "v80", "v81", "v82", "v83", "v84", "v85", "v86", "v87", "v88", "v89", "v90", "v91", "v92", "v93", "v94", "v95", "v96", "v97", "v98", "v99", "v100", "v101", "v102", "v103", "v104", "v105", "v106", "v107", "v108", "v109", "v110", "v111", "v112", "v113", "v114", "v115", "v116", "v117", "v118", "v119", "v120", "v121", "v122", "v123", "v124", "v125", "v126", "v127", "v128", "v129", "v130", "v131", "v132", "v133", "v134", "v135", "v144", "v145", "v146", "v147", "v148", "v149", "v150", "v151", "v152", "v153", "v154", "v155", "v156", "v157", "v158", "v159", "v160", "v161", "v162", "v163", "v164", "v165", "v166", "v167", "v168", "v169", "v170", "v171", "v172", "v173", "v174", "v175", "v176", "v177", "v178", "v179", "v180", "v181", "v182", "v183", "v184", "v185", "v186", "v187", "v188", "v189", "v190", "v191", "v192", "v193", "v194", "v195", "v196", "v197", "v198", "v199", "v200", "v201", "v202", "v203", "v204", "v205", "v206", "v207", "v208", "v209", "v210", "v211", "v212", "v213", "v214", "v215", "v216", "v217", "v218", "v219", "v220", "v221", "v222", "v223", "v239", "scc", "memory");
    }
    STAMP(7);
#pragma unroll
    for (int s = 0; s < 3; ++s) {
        LAS const char* sp = lds + L_CB + (((w - 1 - s) & 3) * 3 + s) * 4096 + lane * 16;
#pragma unroll
        for (int g = 0; g < 4; ++g) {
            const f32x4 v = *(LAS const f32x4*)(sp + g * 1024);
            acc[4 * g] += v[0]; acc[4 * g + 1] += v[1]; acc[4 * g + 2] += v[2]; acc[4 * g + 3] += v[3];
        }
    }
    float l = half_hi(acc[12]);
    if (__builtin_expect(__any(!(l >= L_MIN && l <= L_MAX)), 0)) {
        const f32x16 zero = {};
        float m = -__builtin_inff();
#pragma unroll 1
        for (int t = 0; t < 64; ++t) {
            f32x16 s = MFMA(asbf(ld16_wt(rsKVr, vok, t * TS)), asbf(qo0), zero);
            s = MFMA(asbf(ld16_wt(rsKVr, vok + 1024, t * TS)), asbf(qo1), s);
#pragma unroll
            for (int i = 0; i < 16; ++i) m = __builtin_fmaxf(m, s[i]);
        }
        m = __builtin_fmaxf(half_lo(m), half_hi(m));
        acc = zero;
#pragma unroll 1
        for (int t = 0; t < 64; ++t) {
            f32x16 s = MFMA(asbf(ld16_wt(rsKVr, vok, t * TS)), asbf(qo0), zero);
            s = MFMA(asbf(ld16_wt(rsKVr, vok + 1024, t * TS)), asbf(qo1), s);
#pragma unroll
            for (int i = 0; i < 16; ++i) s[i] = __builtin_amdgcn_exp2f(s[i] - m);
            acc = MFMA(asbf(ld16_wt(rsKVr, vov, t * TS)), asbf(pack8(s, 0)), acc);
            acc = MFMA(asbf(ld16_wt(rsKVr, vov + 2 * VROW, t * TS)), asbf(pack8(s, 1)), acc);
        }
        l = half_hi(acc[12]);
    }
    {
        const float rl = __builtin_amdgcn_rcpf(l);
#pragma unroll
        for (int i = 0; i < 16; ++i) acc[i] = (i >= 12 && h) ? 0.f : acc[i] * rl;
        const unsigned so = (kt * NH + hd) * 2048;
        u32x4 p0 = pack8(acc, 0), p1 = pack8(acc, 1);
#pragma unroll
        for (int i = 0; i < 4; ++i) { p0[i] = min(p0[i], POISON - 1); p1[i] = min(p1[i], POISON - 1); }
        st16_wt(p0, rsOI, lane * 16, so);
        st16_wt(p1, rsOI, lane * 16 + 1024, so);
    }
    STAMP(8);
    BAR();

    bf16x8 wf[32];
    {
        LAS const char* wb = lds + L_WO + (4 * h + q4) * 256 + ((w ^ q4) * 64) + g16 * 32 + p4 * 8;
#pragma unroll
        for (int k = 0; k < 32; ++k) {
            const int ro = ((k >> 1) * DH + (k & 1) * 16) * 256;
            wf[k] = cat(vtr(wb + ro), vtr(wb + ro + 2048 - (k == 31 ? h * 1024 : 0)));
        }
    }
    STAMP(10);
    BAR();
    STAMP(11);
    if (cg < 3 || w < 2) {
        f32x16 c = {};
        bf16x8 of[3][4];
#define RDO(i) do { _Pragma("unroll") for (int j = 0; j < 4; ++j) of[(i) % 3][j] = *(LAS const bf16x8*)(lds + L_CB + ((i) * 4 + j) * 1024 + lane * 16); } while (0)
        RDO(0); RDO(1); SBAR();
#pragma unroll
        for (int i = 0; i < 8; ++i) {
            if (i + 2 < 8) RDO(i + 2);
            SBAR();
#pragma unroll
            for (int j = 0; j < 4; ++j) c = MFMA(wf[i * 4 + j], of[i % 3][j], c);
            SBAR();
        }
#undef RDO
        STAMP(12);
        char* orow = out + (size_t)(q32 * 32 + r) * (DM * 2) + cg * 256 + w * 64 + h * 16;
#pragma unroll
        for (int g = 0; g < 4; g += 2) {
            const unsigned x0 = cvtpk(c[4 * g], c[4 * g + 1]), x1 = cvtpk(c[4 * g + 2], c[4 * g + 3]);
            const unsigned y0 = cvtpk(c[4 * g + 4], c[4 * g + 5]), y1 = cvtpk(c[4 * g + 6], c[4 * g + 7]);
            auto s0 = __builtin_amdgcn_permlane32_swap(x0, y0, false, false);
            auto s1 = __builtin_amdgcn_permlane32_swap(x1, y1, false, false);
            *(u32x4*)(orow + g * 16) = (u32x4){s0[0], s1[0], s0[1], s1[1]};
        }
    }
    STAMP(13);
    STAMP_FLUSH();
}

extern "C" void kernel_launch(void* const* d_in, const int* in_sizes, int n_in, void* d_out, int out_size, void* d_ws, size_t ws_size,
                              hipStream_t stream) {
    (void)hipFuncSetAttribute((const void*)mha_kernel, hipFuncAttributeMaxDynamicSharedMemorySize, LDS_BYTES);
    mha_kernel<<<256, 512, LDS_BYTES, stream>>>((const char*)d_in[0], (const char*)d_in[1], (const char*)d_in[2], (char*)d_out, (char*)d_ws);
}
```

```cpp
#include <hip/hip_runtime.h>
#include <hip/hip_bf16.h>
#include <stdint.h>

typedef short bf16x8 __attribute__((ext_vector_type(8)));
typedef short s16x4 __attribute__((ext_vector_type(4)));
typedef float f32x16 __attribute__((ext_vector_type(16)));
typedef float f32x4 __attribute__((ext_vector_type(4)));
typedef float f32x2 __attribute__((ext_vector_type(2)));
typedef unsigned u32x4 __attribute__((ext_vector_type(4)));
typedef __bf16 bf16x2_t __attribute__((ext_vector_type(2)));
typedef short v4i16_t __attribute__((ext_vector_type(4)));
typedef unsigned long long u64;
#define LAS __attribute__((address_space(3)))
#define GAS __attribute__((address_space(1)))
#define DI __device__ __forceinline__
#define MFMA(a, b, c) __builtin_amdgcn_mfma_f32_32x32x16_bf16((a), (b), (c), 0, 0, 0)
#define SBAR() __builtin_amdgcn_sched_barrier(0)
#define RLX_AGENT __ATOMIC_RELAXED, __HIP_MEMORY_SCOPE_AGENT
#define BAR() asm volatile("s_waitcnt lgkmcnt(0)\n\ts_barrier" ::: "memory")

#define MAP_HD(b) (((b) & 7) * 2 + (((b) >> 3) & 1))
#define MAP_QT(b) ((b) >> 4)
constexpr int SEQ = 2048, DM = 448, NH = 16, DH = 28, N3 = 1344;
constexpr float QSCALE = 0.18898223650461362f * 1.4426950408889634f;
constexpr float L_MIN = 8.673617379884035e-19f, L_MAX = 1.152921504606846976e18f;
constexpr unsigned VROW = 456 * 16, TS = 4 * VROW;
constexpr unsigned WS_K = 0, WS_V = 2u << 20, WS_O = 4u << 20, WS_F = 6u << 20;
constexpr unsigned POISON = 0xffffffffu;
constexpr unsigned F_G = 0, F_R = 4096, F_P = 8192, F_KV = 131072;
constexpr unsigned L_WQ = 0, L_RING = 28672, STG = 24576;
constexpr unsigned L_QX = 131072, L_Q2 = L_QX + 8192, L_GB = 152064;
constexpr unsigned L_WO = 0, L_CB = 114688;
constexpr unsigned LDS_BYTES = 163840;


DI unsigned rfl(unsigned x) { return __builtin_amdgcn_readfirstlane(x); }
DI unsigned cvtpk(float lo, float hi) { f32x2 v = {lo, hi}; bf16x2_t b = __builtin_convertvector(v, bf16x2_t); return __builtin_bit_cast(unsigned, b); }
DI u32x4 pack8(const f32x16& x, int s) {
    return (u32x4){cvtpk(x[8 * s], x[8 * s + 1]), cvtpk(x[8 * s + 2], x[8 * s + 3]), cvtpk(x[8 * s + 4], x[8 * s + 5]), cvtpk(x[8 * s + 6], x[8 * s + 7])};
}
DI bf16x8 asbf(u32x4 x) { return __builtin_bit_cast(bf16x8, x); }
DI s16x4 vtr(LAS const char* p) { return __builtin_bit_cast(s16x4, __builtin_amdgcn_ds_read_tr16_b64_v4i16((LAS v4i16_t*)p)); }
DI bf16x8 cat(s16x4 lo, s16x4 hi) { return __builtin_shufflevector(lo, hi, 0, 1, 2, 3, 4, 5, 6, 7); }
DI float half_lo(float x) { auto r = __builtin_amdgcn_permlane32_swap(__float_as_uint(x), __float_as_uint(x), false, false); return __uint_as_float(r[0]); }
DI float half_hi(float x) { auto r = __builtin_amdgcn_permlane32_swap(__float_as_uint(x), __float_as_uint(x), false, false); return __uint_as_float(r[1]); }
DI void dma16(unsigned lds_dst, unsigned voff, __amdgpu_buffer_rsrc_t rs, unsigned soff) {
    asm volatile("s_mov_b32 m0, %0\n\ts_nop 0\n\tbuffer_load_dwordx4 %1, %2, %3 offen lds" ::"s"(lds_dst), "v"(voff), "s"(rs), "s"(soff) : "m0", "memory");
}
DI void st16_wt(u32x4 v, __amdgpu_buffer_rsrc_t rs, unsigned voff, unsigned soff) { __builtin_amdgcn_raw_buffer_store_b128(v, rs, voff, soff, 16); }
DI u32x4 ld16_wt(__amdgpu_buffer_rsrc_t rs, unsigned voff, unsigned soff) { return __builtin_amdgcn_raw_buffer_load_b128(rs, voff, soff, 16); }
DI __amdgpu_buffer_rsrc_t mkrs(const void* p, unsigned bytes) { return __builtin_amdgcn_make_buffer_rsrc((void*)p, (short)0, (int)bytes, 0x00020000); }
DI unsigned poll_tags(const GAS u64* g, unsigned tag) {
    u64 v = __hip_atomic_load(g, RLX_AGENT);
#pragma nounroll
    for (unsigned spins = 0; spins < (1u << 16); ++spins) {
        if (__all((unsigned)v == tag)) break;
        if ((unsigned)v != tag) v = __hip_atomic_load(g, RLX_AGENT);
    }
    __builtin_amdgcn_fence(__ATOMIC_ACQUIRE, "wavefront");
    return (unsigned)(v >> 32);
}
DI unsigned touch(const GAS char* p) { unsigned t; asm volatile("global_load_dword %0, %1, off" : "=v"(t) : "v"(p) : "memory"); return t; }

template <bool ISK>
DI void kv_gemm(LAS char* lds, unsigned w, int lane, bf16x8 (&xf)[7][4], f32x16& a0, f32x16& a1) {
    const int r = lane & 31, h = lane >> 5, i16 = lane & 15, q4 = i16 >> 2, p4 = i16 & 3, g16 = (lane >> 4) & 1;
    const int pc = ISK ? (((p4 & 1) << 1) | (p4 >> 1)) : p4;
    LAS const char* xb = lds + L_RING + w * 4096 + r * 128;
    LAS const char* wb0 = lds + L_RING + 16384 + (8 * h + q4) * 128 + ((q4 >> 1) * 64) + g16 * 32 + pc * 8;
    LAS const char* wb1 = lds + L_RING + 16384 + (8 * h + q4) * 128 + (((q4 >> 1) ^ 1) * 64) + g16 * 32 + pc * 8;
    const int xs = (r >> 1) & 7;
    bf16x8 wf[2][4][2];
#define RD(i) do { const int so = ((i) % 5) * STG; _Pragma("unroll") for (int ks = 0; ks < 4; ++ks) {                          \
        xf[i][ks] = *(LAS const bf16x8*)(xb + so + (((2 * ks + h) ^ xs) * 16));                                                \
        wf[(i) & 1][ks][0] = cat(vtr(wb0 + so + ks * 2048), vtr(wb0 + so + ks * 2048 + 512));                                  \
        wf[(i) & 1][ks][1] = cat(vtr(wb1 + so + ks * 2048), vtr(wb1 + so + ks * 2048 + 512)); } } while (0)
#define MM(i) do { _Pragma("unroll") for (int ks = 0; ks < 4; ++ks) {                                                          \
        if (ISK) { a0 = MFMA(wf[(i) & 1][ks][0], xf[i][ks], a0); a1 = MFMA(wf[(i) & 1][ks][1], xf[i][ks], a1); }               \
        else     { a0 = MFMA(xf[i][ks], wf[(i) & 1][ks][0], a0); a1 = MFMA(xf[i][ks], wf[(i) & 1][ks][1], a1); } } } while (0)
    BAR(); RD(0);
#pragma unroll
    for (int i = 0; i < 5; ++i) { BAR(); RD(i + 1); SBAR(); MM(i); SBAR(); }
    MM(5); SBAR();
    BAR(); RD(6); SBAR();
#pragma unroll
    for (int ks = 0; ks < 4; ++ks) a0 = ISK ? MFMA(wf[0][ks][0], xf[6][ks], a0) : MFMA(xf[6][ks], wf[0][ks][0], a0);
    SBAR();
#pragma unroll
    for (int ks = 0; ks < 4; ++ks) a1 = ISK ? MFMA(wf[0][ks][1], xf[6][ks], a1) : MFMA(xf[6][ks], wf[0][ks][1], a1);
    SBAR();
#undef RD
#undef MM
}

DI void stage_dma(unsigned mw, unsigned mx, unsigned sw, unsigned sx, unsigned wvo, unsigned xvo0, unsigned xvo1, __amdgpu_buffer_rsrc_t rsW,
                  __amdgpu_buffer_rsrc_t rsX) {
    unsigned t;
    asm volatile("s_mov_b32 m0, %1\n\ts_add_u32 %0, %3, 21504\n\tbuffer_load_dwordx4 %5, %8, %3 offen lds\n\t"
                 "s_add_u32 m0, m0, 1024\n\ts_nop 0\n\tbuffer_load_dwordx4 %5, %8, %0 offen lds\n\t"
                 "s_mov_b32 m0, %2\n\ts_add_u32 %0, %4, 7168\n\tbuffer_load_dwordx4 %6, %9, %4 offen lds\n\t"
                 "s_add_u32 m0, m0, 1024\n\ts_nop 0\n\tbuffer_load_dwordx4 %7, %9, %0 offen lds\n\t"
                 "s_add_u32 m0, m0, 1024\n\ts_add_u32 %0, %4, 14336\n\tbuffer_load_dwordx4 %6, %9, %0 offen lds\n\t"
                 "s_add_u32 m0, m0, 1024\n\ts_add_u32 %0, %4, 21504\n\tbuffer_load_dwordx4 %7, %9, %0 offen lds"
                 : "=&s"(t) : "s"(mw), "s"(mx), "s"(sw), "s"(sx), "v"(wvo), "v"(xvo0), "v"(xvo1), "s"(rsW), "s"(rsX) : "m0", "scc", "memory");
}

__global__ __launch_bounds__(512, 2) void mha_kernel(const char* __restrict__ x, const char* __restrict__ wqkv, const char* __restrict__ wo,
                                                     char* __restrict__ out, char* __restrict__ ws) {
    extern __shared__ __attribute__((aligned(16))) char lds_[];
    LAS char* lds = (LAS char*)lds_;
    const unsigned wv = rfl(threadIdx.x >> 6), w = wv & 3;
    const unsigned lds0 = (unsigned)(uintptr_t)lds_;
    GAS char* wsg = (GAS char*)ws;
#define DECODE()                                                                                                              \
    unsigned b = blockIdx.x, lane = threadIdx.x & 63;                                                                        \
    asm volatile("" : "+s"(b)); asm volatile("" : "+v"(lane));                                                                \
    const unsigned hd = MAP_HD(b), qt = MAP_QT(b);                                                                            \
    const unsigned ch = hd < 14 ? hd : hd - 2;         \
    const unsigned rot = (hd + qt) % 7;
#define DECODE2()                                                                                                             \
    const bool isK = ch < 7;                                                                                                  \
    const unsigned g0 = (7 * hd) >> 1;                                                  \
    const unsigned cg = (b >> 3) & 3, q32 = (b & 7) * 8 + (b >> 5);     \
    unsigned kbi[7];                                                                                                          \
    _Pragma("unroll") for (int i = 0; i < 7; ++i) { const unsigned k = rot + i; kbi[i] = k >= 7 ? k - 7 : k; }               \
    const __amdgpu_buffer_rsrc_t rsOI = mkrs(ws + WS_O, 2u << 20);

    if (__builtin_expect(wv >= 4, 1)) {
        DECODE();
        const unsigned xrow = qt * 128 + w * 32 + (lane >> 3);
        const unsigned xvo0 = xrow * (DM * 2) + (((lane & 7) ^ (lane >> 4)) * 16), xvo1 = xvo0 ^ 64;
        const unsigned wvo = (16 * w + (lane >> 3)) * (N3 * 2) + 896 + ch * 128 + (((lane & 7) ^ (((lane >> 4) & 1) << 2)) * 16);
        const __amdgpu_buffer_rsrc_t rsW = mkrs(wqkv, DM * N3 * 2), rsX = mkrs(x, SEQ * DM * 2);
        stage_dma(lds0 + L_RING + 16384 + w * 2048, lds0 + L_RING + w * 4096, rot * (64 * N3 * 2), rot * 128, wvo, xvo0, xvo1, rsW, rsX);
        DECODE2();
        const __amdgpu_buffer_rsrc_t rsF = mkrs(ws + WS_F, 4096);
#define STAGE(i) stage_dma(lds0 + L_RING + ((i) % 5) * STG + 16384 + w * 2048, lds0 + L_RING + ((i) % 5) * STG + w * 4096, kbi[i] * (64 * N3 * 2), kbi[i] * 128, \
                           wvo, xvo0, xvo1, rsW, rsX)
#define VBAR(NV) asm volatile("s_waitcnt vmcnt(" #NV ")\n\ts_barrier" ::: "memory")
        STAGE(1); STAGE(2);
        VBAR(12); STAGE(3);
        VBAR(12); STAGE(4);
        VBAR(12); STAGE(5);
        VBAR(12); STAGE(6);
        VBAR(12);
        unsigned gen;
        { const unsigned vo = F_G; asm volatile("buffer_load_dword %0, %1, %2, 0 offen sc1" : "=v"(gen) : "v"(vo), "s"(rsF) : "memory"); }
        {
            const unsigned qvo = (16 * w + (lane >> 2)) * (N3 * 2) + g0 * 16 + (lane & 3) * 16;
#pragma unroll
            for (int j = 0; j < 7; ++j) dma16(lds0 + L_WQ + (16 * w + 64 * kbi[j]) * 64, qvo, rsW, kbi[j] * 64 * (N3 * 2));
        }
        VBAR(14); VBAR(8);
        asm volatile("s_waitcnt vmcnt(7)" : "+v"(gen)::"memory");
        unsigned tag = rfl(gen);
        { const unsigned t = (tag ^ (tag >> 15)) * 0x2c1b3c6du + 0x9e3779b9u; tag = (t == tag) ? t + 1 : t; }
        const unsigned hh = lane & 15, wh = (lane >> 4) & 1;
        const unsigned c = isK ? (((7 * hh >> 1) + 3 * wh) >> 3) : 7 + ((28 * hh + 27 * wh) >> 6);
        GAS u64* fp = (GAS u64*)(wsg + WS_F + (lane < 32 ? F_KV + ((hh * 4 + (isK ? 0 : 2) + wh) * 16 + qt) * 128 : F_R + (qt * 16 + hd) * 8));
        const bool fon = w == 0 && ((lane < 32 && c == hd) || lane == 32);
        asm volatile("s_waitcnt lgkmcnt(0)\n\ts_barrier" : "+v"(fp)::"memory");
        if (w == 0) {
            if (fon) __hip_atomic_store(fp, ((u64)*(LAS const unsigned*)(lds + L_GB) << 32) | tag, RLX_AGENT);
            asm volatile("s_waitcnt vmcnt(1)" ::: "memory");
        } else {
            asm volatile("s_waitcnt vmcnt(0)" ::: "memory");
        }
        asm volatile("s_waitcnt lgkmcnt(0)\n\ts_barrier" ::: "memory");
        LAS unsigned* ord = (LAS unsigned*)(lds + L_CB + w * 12288 + (w == 2 ? 2048 : 0));
        if (lane < 5) ord[lane] = lane == 4 ? 0x40000000u : 0xffffffffu;
        {
            const unsigned tt = w + 4 * ((((lane >> 2) & 3) + (qt >> 2)) & 3);
            const GAS u64* fl = (const GAS u64*)(wsg + WS_F + F_KV + hd * 8192 + ((lane & 3) * 16 + tt) * 128);
            u64 v = __hip_atomic_load(fl, RLX_AGENT);
            unsigned done = 0;
#pragma nounroll
            for (unsigned spins = 0; done < 4; ++spins) {
                const unsigned miss = spins < (1u << 16) ? (unsigned)__ballot((unsigned)v != tag) & 0xffffu : 0u;
                const unsigned n = miss ? __builtin_ctz(miss) >> 2 : 4;
                if (n > done) {
                    __builtin_amdgcn_fence(__ATOMIC_ACQUIRE, "wavefront");
                    if (lane < 4 * n && (lane & 3) == 0) ord[lane >> 2] = tt * (4 * TS);
                    if (done == 0) {
                        const unsigned g = (unsigned)(v >> 32);
                        if (w == (qt & 3) && lane == 0) *(LAS unsigned*)(lds + L_GB + 16) = max(g, (unsigned)__builtin_amdgcn_readlane((int)g, 1));
                        BAR();
                        asm volatile("s_barrier" ::: "memory");
                    }
                    done = n;
                }
                if ((unsigned)v != tag) v = __hip_atomic_load(fl, RLX_AGENT);
            }
        }
        st16_wt((u32x4){POISON, POISON, POISON, POISON}, rsOI, lane * 16, ((qt * 4 + w) * NH + hd) * 2048);
        st16_wt((u32x4){POISON, POISON, POISON, POISON}, rsOI, lane * 16 + 1024, ((qt * 4 + w) * NH + hd) * 2048);
        asm volatile("s_waitcnt vmcnt(0)" ::: "memory");
        if (lane == 0) __hip_atomic_store((GAS u64*)(wsg + WS_F + F_P + ((qt * 4 + w) * 16 + hd) * 8), (u64)tag, RLX_AGENT);
        if (w == 1 && b == 0) {
            const GAS u64* fr = (const GAS u64*)(wsg + WS_F + F_R) + lane;
#pragma nounroll
            for (unsigned spins = 0; spins < (1u << 16); ++spins) {
                const u64 v0 = __hip_atomic_load(fr, RLX_AGENT), v1 = __hip_atomic_load(fr + 64, RLX_AGENT);
                const u64 v2 = __hip_atomic_load(fr + 128, RLX_AGENT), v3 = __hip_atomic_load(fr + 192, RLX_AGENT);
                if (__all((unsigned)v0 == tag && (unsigned)v1 == tag && (unsigned)v2 == tag && (unsigned)v3 == tag)) break;
                __builtin_amdgcn_s_sleep(4);
            }
            if (lane == 0) __hip_atomic_store((GAS unsigned*)(wsg + WS_F + F_G), tag, RLX_AGENT);
        }
        {
            const __amdgpu_buffer_rsrc_t rsO = mkrs(wo, DM * DM * 2);
            const unsigned vow = (4 * w + (lane >> 4)) * (DM * 2) + cg * 256 + ((((lane >> 2) & 3) ^ (lane >> 4)) * 64) + (lane & 3) * 16;
#pragma unroll 1
            for (unsigned j = 0; j < 28; ++j) { dma16(lds0 + L_WO + (4 * j + w) * 1024, vow, rsO, j * 14336); __builtin_amdgcn_s_sleep(1); }
        }
        (void)poll_tags((const GAS u64*)(wsg + WS_F + F_P + q32 * 128) + w * 4 + (lane & 3), tag);
        VBAR(0);
        asm volatile("s_barrier" ::: "memory");
        {
            unsigned pend = 15;
#pragma nounroll
            for (unsigned spins = 0; pend && spins < (1u << 16); ++spins) {
                u32x4 t[8];
                asm volatile("" ::: "memory");
#pragma unroll
                for (int j = 0; j < 4; ++j)
                    if (pend & (1u << j)) {
                        t[2 * j] = ld16_wt(rsOI, lane * 16 + j * 2048, q32 * (NH * 2048) + w * 8192);
                        t[2 * j + 1] = ld16_wt(rsOI, lane * 16 + j * 2048 + 1024, q32 * (NH * 2048) + w * 8192);
                    }
#pragma unroll
                for (int j = 0; j < 4; ++j)
                    if (pend & (1u << j)) {
                        const u32x4 u = t[2 * j], v = t[2 * j + 1];
                        *(LAS u32x4*)(lds + L_CB + (w * 8 + 2 * j) * 1024 + lane * 16) = u;
                        *(LAS u32x4*)(lds + L_CB + (w * 8 + 2 * j + 1) * 1024 + lane * 16) = v;
                        const unsigned m = max(max(max(u[0], u[1]), max(u[2], u[3])), max(max(v[0], v[1]), max(v[2], v[3])));
                        if (__all(m != POISON)) pend &= ~(1u << j);
                    }
            }
        }
        BAR();
        return;
    }

    DECODE(); DECODE2();
    const int r = lane & 31, h = lane >> 5, i16 = lane & 15, q4 = i16 >> 2, p4 = i16 & 3, g16 = (lane >> 4) & 1;
    f32x16 a0 = {}, a1 = {};
    bf16x8 xf[7][4];
    const unsigned kt = qt * 4 + w;
    unsigned tch1;
    {
        unsigned long long pc0;
        unsigned len;
        asm volatile("s_getpc_b64 %0\nMHA_PF%=:\n\ts_mov_b32 %1, MHA_CODE_END - MHA_PF%=" : "=s"(pc0), "=s"(len));
        tch1 = touch((const GAS char*)(pc0 & ~3ull) + min((((b >> 3) & 3) * 4 + w) * 1024 + (lane & 7) * 128, len));
    }
    if (threadIdx.x == 0) *(LAS unsigned*)(lds + L_GB) = 0;
    unsigned vok = WS_K + (g0 + h) * 512 + r * 16, vov = r <= DH ? WS_V + h * VROW + (r < DH ? DH * hd + r : 448) * 16 : 0x70000000u;
    const __amdgpu_buffer_rsrc_t rsKVr = mkrs(ws, 4u << 20);
    unsigned q2a = lds0 + L_Q2 + r * 16, kba = lds0 + L_GB + 16, ordn = lds0 + L_CB + w * 12288 + (w == 2 ? 2048 : 0) + 4, puta = lds0 + L_CB + w * 12288 + lane * 16;
    unsigned qxa[4];
#pragma unroll
    for (int i = 0; i < 4; ++i) qxa[i] = lds0 + L_QX + ((w + 1 + i) & 3) * 2048 + lane * 16;
    LAS const char* qb = lds + L_WQ + (8 * h + q4) * 64 + g16 * 32 + (((p4 & 1) << 1) | (p4 >> 1)) * 8;
    asm volatile("" : "+v"(vok), "+v"(vov), "+v"(q2a), "+v"(kba), "+v"(ordn), "+v"(puta), "+v"(qxa[0]), "+v"(qxa[1]), "+v"(qxa[2]), "+v"(qxa[3]), "+v"(qb));
    const __amdgpu_buffer_rsrc_t rsKV = mkrs(ws, hd < 14 ? 4u << 20 : 0u);
    if (isK) {
        unsigned vo = WS_K + (8 * ch + h) * 512 + r * 16;
        asm volatile("" : "+v"(vo));
        kv_gemm<true>(lds, w, lane, xf, a0, a1);
        st16_wt(pack8(a0, 0), rsKV, vo, kt * TS); st16_wt(pack8(a0, 1), rsKV, vo + 1024, kt * TS);
        st16_wt(pack8(a1, 0), rsKV, vo + 2048, kt * TS); st16_wt(pack8(a1, 1), rsKV, vo + 3072, kt * TS);
        SBAR();
        float s0 = 0.f, s1 = 0.f, s2 = 0.f, s3 = 0.f;
#pragma unroll
        for (int j = 0; j < 8; ++j) { s0 += a0[j] * a0[j]; s1 += a0[8 + j] * a0[8 + j]; s2 += a1[j] * a1[j]; s3 += a1[8 + j] * a1[8 + j]; }
        asm volatile("ds_max_u32 %0, %1" ::"v"(lds0 + L_GB), "v"(max(max(__float_as_uint(s0), __float_as_uint(s1)), max(__float_as_uint(s2), __float_as_uint(s3)))) : "memory");
    } else {
        unsigned v0 = WS_V + h * VROW + (64 * (ch - 7) + r) * 16, v2 = v0 + 2 * VROW;
        if (lane < 32)
            st16_wt((u32x4){0x3f803f80u, 0x3f803f80u, 0x3f803f80u, 0x3f803f80u}, rsKV, WS_V + (lane >> 3) * VROW + (448 + (lane & 7)) * 16, kt * TS);
        asm volatile("" : "+v"(v0), "+v"(v2));
        kv_gemm<false>(lds, w, lane, xf, a0, a1);
        st16_wt(pack8(a0, 0), rsKV, v0, kt * TS); st16_wt(pack8(a0, 1), rsKV, v2, kt * TS);
        st16_wt(pack8(a1, 0), rsKV, v0 + 512, kt * TS); st16_wt(pack8(a1, 1), rsKV, v2 + 512, kt * TS);
        SBAR();
    }
    asm volatile("s_waitcnt vmcnt(0) lgkmcnt(0)" : "+v"(tch1)::"memory");
    asm volatile("s_barrier" ::: "memory");
    asm volatile("s_barrier" ::: "memory");
    f32x16 aq = {};
    {
        bf16x8 wq[3][4];
#define RDQ(i) do { LAS const char* qk = qb + kbi[i] * 4096; _Pragma("unroll") for (int ks = 0; ks < 4; ++ks)                  \
            wq[(i) % 3][ks] = cat(vtr(qk + ks * 1024), vtr(qk + ks * 1024 + 256)); } while (0)
        RDQ(0); RDQ(1); SBAR();
#pragma unroll
        for (int i = 0; i < 7; ++i) {
            if (i + 2 < 7) RDQ(i + 2);
            SBAR();
#pragma unroll
            for (int ks = 0; ks < 4; ++ks) aq = MFMA(wq[i % 3][ks], xf[i][ks], aq);
            SBAR();
        }
#undef RDQ
    }
    {
        const bool zlo = (hd & 1) && !h, zhi = !(hd & 1) && h;
        float qs[4] = {0.f, 0.f, 0.f, 0.f};
#pragma unroll
        for (int i = 0; i < 16; ++i) {
            aq[i] *= QSCALE;
            if (i < 4) aq[i] = zlo ? 0.f : aq[i];
            if (i >= 12) aq[i] = zhi ? 0.f : aq[i];
            qs[i & 3] += aq[i] * aq[i];
        }
        *(LAS u32x4*)(lds + (qxa[3] - lds0)) = pack8(aq, 0);
        *(LAS u32x4*)(lds + (qxa[3] - lds0) + 1024) = pack8(aq, 1);
        float q2 = (qs[0] + qs[1]) + (qs[2] + qs[3]);
        q2 = half_lo(q2) + half_hi(q2);
        *(LAS float*)(lds + (q2a - lds0) + w * 4) = q2;
    }

    f32x16 acc;
    u32x4 qo0, qo1;
    {
        unsigned sb = (w + (qt & 12)) * (4 * TS), cnt = 4;
        asm volatile(
        "s_setprio 2\n\t"
        "v_mov_b64 v[0:1], 0\n\t"
        "v_mov_b64 v[2:3], 0\n\t"
        "v_mov_b64 v[4:5], 0\n\t"
        "v_mov_b64 v[6:7], 0\n\t"
        "v_mov_b64 v[8:9], 0\n\t"
        "v_mov_b64 v[10:11], 0\n\t"
        "v_mov_b64 v[12:13], 0\n\t"
        "v_mov_b64 v[14:15], 0\n\t"
        "v_mov_b64 v[16:17], 0\n\t"
        "v_mov_b64 v[18:19], 0\n\t"
        "v_mov_b64 v[20:21], 0\n\t"
        "v_mov_b64 v[22:23], 0\n\t"
        "v_mov_b64 v[24:25], 0\n\t"
        "v_mov_b64 v[26:27], 0\n\t"
        "v_mov_b64 v[28:29], 0\n\t"
        "v_mov_b64 v[30:31], 0\n\t"
        "v_mov_b64 v[32:33], 0\n\t"
        "v_mov_b64 v[34:35], 0\n\t"
        "v_mov_b64 v[36:37], 0\n\t"
        "v_mov_b64 v[38:39], 0\n\t"
        "v_mov_b64 v[40:41], 0\n\t"
        "v_mov_b64 v[42:43], 0\n\t"
        "v_mov_b64 v[44:45], 0\n\t"
        "v_mov_b64 v[46:47], 0\n\t"
        "v_mov_b64 v[48:49], 0\n\t"
        "v_mov_b64 v[50:51], 0\n\t"
        "v_mov_b64 v[52:53], 0\n\t"
        "v_mov_b64 v[54:55], 0\n\t"
        "v_mov_b64 v[56:57], 0\n\t"
        "v_mov_b64 v[58:59], 0\n\t"
        "v_mov_b64 v[60:61], 0\n\t"
        "v_mov_b64 v[62:63], 0\n\t"
        "v_mov_b64 v[152:153], 0\n\t"
        "v_mov_b64 v[154:155], 0\n\t"
        "v_mov_b64 v[156:157], 0\n\t"
        "v_mov_b64 v[158:159], 0\n\t"
        "v_mov_b64 v[216:217], 0\n\t"
        "v_mov_b64 v[218:219], 0\n\t"
        "v_mov_b64 v[220:221], 0\n\t"
        "v_mov_b64 v[222:223], 0\n\t"
        "s_waitcnt lgkmcnt(0)\n\t"
        "s_barrier\n\t"
        "buffer_load_dwordx4 v[160:163], v224, %[rs], %[sb] offen sc1\n\t"
        "buffer_load_dwordx4 v[164:167], v224, %[rs], %[sb] offen offset:1024 sc1\n\t"
        "buffer_load_dwordx4 v[168:171], v225, %[rs], %[sb] offen sc1\n\t"
        "buffer_load_dwordx4 v[172:175], v226, %[rs], %[sb] offen sc1\n\t"
        "ds_read_b128 v[80:83], v236\n\t"
        "ds_read_b32 v237, v237\n\t"
        "ds_read_b128 v[112:115], v241\n\t"
        "ds_read_b128 v[116:119], v241 offset:1024\n\t"
        "ds_read_b128 v[120:123], v242\n\t"
        "ds_read_b128 v[124:127], v242 offset:1024\n\t"
        "ds_read_b128 v[128:131], v243\n\t"
        "ds_read_b128 v[132:135], v243 offset:1024\n\t"
        "ds_read_b128 v[136:139], v244\n\t"
        "ds_read_b128 v[140:143], v244 offset:1024\n\t"
        "s_waitcnt lgkmcnt(0)\n\t"
        "s_barrier\n\t"
        "v_max3_f32 v236, v80, v81, v82\n\t"
        "v_max_f32 v236, v236, v83\n\t"
        "v_mul_f32 v236, v236, v237\n\t"
        "v_sqrt_f32 v236, v236\n\t"
        "s_nop 0\n\t"
        "v_mul_f32 v96, -0.5, v236\n\t"
        "v_mov_b32 v97, v96\n\t"
        "v_mov_b64 v[98:99], v[96:97]\n\t"
        "v_mov_b64 v[100:101], v[96:97]\n\t"
        "v_mov_b64 v[102:103], v[96:97]\n\t"
        "v_mov_b64 v[104:105], v[96:97]\n\t"
        "v_mov_b64 v[106:107], v[96:97]\n\t"
        "v_mov_b64 v[108:109], v[96:97]\n\t"
        "v_mov_b64 v[110:111], v[96:97]\n\t"
        "buffer_load_dwordx4 v[176:179], v227, %[rs], %[sb] offen sc1\n\t"
        "buffer_load_dwordx4 v[180:183], v227, %[rs], %[sb] offen offset:1024 sc1\n\t"
        "buffer_load_dwordx4 v[184:187], v228, %[rs], %[sb] offen sc1\n\t"
        "buffer_load_dwordx4 v[188:191], v229, %[rs], %[sb] offen sc1\n\t"
        "s_waitcnt vmcnt(4)\n\t"
        "v_mfma_f32_32x32x16_bf16 v[64:79], v[160:163], v[112:115], v[96:111]\n\t"
        "v_mfma_f32_32x32x16_bf16 v[64:79], v[164:167], v[116:119], v[64:79]\n\t"
        "buffer_load_dwordx4 v[192:195], v230, %[rs], %[sb] offen sc1\n\t"
        "buffer_load_dwordx4 v[196:199], v230, %[rs], %[sb] offen offset:1024 sc1\n\t"
        "buffer_load_dwordx4 v[200:203], v231, %[rs], %[sb] offen sc1\n\t"
        "buffer_load_dwordx4 v[204:207], v232, %[rs], %[sb] offen sc1\n\t"
        "s_nop 9\n\t"
        "LOOP%=:\n\t"
        "v_mfma_f32_32x32x16_bf16 v[80:95], v[160:163], v[120:123], v[96:111]\n\t"
        "v_exp_f32 v64, v64\n\t"
        "v_exp_f32 v65, v65\n\t"
        "v_exp_f32 v66, v66\n\t"
        "v_exp_f32 v67, v67\n\t"
        "v_mfma_f32_32x32x16_bf16 v[80:95], v[164:167], v[124:127], v[80:95]\n\t"
        "v_exp_f32 v68, v68\n\t"
        "v_exp_f32 v69, v69\n\t"
        "v_exp_f32 v70, v70\n\t"
        "v_exp_f32 v71, v71\n\t"
        "v_cvt_pk_bf16_f32 v144, v64, v65\n\t"
        "v_cvt_pk_bf16_f32 v145, v66, v67\n\t"
        "v_mfma_f32_32x32x16_bf16 v[48:63], v[216:219], v[152:155], v[48:63]\n\t"
        "v_exp_f32 v72, v72\n\t"
        "v_exp_f32 v73, v73\n\t"
        "v_exp_f32 v74, v74\n\t"
        "v_exp_f32 v75, v75\n\t"
        "v_cvt_pk_bf16_f32 v146, v68, v69\n\t"
        "v_cvt_pk_bf16_f32 v147, v70, v71\n\t"
        "v_mfma_f32_32x32x16_bf16 v[48:63], v[220:223], v[156:159], v[48:63]\n\t"
        "v_exp_f32 v76, v76\n\t"
        "v_exp_f32 v77, v77\n\t"
        "v_exp_f32 v78, v78\n\t"
        "v_exp_f32 v79, v79\n\t"
        "v_cvt_pk_bf16_f32 v148, v72, v73\n\t"
        "v_cvt_pk_bf16_f32 v149, v74, v75\n\t"
        "v_cvt_pk_bf16_f32 v150, v76, v77\n\t"
        "v_cvt_pk_bf16_f32 v151, v78, v79\n\t"
        "v_mfma_f32_32x32x16_bf16 v[64:79], v[160:163], v[128:131], v[96:111]\n\t"
        "buffer_load_dwordx4 v[208:211], v233, %[rs], %[sb] offen sc1\n\t"
        "buffer_load_dwordx4 v[212:215], v233, %[rs], %[sb] offen offset:1024 sc1\n\t"
        "buffer_load_dwordx4 v[216:219], v234, %[rs], %[sb] offen sc1\n\t"
        "buffer_load_dwordx4 v[220:223], v235, %[rs], %[sb] offen sc1\n\t"
        "v_exp_f32 v80, v80\n\t"
        "v_exp_f32 v81, v81\n\t"
        "v_exp_f32 v82, v82\n\t"
        "v_exp_f32 v83, v83\n\t"
        "v_mfma_f32_32x32x16_bf16 v[64:79], v[164:167], v[132:135], v[64:79]\n\t"
        "v_exp_f32 v84, v84\n\t"
        "v_exp_f32 v85, v85\n\t"
        "v_exp_f32 v86, v86\n\t"
        "v_exp_f32 v87, v87\n\t"
        "v_cvt_pk_bf16_f32 v152, v80, v81\n\t"
        "v_cvt_pk_bf16_f32 v153, v82, v83\n\t"
        "v_mfma_f32_32x32x16_bf16 v[0:15], v[168:171], v[144:147], v[0:15]\n\t"
        "v_exp_f32 v88, v88\n\t"
        "v_exp_f32 v89, v89\n\t"
        "v_exp_f32 v90, v90\n\t"
        "v_exp_f32 v91, v91\n\t"
        "v_cvt_pk_bf16_f32 v154, v84, v85\n\t"
        "v_cvt_pk_bf16_f32 v155, v86, v87\n\t"
        "v_mfma_f32_32x32x16_bf16 v[0:15], v[172:175], v[148:151], v[0:15]\n\t"
        "v_exp_f32 v92, v92\n\t"
        "v_exp_f32 v93, v93\n\t"
        "v_exp_f32 v94, v94\n\t"
        "v_exp_f32 v95, v95\n\t"
        "v_cvt_pk_bf16_f32 v156, v88, v89\n\t"
        "v_cvt_pk_bf16_f32 v157, v90, v91\n\t"
        "v_cvt_pk_bf16_f32 v158, v92, v93\n\t"
        "v_cvt_pk_bf16_f32 v159, v94, v95\n\t"
        "v_mfma_f32_32x32x16_bf16 v[80:95], v[160:163], v[136:139], v[96:111]\n\t"
        "ds_read_b32 v239, v238\n\t"
        "v_exp_f32 v64, v64\n\t"
        "v_exp_f32 v65, v65\n\t"
        "v_exp_f32 v66, v66\n\t"
        "v_exp_f32 v67, v67\n\t"
        "v_mfma_f32_32x32x16_bf16 v[80:95], v[164:167], v[140:143], v[80:95]\n\t"
        "v_exp_f32 v68, v68\n\t"
        "v_exp_f32 v69, v69\n\t"
        "v_exp_f32 v70, v70\n\t"
        "v_exp_f32 v71, v71\n\t"
        "v_cvt_pk_bf16_f32 v144, v64, v65\n\t"
        "v_cvt_pk_bf16_f32 v145, v66, v67\n\t"
        "v_mfma_f32_32x32x16_bf16 v[16:31], v[168:171], v[152:155], v[16:31]\n\t"
        "v_exp_f32 v72, v72\n\t"
        "v_exp_f32 v73, v73\n\t"
        "v_exp_f32 v74, v74\n\t"
        "v_exp_f32 v75, v75\n\t"
        "v_cvt_pk_bf16_f32 v146, v68, v69\n\t"
        "v_cvt_pk_bf16_f32 v147, v70, v71\n\t"
        "v_mfma_f32_32x32x16_bf16 v[16:31], v[172:175], v[156:159], v[16:31]\n\t"
        "v_exp_f32 v76, v76\n\t"
        "v_exp_f32 v77, v77\n\t"
        "v_exp_f32 v78, v78\n\t"
        "v_exp_f32 v79, v79\n\t"
        "v_cvt_pk_bf16_f32 v148, v72, v73\n\t"
        "v_cvt_pk_bf16_f32 v149, v74, v75\n\t"
        "v_cvt_pk_bf16_f32 v150, v76, v77\n\t"
        "v_cvt_pk_bf16_f32 v151, v78, v79\n\t"
        "s_waitcnt vmcnt(8)\n\t"
        "v_mfma_f32_32x32x16_bf16 v[64:79], v[176:179], v[112:115], v[96:111]\n\t"
        "v_exp_f32 v80, v80\n\t"
        "v_exp_f32 v81, v81\n\t"
        "v_exp_f32 v82, v82\n\t"
        "v_exp_f32 v83, v83\n\t"
        "v_mfma_f32_32x32x16_bf16 v[64:79], v[180:183], v[116:119], v[64:79]\n\t"
        "v_exp_f32 v84, v84\n\t"
        "v_exp_f32 v85, v85\n\t"
        "v_exp_f32 v86, v86\n\t"
        "v_exp_f32 v87, v87\n\t"
        "v_cvt_pk_bf16_f32 v152, v80, v81\n\t"
        "v_cvt_pk_bf16_f32 v153, v82, v83\n\t"
        "v_mfma_f32_32x32x16_bf16 v[32:47], v[168:171], v[144:147], v[32:47]\n\t"
        "v_exp_f32 v88, v88\n\t"
        "v_exp_f32 v89, v89\n\t"
        "v_exp_f32 v90, v90\n\t"
        "v_exp_f32 v91, v91\n\t"
        "v_cvt_pk_bf16_f32 v154, v84, v85\n\t"
        "v_cvt_pk_bf16_f32 v155, v86, v87\n\t"
        "v_mfma_f32_32x32x16_bf16 v[32:47], v[172:175], v[148:151], v[32:47]\n\t"
        "v_exp_f32 v92, v92\n\t"
        "v_exp_f32 v93, v93\n\t"
        "v_exp_f32 v94, v94\n\t"
        "v_exp_f32 v95, v95\n\t"
        "v_cvt_pk_bf16_f32 v156, v88, v89\n\t"
        "v_cvt_pk_bf16_f32 v157, v90, v91\n\t"
        "v_cvt_pk_bf16_f32 v158, v92, v93\n\t"
        "v_cvt_pk_bf16_f32 v159, v94, v95\n\t"
        "v_mfma_f32_32x32x16_bf16 v[80:95], v[176:179], v[120:123], v[96:111]\n\t"
        "CHK%=:\n\t"
        "s_waitcnt lgkmcnt(0)\n\t"
        "v_readfirstlane_b32 %[sb], v239\n\t"
        "ds_read_b32 v239, v238\n\t"
        "s_cmp_eq_u32 %[sb], -1\n\t"
        "s_cbranch_scc1 CHK%=\n\t"
        "v_add_u32 v238, 4, v238\n\t"
        "v_exp_f32 v64, v64\n\t"
        "v_exp_f32 v65, v65\n\t"
        "v_exp_f32 v66, v66\n\t"
        "v_exp_f32 v67, v67\n\t"
        "v_mfma_f32_32x32x16_bf16 v[80:95], v[180:183], v[124:127], v[80:95]\n\t"
        "v_exp_f32 v68, v68\n\t"
        "v_exp_f32 v69, v69\n\t"
        "v_exp_f32 v70, v70\n\t"
        "v_exp_f32 v71, v71\n\t"
        "v_cvt_pk_bf16_f32 v144, v64, v65\n\t"
        "v_cvt_pk_bf16_f32 v145, v66, v67\n\t"
        "v_mfma_f32_32x32x16_bf16 v[48:63], v[168:171], v[152:155], v[48:63]\n\t"
        "v_exp_f32 v72, v72\n\t"
        "v_exp_f32 v73, v73\n\t"
        "v_exp_f32 v74, v74\n\t"
        "v_exp_f32 v75, v75\n\t"
        "v_cvt_pk_bf16_f32 v146, v68, v69\n\t"
        "v_cvt_pk_bf16_f32 v147, v70, v71\n\t"
        "v_mfma_f32_32x32x16_bf16 v[48:63], v[172:175], v[156:159], v[48:63]\n\t"
        "v_exp_f32 v76, v76\n\t"
        "v_exp_f32 v77, v77\n\t"
        "v_exp_f32 v78, v78\n\t"
        "v_exp_f32 v79, v79\n\t"
        "v_cvt_pk_bf16_f32 v148, v72, v73\n\t"
        "v_cvt_pk_bf16_f32 v149, v74, v75\n\t"
        "v_cvt_pk_bf16_f32 v150, v76, v77\n\t"
        "v_cvt_pk_bf16_f32 v151, v78, v79\n\t"
        "v_mfma_f32_32x32x16_bf16 v[64:79], v[176:179], v[128:131], v[96:111]\n\t"
        "buffer_load_dwordx4 v[160:163], v224, %[rs], %[sb] offen sc1\n\t"
        "buffer_load_dwordx4 v[164:167], v224, %[rs], %[sb] offen offset:1024 sc1\n\t"
        "buffer_load_dwordx4 v[168:171], v225, %[rs], %[sb] offen sc1\n\t"
        "buffer_load_dwordx4 v[172:175], v226, %[rs], %[sb] offen sc1\n\t"
        "v_exp_f32 v80, v80\n\t"
        "v_exp_f32 v81, v81\n\t"
        "v_exp_f32 v82, v82\n\t"
        "v_exp_f32 v83, v83\n\t"
        "v_mfma_f32_32x32x16_bf16 v[64:79], v[180:183], v[132:135], v[64:79]\n\t"
        "v_exp_f32 v84, v84\n\t"
        "v_exp_f32 v85, v85\n\t"
        "v_exp_f32 v86, v86\n\t"
        "v_exp_f32 v87, v87\n\t"
        "v_cvt_pk_bf16_f32 v152, v80, v81\n\t"
        "v_cvt_pk_bf16_f32 v153, v82, v83\n\t"
        "v_mfma_f32_32x32x16_bf16 v[0:15], v[184:187], v[144:147], v[0:15]\n\t"
        "v_exp_f32 v88, v88\n\t"
        "v_exp_f32 v89, v89\n\t"
        "v_exp_f32 v90, v90\n\t"
        "v_exp_f32 v91, v91\n\t"
        "v_cvt_pk_bf16_f32 v154, v84, v85\n\t"
        "v_cvt_pk_bf16_f32 v155, v86, v87\n\t"
        "v_mfma_f32_32x32x16_bf16 v[0:15], v[188:191], v[148:151], v[0:15]\n\t"
        "v_exp_f32 v92, v92\n\t"
        "v_exp_f32 v93, v93\n\t"
        "v_exp_f32 v94, v94\n\t"
        "v_exp_f32 v95, v95\n\t"
        "v_cvt_pk_bf16_f32 v156, v88, v89\n\t"
        "v_cvt_pk_bf16_f32 v157, v90, v91\n\t"
        "v_cvt_pk_bf16_f32 v158, v92, v93\n\t"
        "v_cvt_pk_bf16_f32 v159, v94, v95\n\t"
        "v_mfma_f32_32x32x16_bf16 v[80:95], v[176:179], v[136:139], v[96:111]\n\t"
        "v_exp_f32 v64, v64\n\t"
        "v_exp_f32 v65, v65\n\t"
        "v_exp_f32 v66, v66\n\t"
        "v_exp_f32 v67, v67\n\t"
        "v_mfma_f32_32x32x16_bf16 v[80:95], v[180:183], v[140:143], v[80:95]\n\t"
        "v_exp_f32 v68, v68\n\t"
        "v_exp_f32 v69, v69\n\t"
        "v_exp_f32 v70, v70\n\t"
        "v_exp_f32 v71, v71\n\t"
        "v_cvt_pk_bf16_f32 v144, v64, v65\n\t"
        "v_cvt_pk_bf16_f32 v145, v66, v67\n\t"
        "v_mfma_f32_32x32x16_bf16 v[16:31], v[184:187], v[152:155], v[16:31]\n\t"
        "v_exp_f32 v72, v72\n\t"
        "v_exp_f32 v73, v73\n\t"
        "v_exp_f32 v74, v74\n\t"
        "v_exp_f32 v75, v75\n\t"
        "v_cvt_pk_bf16_f32 v146, v68, v69\n\t"
        "v_cvt_pk_bf16_f32 v147, v70, v71\n\t"
        "v_mfma_f32_32x32x16_bf16 v[16:31], v[188:191], v[156:159], v[16:31]\n\t"
        "v_exp_f32 v76, v76\n\t"
        "v_exp_f32 v77, v77\n\t"
        "v_exp_f32 v78, v78\n\t"
        "v_exp_f32 v79, v79\n\t"
        "v_cvt_pk_bf16_f32 v148, v72, v73\n\t"
        "v_cvt_pk_bf16_f32 v149, v74, v75\n\t"
        "v_cvt_pk_bf16_f32 v150, v76, v77\n\t"
        "v_cvt_pk_bf16_f32 v151, v78, v79\n\t"
        "s_waitcnt vmcnt(8)\n\t"
        "v_mfma_f32_32x32x16_bf16 v[64:79], v[192:195], v[112:115], v[96:111]\n\t"
        "v_exp_f32 v80, v80\n\t"
        "v_exp_f32 v81, v81\n\t"
        "v_exp_f32 v82, v82\n\t"
        "v_exp_f32 v83, v83\n\t"
        "v_mfma_f32_32x32x16_bf16 v[64:79], v[196:199], v[116:119], v[64:79]\n\t"
        "v_exp_f32 v84, v84\n\t"
        "v_exp_f32 v85, v85\n\t"
        "v_exp_f32 v86, v86\n\t"
        "v_exp_f32 v87, v87\n\t"
        "v_cvt_pk_bf16_f32 v152, v80, v81\n\t"
        "v_cvt_pk_bf16_f32 v153, v82, v83\n\t"
        "v_mfma_f32_32x32x16_bf16 v[32:47], v[184:187], v[144:147], v[32:47]\n\t"
        "v_exp_f32 v88, v88\n\t"
        "v_exp_f32 v89, v89\n\t"
        "v_exp_f32 v90, v90\n\t"
        "v_exp_f32 v91, v91\n\t"
        "v_cvt_pk_bf16_f32 v154, v84, v85\n\t"
        "v_cvt_pk_bf16_f32 v155, v86, v87\n\t"
        "v_mfma_f32_32x32x16_bf16 v[32:47], v[188:191], v[148:151], v[32:47]\n\t"
        "v_exp_f32 v92, v92\n\t"
        "v_exp_f32 v93, v93\n\t"
        "v_exp_f32 v94, v94\n\t"
        "v_exp_f32 v95, v95\n\t"
        "v_cvt_pk_bf16_f32 v156, v88, v89\n\t"
        "v_cvt_pk_bf16_f32 v157, v90, v91\n\t"
        "v_cvt_pk_bf16_f32 v158, v92, v93\n\t"
        "v_cvt_pk_bf16_f32 v159, v94, v95\n\t"
        "v_mfma_f32_32x32x16_bf16 v[80:95], v[192:195], v[120:123], v[96:111]\n\t"
        "v_exp_f32 v64, v64\n\t"
        "v_exp_f32 v65, v65\n\t"
        "v_exp_f32 v66, v66\n\t"
        "v_exp_f32 v67, v67\n\t"
        "v_mfma_f32_32x32x16_bf16 v[80:95], v[196:199], v[124:127], v[80:95]\n\t"
        "v_exp_f32 v68, v68\n\t"
        "v_exp_f32 v69, v69\n\t"
        "v_exp_f32 v70, v70\n\t"
        "v_exp_f32 v71, v71\n\t"
        "v_cvt_pk_bf16_f32 v144, v64, v65\n\t"
        "v_cvt_pk_bf16_f32 v145, v66, v67\n\t"
        "v_mfma_f32_32x32x16_bf16 v[48:63], v[184:187], v[152:155], v[48:63]\n\t"
        "v_exp_f32 v72, v72\n\t"
        "v_exp_f32 v73, v73\n\t"
        "v_exp_f32 v74, v74\n\t"
        "v_exp_f32 v75, v75\n\t"
        "v_cvt_pk_bf16_f32 v146, v68, v69\n\t"
        "v_cvt_pk_bf16_f32 v147, v70, v71\n\t"
        "v_mfma_f32_32x32x16_bf16 v[48:63], v[188:191], v[156:159], v[48:63]\n\t"
        "v_exp_f32 v76, v76\n\t"
        "v_exp_f32 v77, v77\n\t"
        "v_exp_f32 v78, v78\n\t"
        "v_exp_f32 v79, v79\n\t"
        "v_cvt_pk_bf16_f32 v148, v72, v73\n\t"
        "v_cvt_pk_bf16_f32 v149, v74, v75\n\t"
        "v_cvt_pk_bf16_f32 v150, v76, v77\n\t"
        "v_cvt_pk_bf16_f32 v151, v78, v79\n\t"
        "v_mfma_f32_32x32x16_bf16 v[64:79], v[192:195], v[128:131], v[96:111]\n\t"
        "buffer_load_dwordx4 v[176:179], v227, %[rs], %[sb] offen sc1\n\t"
        "buffer_load_dwordx4 v[180:183], v227, %[rs], %[sb] offen offset:1024 sc1\n\t"
        "buffer_load_dwordx4 v[184:187], v228, %[rs], %[sb] offen sc1\n\t"
        "buffer_load_dwordx4 v[188:191], v229, %[rs], %[sb] offen sc1\n\t"
        "v_exp_f32 v80, v80\n\t"
        "v_exp_f32 v81, v81\n\t"
        "v_exp_f32 v82, v82\n\t"
        "v_exp_f32 v83, v83\n\t"
        "v_mfma_f32_32x32x16_bf16 v[64:79], v[196:199], v[132:135], v[64:79]\n\t"
        "v_exp_f32 v84, v84\n\t"
        "v_exp_f32 v85, v85\n\t"
        "v_exp_f32 v86, v86\n\t"
        "v_exp_f32 v87, v87\n\t"
        "v_cvt_pk_bf16_f32 v152, v80, v81\n\t"
        "v_cvt_pk_bf16_f32 v153, v82, v83\n\t"
        "v_mfma_f32_32x32x16_bf16 v[0:15], v[200:203], v[144:147], v[0:15]\n\t"
        "v_exp_f32 v88, v88\n\t"
        "v_exp_f32 v89, v89\n\t"
        "v_exp_f32 v90, v90\n\t"
        "v_exp_f32 v91, v91\n\t"
        "v_cvt_pk_bf16_f32 v154, v84, v85\n\t"
        "v_cvt_pk_bf16_f32 v155, v86, v87\n\t"
        "v_mfma_f32_32x32x16_bf16 v[0:15], v[204:207], v[148:151], v[0:15]\n\t"
        "v_exp_f32 v92, v92\n\t"
        "v_exp_f32 v93, v93\n\t"
        "v_exp_f32 v94, v94\n\t"
        "v_exp_f32 v95, v95\n\t"
        "v_cvt_pk_bf16_f32 v156, v88, v89\n\t"
        "v_cvt_pk_bf16_f32 v157, v90, v91\n\t"
        "v_cvt_pk_bf16_f32 v158, v92, v93\n\t"
        "v_cvt_pk_bf16_f32 v159, v94, v95\n\t"
        "v_mfma_f32_32x32x16_bf16 v[80:95], v[192:195], v[136:139], v[96:111]\n\t"
        "v_exp_f32 v64, v64\n\t"
        "v_exp_f32 v65, v65\n\t"
        "v_exp_f32 v66, v66\n\t"
        "v_exp_f32 v67, v67\n\t"
        "v_mfma_f32_32x32x16_bf16 v[80:95], v[196:199], v[140:143], v[80:95]\n\t"
        "v_exp_f32 v68, v68\n\t"
        "v_exp_f32 v69, v69\n\t"
        "v_exp_f32 v70, v70\n\t"
        "v_exp_f32 v71, v71\n\t"
        "v_cvt_pk_bf16_f32 v144, v64, v65\n\t"
        "v_cvt_pk_bf16_f32 v145, v66, v67\n\t"
        "v_mfma_f32_32x32x16_bf16 v[16:31], v[200:203], v[152:155], v[16:31]\n\t"
        "v_exp_f32 v72, v72\n\t"
        "v_exp_f32 v73, v73\n\t"
        "v_exp_f32 v74, v74\n\t"
        "v_exp_f32 v75, v75\n\t"
        "v_cvt_pk_bf16_f32 v146, v68, v69\n\t"
        "v_cvt_pk_bf16_f32 v147, v70, v71\n\t"
        "v_mfma_f32_32x32x16_bf16 v[16:31], v[204:207], v[156:159], v[16:31]\n\t"
        "v_exp_f32 v76, v76\n\t"
        "v_exp_f32 v77, v77\n\t"
        "v_exp_f32 v78, v78\n\t"
        "v_exp_f32 v79, v79\n\t"
        "v_cvt_pk_bf16_f32 v148, v72, v73\n\t"
        "v_cvt_pk_bf16_f32 v149, v74, v75\n\t"
        "v_cvt_pk_bf16_f32 v150, v76, v77\n\t"
        "v_cvt_pk_bf16_f32 v151, v78, v79\n\t"
        "s_waitcnt vmcnt(8)\n\t"
        "v_mfma_f32_32x32x16_bf16 v[64:79], v[208:211], v[112:115], v[96:111]\n\t"
        "v_exp_f32 v80, v80\n\t"
        "v_exp_f32 v81, v81\n\t"
        "v_exp_f32 v82, v82\n\t"
        "v_exp_f32 v83, v83\n\t"
        "v_mfma_f32_32x32x16_bf16 v[64:79], v[212:215], v[116:119], v[64:79]\n\t"
        "v_exp_f32 v84, v84\n\t"
        "v_exp_f32 v85, v85\n\t"
        "v_exp_f32 v86, v86\n\t"
        "v_exp_f32 v87, v87\n\t"
        "v_cvt_pk_bf16_f32 v152, v80, v81\n\t"
        "v_cvt_pk_bf16_f32 v153, v82, v83\n\t"
        "v_mfma_f32_32x32x16_bf16 v[32:47], v[200:203], v[144:147], v[32:47]\n\t"
        "v_exp_f32 v88, v88\n\t"
        "v_exp_f32 v89, v89\n\t"
        "v_exp_f32 v90, v90\n\t"
        "v_exp_f32 v91, v91\n\t"
        "v_cvt_pk_bf16_f32 v154, v84, v85\n\t"
        "v_cvt_pk_bf16_f32 v155, v86, v87\n\t"
        "v_mfma_f32_32x32x16_bf16 v[32:47], v[204:207], v[148:151], v[32:47]\n\t"
        "v_exp_f32 v92, v92\n\t"
        "v_exp_f32 v93, v93\n\t"
        "v_exp_f32 v94, v94\n\t"
        "v_exp_f32 v95, v95\n\t"
        "v_cvt_pk_bf16_f32 v156, v88, v89\n\t"
        "v_cvt_pk_bf16_f32 v157, v90, v91\n\t"
        "v_cvt_pk_bf16_f32 v158, v92, v93\n\t"
        "v_cvt_pk_bf16_f32 v159, v94, v95\n\t"
        "v_mfma_f32_32x32x16_bf16 v[80:95], v[208:211], v[120:123], v[96:111]\n\t"
        "v_exp_f32 v64, v64\n\t"
        "v_exp_f32 v65, v65\n\t"
        "v_exp_f32 v66, v66\n\t"
        "v_exp_f32 v67, v67\n\t"
        "v_mfma_f32_32x32x16_bf16 v[80:95], v[212:215], v[124:127], v[80:95]\n\t"
        "v_exp_f32 v68, v68\n\t"
        "v_exp_f32 v69, v69\n\t"
        "v_exp_f32 v70, v70\n\t"
        "v_exp_f32 v71, v71\n\t"
        "v_cvt_pk_bf16_f32 v144, v64, v65\n\t"
        "v_cvt_pk_bf16_f32 v145, v66, v67\n\t"
        "v_mfma_f32_32x32x16_bf16 v[48:63], v[200:203], v[152:155], v[48:63]\n\t"
        "v_exp_f32 v72, v72\n\t"
        "v_exp_f32 v73, v73\n\t"
        "v_exp_f32 v74, v74\n\t"
        "v_exp_f32 v75, v75\n\t"
        "v_cvt_pk_bf16_f32 v146, v68, v69\n\t"
        "v_cvt_pk_bf16_f32 v147, v70, v71\n\t"
        "v_mfma_f32_32x32x16_bf16 v[48:63], v[204:207], v[156:159], v[48:63]\n\t"
        "v_exp_f32 v76, v76\n\t"
        "v_exp_f32 v77, v77\n\t"
        "v_exp_f32 v78, v78\n\t"
        "v_exp_f32 v79, v79\n\t"
        "v_cvt_pk_bf16_f32 v148, v72, v73\n\t"
        "v_cvt_pk_bf16_f32 v149, v74, v75\n\t"
        "v_cvt_pk_bf16_f32 v150, v76, v77\n\t"
        "v_cvt_pk_bf16_f32 v151, v78, v79\n\t"
        "v_mfma_f32_32x32x16_bf16 v[64:79], v[208:211], v[128:131], v[96:111]\n\t"
        "buffer_load_dwordx4 v[192:195], v230, %[rs], %[sb] offen sc1\n\t"
        "buffer_load_dwordx4 v[196:199], v230, %[rs], %[sb] offen offset:1024 sc1\n\t"
        "buffer_load_dwordx4 v[200:203], v231, %[rs], %[sb] offen sc1\n\t"
        "buffer_load_dwordx4 v[204:207], v232, %[rs], %[sb] offen sc1\n\t"
        "s_sub_u32 %[cnt], %[cnt], 1\n\t"
        "s_cmp_eq_u32 %[cnt], 0\n\t"
        "v_exp_f32 v80, v80\n\t"
        "v_exp_f32 v81, v81\n\t"
        "v_exp_f32 v82, v82\n\t"
        "v_exp_f32 v83, v83\n\t"
        "v_mfma_f32_32x32x16_bf16 v[64:79], v[212:215], v[132:135], v[64:79]\n\t"
        "v_exp_f32 v84, v84\n\t"
        "v_exp_f32 v85, v85\n\t"
        "v_exp_f32 v86, v86\n\t"
        "v_exp_f32 v87, v87\n\t"
        "v_cvt_pk_bf16_f32 v152, v80, v81\n\t"
        "v_cvt_pk_bf16_f32 v153, v82, v83\n\t"
        "v_mfma_f32_32x32x16_bf16 v[0:15], v[216:219], v[144:147], v[0:15]\n\t"
        "v_exp_f32 v88, v88\n\t"
        "v_exp_f32 v89, v89\n\t"
        "v_exp_f32 v90, v90\n\t"
        "v_exp_f32 v91, v91\n\t"
        "v_cvt_pk_bf16_f32 v154, v84, v85\n\t"
        "v_cvt_pk_bf16_f32 v155, v86, v87\n\t"
        "v_mfma_f32_32x32x16_bf16 v[0:15], v[220:223], v[148:151], v[0:15]\n\t"
        "v_exp_f32 v92, v92\n\t"
        "v_exp_f32 v93, v93\n\t"
        "v_exp_f32 v94, v94\n\t"
        "v_exp_f32 v95, v95\n\t"
        "v_cvt_pk_bf16_f32 v156, v88, v89\n\t"
        "v_cvt_pk_bf16_f32 v157, v90, v91\n\t"
        "v_cvt_pk_bf16_f32 v158, v92, v93\n\t"
        "v_cvt_pk_bf16_f32 v159, v94, v95\n\t"
        "s_cbranch_scc1 TAIL%=\n\t"
        "v_mfma_f32_32x32x16_bf16 v[80:95], v[208:211], v[136:139], v[96:111]\n\t"
        "v_exp_f32 v64, v64\n\t"
        "v_exp_f32 v65, v65\n\t"
        "v_exp_f32 v66, v66\n\t"
        "v_exp_f32 v67, v67\n\t"
        "v_mfma_f32_32x32x16_bf16 v[80:95], v[212:215], v[140:143], v[80:95]\n\t"
        "v_exp_f32 v68, v68\n\t"
        "v_exp_f32 v69, v69\n\t"
        "v_exp_f32 v70, v70\n\t"
        "v_exp_f32 v71, v71\n\t"
        "v_cvt_pk_bf16_f32 v144, v64, v65\n\t"
        "v_cvt_pk_bf16_f32 v145, v66, v67\n\t"
        "v_mfma_f32_32x32x16_bf16 v[16:31], v[216:219], v[152:155], v[16:31]\n\t"
        "v_exp_f32 v72, v72\n\t"
        "v_exp_f32 v73, v73\n\t"
        "v_exp_f32 v74, v74\n\t"
        "v_exp_f32 v75, v75\n\t"
        "v_cvt_pk_bf16_f32 v146, v68, v69\n\t"
        "v_cvt_pk_bf16_f32 v147, v70, v71\n\t"
        "v_mfma_f32_32x32x16_bf16 v[16:31], v[220:223], v[156:159], v[16:31]\n\t"
        "v_exp_f32 v76, v76\n\t"
        "v_exp_f32 v77, v77\n\t"
        "v_exp_f32 v78, v78\n\t"
        "v_exp_f32 v79, v79\n\t"
        "v_cvt_pk_bf16_f32 v148, v72, v73\n\t"
        "v_cvt_pk_bf16_f32 v149, v74, v75\n\t"
        "v_cvt_pk_bf16_f32 v150, v76, v77\n\t"
        "v_cvt_pk_bf16_f32 v151, v78, v79\n\t"
        "s_waitcnt vmcnt(8)\n\t"
        "v_mfma_f32_32x32x16_bf16 v[64:79], v[160:163], v[112:115], v[96:111]\n\t"
        "v_exp_f32 v80, v80\n\t"
        "v_exp_f32 v81, v81\n\t"
        "v_exp_f32 v82, v82\n\t"
        "v_exp_f32 v83, v83\n\t"
        "v_mfma_f32_32x32x16_bf16 v[64:79], v[164:167], v[116:119], v[64:79]\n\t"
        "v_exp_f32 v84, v84\n\t"
        "v_exp_f32 v85, v85\n\t"
        "v_exp_f32 v86, v86\n\t"
        "v_exp_f32 v87, v87\n\t"
        "v_cvt_pk_bf16_f32 v152, v80, v81\n\t"
        "v_cvt_pk_bf16_f32 v153, v82, v83\n\t"
        "v_mfma_f32_32x32x16_bf16 v[32:47], v[216:219], v[144:147], v[32:47]\n\t"
        "v_exp_f32 v88, v88\n\t"
        "v_exp_f32 v89, v89\n\t"
        "v_exp_f32 v90, v90\n\t"
        "v_exp_f32 v91, v91\n\t"
        "v_cvt_pk_bf16_f32 v154, v84, v85\n\t"
        "v_cvt_pk_bf16_f32 v155, v86, v87\n\t"
        "v_mfma_f32_32x32x16_bf16 v[32:47], v[220:223], v[148:151], v[32:47]\n\t"
        "v_exp_f32 v92, v92\n\t"
        "v_exp_f32 v93, v93\n\t"
        "v_exp_f32 v94, v94\n\t"
        "v_exp_f32 v95, v95\n\t"
        "v_cvt_pk_bf16_f32 v156, v88, v89\n\t"
        "v_cvt_pk_bf16_f32 v157, v90, v91\n\t"
        "v_cvt_pk_bf16_f32 v158, v92, v93\n\t"
        "v_cvt_pk_bf16_f32 v159, v94, v95\n\t"
        "s_branch LOOP%=\n\t"
        "TAIL%=:\n\t"
        "v_mfma_f32_32x32x16_bf16 v[80:95], v[208:211], v[136:139], v[96:111]\n\t"
        "v_exp_f32 v64, v64\n\t"
        "v_exp_f32 v65, v65\n\t"
        "v_exp_f32 v66, v66\n\t"
        "v_exp_f32 v67, v67\n\t"
        "v_mfma_f32_32x32x16_bf16 v[80:95], v[212:215], v[140:143], v[80:95]\n\t"
        "ds_write_b128 v240, v[0:3] offset:0\n\t"
        "ds_write_b128 v240, v[4:7] offset:1024\n\t"
        "ds_write_b128 v240, v[8:11] offset:2048\n\t"
        "ds_write_b128 v240, v[12:15] offset:3072\n\t"
        "v_exp_f32 v68, v68\n\t"
        "v_exp_f32 v69, v69\n\t"
        "v_exp_f32 v70, v70\n\t"
        "v_exp_f32 v71, v71\n\t"
        "v_cvt_pk_bf16_f32 v144, v64, v65\n\t"
        "v_cvt_pk_bf16_f32 v145, v66, v67\n\t"
        "v_mfma_f32_32x32x16_bf16 v[16:31], v[216:219], v[152:155], v[16:31]\n\t"
        "v_exp_f32 v72, v72\n\t"
        "v_exp_f32 v73, v73\n\t"
        "v_exp_f32 v74, v74\n\t"
        "v_exp_f32 v75, v75\n\t"
        "v_cvt_pk_bf16_f32 v146, v68, v69\n\t"
        "v_cvt_pk_bf16_f32 v147, v70, v71\n\t"
        "v_mfma_f32_32x32x16_bf16 v[16:31], v[220:223], v[156:159], v[16:31]\n\t"
        "v_exp_f32 v76, v76\n\t"
        "v_exp_f32 v77, v77\n\t"
        "v_exp_f32 v78, v78\n\t"
        "v_exp_f32 v79, v79\n\t"
        "v_cvt_pk_bf16_f32 v148, v72, v73\n\t"
        "v_cvt_pk_bf16_f32 v149, v74, v75\n\t"
        "v_cvt_pk_bf16_f32 v150, v76, v77\n\t"
        "v_cvt_pk_bf16_f32 v151, v78, v79\n\t"
        "v_exp_f32 v80, v80\n\t"
        "v_exp_f32 v81, v81\n\t"
        "v_exp_f32 v82, v82\n\t"
        "v_exp_f32 v83, v83\n\t"
        "v_exp_f32 v84, v84\n\t"
        "v_exp_f32 v85, v85\n\t"
        "v_exp_f32 v86, v86\n\t"
        "v_exp_f32 v87, v87\n\t"
        "v_cvt_pk_bf16_f32 v152, v80, v81\n\t"
        "v_cvt_pk_bf16_f32 v153, v82, v83\n\t"
        "v_mfma_f32_32x32x16_bf16 v[32:47], v[216:219], v[144:147], v[32:47]\n\t"
        "ds_write_b128 v240, v[16:19] offset:4096\n\t"
        "ds_write_b128 v240, v[20:23] offset:5120\n\t"
        "ds_write_b128 v240, v[24:27] offset:6144\n\t"
        "ds_write_b128 v240, v[28:31] offset:7168\n\t"
        "v_exp_f32 v88, v88\n\t"
        "v_exp_f32 v89, v89\n\t"
        "v_exp_f32 v90, v90\n\t"
        "v_exp_f32 v91, v91\n\t"
        "v_cvt_pk_bf16_f32 v154, v84, v85\n\t"
        "v_cvt_pk_bf16_f32 v155, v86, v87\n\t"
        "v_mfma_f32_32x32x16_bf16 v[32:47], v[220:223], v[148:151], v[32:47]\n\t"
        "v_exp_f32 v92, v92\n\t"
        "v_exp_f32 v93, v93\n\t"
        "v_exp_f32 v94, v94\n\t"
        "v_exp_f32 v95, v95\n\t"
        "v_cvt_pk_bf16_f32 v156, v88, v89\n\t"
        "v_cvt_pk_bf16_f32 v157, v90, v91\n\t"
        "v_cvt_pk_bf16_f32 v158, v92, v93\n\t"
        "v_cvt_pk_bf16_f32 v159, v94, v95\n\t"
        "s_nop 1\n\t"
        "v_mfma_f32_32x32x16_bf16 v[48:63], v[216:219], v[152:155], v[48:63]\n\t"
        "v_mfma_f32_32x32x16_bf16 v[48:63], v[220:223], v[156:159], v[48:63]\n\t"
        "s_nop 3\n\t"
        "ds_write_b128 v240, v[32:35] offset:8192\n\t"
        "ds_write_b128 v240, v[36:39] offset:9216\n\t"
        "ds_write_b128 v240, v[40:43] offset:10240\n\t"
        "ds_write_b128 v240, v[44:47] offset:11264\n\t"
        "s_waitcnt vmcnt(0) lgkmcnt(0)\n\t"
        "s_barrier\n\t"
        "s_nop 7\n\t"
        : "={v[48:63]}"(acc), [sb] "+s"(sb), [cnt] "+s"(cnt), "+{v236}"(q2a), "+{v237}"(kba), "+{v238}"(ordn), "={v[136:139]}"(qo0), "={v[140:143]}"(qo1)
        : "{v224}"(vok + 0 * TS), "{v225}"(vov + 0 * TS), "{v226}"(vov + 2 * VROW + 0 * TS), "{v227}"(vok + 1 * TS), "{v228}"(vov + 1 * TS), "{v229}"(vov + 2 * VROW + 1 * TS), "{v230}"(vok + 2 * TS), "{v231}"(vov + 2 * TS), "{v232}"(vov + 2 * VROW + 2 * TS), "{v233}"(vok + 3 * TS), "{v234}"(vov + 3 * TS), "{v235}"(vov + 2 * VROW + 3 * TS),
          "{v240}"(puta), "{v241}"(qxa[0]), "{v242}"(qxa[1]), "{v243}"(qxa[2]), "{v244}"(qxa[3]), [rs] "s"(rsKVr)
        : "v0", "v1", "v2", "v3", "v4", "v5", "v6", "v7", "v8", "v9", "v10", "v11", "v12", "v13", "v14", "v15", "v16", "v17", "v18", "v19", "v20", "v21", "v22", "v23", "v24", "v25", "v26", "v27", "v28", "v29", "v30", "v31", "v32", "v33", "v34", "v35", "v36", "v37", "v38", "v39", "v40", "v41", "v42", "v43", "v44", "v45", "v46", "v47", "v64", "v65", "v66", "v67", "v68", "v69", "v70", "v71", "v72", "v73", "v74", "v75", "v76", "v77", "v78", "v79", "v80", "v81", "v82", "v83", "v84", "v85", "v86", "v87", "v88", "v89", "v90", "v91", "v92", "v93", "v94", "v95", "v96", "v97", "v98", "v99", "v100", "v101", "v102", "v103", "v104", "v105", "v106", "v107", "v108", "v109", "v110", "v111", "v112", "v113", "v114", "v115", "v116", "v117", "v118", "v119", "v120", "v121", "v122", "v123", "v124", "v125", "v126", "v127", "v128", "v129", "v130", "v131", "v132", "v133", "v134", "v135", "v144", "v145", "v146", "v147", "v148", "v149", "v150", "v151", "v152", "v153", "v154", "v155", "v156", "v157", "v158", "v159", "v160", "v161", "v162", "v163", "v164", "v165", "v166", "v167", "v168", "v169", "v170", "v171", "v172", "v173", "v174", "v175", "v176", "v177", "v178", "v179", "v180", "v181", "v182", "v183", "v184", "v185", "v186", "v187", "v188", "v189", "v190", "v191", "v192", "v193", "v194", "v195", "v196", "v197", "v198", "v199", "v200", "v201", "v202", "v203", "v204", "v205", "v206", "v207", "v208", "v209", "v210", "v211", "v212", "v213", "v214", "v215", "v216", "v217", "v218", "v219", "v220", "v221", "v222", "v223", "v239", "scc", "memory");
    }
#pragma unroll
    for (int s = 0; s < 3; ++s) {
        LAS const char* sp = lds + L_CB + (((w - 1 - s) & 3) * 3 + s) * 4096 + lane * 16;
#pragma unroll
        for (int g = 0; g < 4; ++g) {
            const f32x4 v = *(LAS const f32x4*)(sp + g * 1024);
            acc[4 * g] += v[0]; acc[4 * g + 1] += v[1]; acc[4 * g + 2] += v[2]; acc[4 * g + 3] += v[3];
        }
    }
    float l = half_hi(acc[12]);
    if (__builtin_expect(__any(!(l >= L_MIN && l <= L_MAX)), 0)) {
        const f32x16 zero = {};
        float m = -__builtin_inff();
#pragma unroll 1
        for (int t = 0; t < 64; ++t) {
            f32x16 s = MFMA(asbf(ld16_wt(rsKVr, vok, t * TS)), asbf(qo0), zero);
            s = MFMA(asbf(ld16_wt(rsKVr, vok + 1024, t * TS)), asbf(qo1), s);
#pragma unroll
            for (int i = 0; i < 16; ++i) m = __builtin_fmaxf(m, s[i]);
        }
        m = __builtin_fmaxf(half_lo(m), half_hi(m));
        acc = zero;
#pragma unroll 1
        for (int t = 0; t < 64; ++t) {
            f32x16 s = MFMA(asbf(ld16_wt(rsKVr, vok, t * TS)), asbf(qo0), zero);
            s = MFMA(asbf(ld16_wt(rsKVr, vok + 1024, t * TS)), asbf(qo1), s);
#pragma unroll
            for (int i = 0; i < 16; ++i) s[i] = __builtin_amdgcn_exp2f(s[i] - m);
            acc = MFMA(asbf(ld16_wt(rsKVr, vov, t * TS)), asbf(pack8(s, 0)), acc);
            acc = MFMA(asbf(ld16_wt(rsKVr, vov + 2 * VROW, t * TS)), asbf(pack8(s, 1)), acc);
        }
        l = half_hi(acc[12]);
    }
    {
        const float rl = __builtin_amdgcn_rcpf(l);
#pragma unroll
        for (int i = 0; i < 16; ++i) acc[i] = (i >= 12 && h) ? 0.f : acc[i] * rl;
        const unsigned so = (kt * NH + hd) * 2048;
        u32x4 p0 = pack8(acc, 0), p1 = pack8(acc, 1);
#pragma unroll
        for (int i = 0; i < 4; ++i) { p0[i] = min(p0[i], POISON - 1); p1[i] = min(p1[i], POISON - 1); }
        st16_wt(p0, rsOI, lane * 16, so);
        st16_wt(p1, rsOI, lane * 16 + 1024, so);
    }
    BAR();

    bf16x8 wf[32];
    LAS const char* ob = lds + L_CB + lane * 16;
    GAS char* orow = (GAS char*)out + (size_t)(q32 * 32 + r) * (DM * 2) + cg * 256 + w * 64 + h * 16;
    asm volatile("" : "+v"(ob), "+v"(orow));
    {
        LAS const char* wb = lds + L_WO + (4 * h + q4) * 256 + ((w ^ q4) * 64) + g16 * 32 + p4 * 8;
#pragma unroll
        for (int k = 0; k < 32; ++k) {
            const int ro = ((k >> 1) * DH + (k & 1) * 16) * 256;
            wf[k] = cat(vtr(wb + ro), vtr(wb + ro + 2048 - (k == 31 ? h * 1024 : 0)));
        }
    }
    asm volatile("s_waitcnt lgkmcnt(0)\n\ts_barrier\nMHA_CODE_END:" ::: "memory");
    if (cg < 3 || w < 2) {
        f32x16 c = {};
        bf16x8 of[3][4];
#define RDO(i) do { _Pragma("unroll") for (int j = 0; j < 4; ++j) of[(i) % 3][j] = *(LAS const bf16x8*)(ob + ((i) * 4 + j) * 1024); } while (0)
        RDO(0); RDO(1); SBAR();
#pragma unroll
        for (int i = 0; i < 8; ++i) {
            if (i + 2 < 8) RDO(i + 2);
            SBAR();
#pragma unroll
            for (int j = 0; j < 4; ++j) c = MFMA(wf[i * 4 + j], of[i % 3][j], c);
            SBAR();
        }
#undef RDO
#pragma unroll
        for (int g = 0; g < 4; g += 2) {
            const unsigned x0 = cvtpk(c[4 * g], c[4 * g + 1]), x1 = cvtpk(c[4 * g + 2], c[4 * g + 3]);
            const unsigned y0 = cvtpk(c[4 * g + 4], c[4 * g + 5]), y1 = cvtpk(c[4 * g + 6], c[4 * g + 7]);
            auto s0 = __builtin_amdgcn_permlane32_swap(x0, y0, false, false);
            auto s1 = __builtin_amdgcn_permlane32_swap(x1, y1, false, false);
            *(GAS u32x4*)(orow + g * 16) = (u32x4){s0[0], s1[0], s0[1], s1[1]};
        }
    }
}

extern "C" void kernel_launch(void* const* d_in, const int* in_sizes, int n_in, void* d_out, int out_size, void* d_ws, size_t ws_size,
                              hipStream_t stream) {
    (void)hipFuncSetAttribute((const void*)mha_kernel, hipFuncAttributeMaxDynamicSharedMemorySize, LDS_BYTES);
    mha_kernel<<<256, 512, LDS_BYTES, stream>>>((const char*)d_in[0], (const char*)d_in[1], (const char*)d_in[2], (char*)d_out, (char*)d_ws);
}
```

```cpp
#include <hip/hip_runtime.h>
#include <hip/hip_bf16.h>
#include <stdint.h>

typedef short bf16x8 __attribute__((ext_vector_type(8)));
typedef short s16x4 __attribute__((ext_vector_type(4)));
typedef float f32x16 __attribute__((ext_vector_type(16)));
typedef float f32x4 __attribute__((ext_vector_type(4)));
typedef float f32x2 __attribute__((ext_vector_type(2)));
typedef unsigned u32x4 __attribute__((ext_vector_type(4)));
typedef __bf16 bf16x2_t __attribute__((ext_vector_type(2)));
typedef short v4i16_t __attribute__((ext_vector_type(4)));
typedef unsigned long long u64;
#define LAS __attribute__((address_space(3)))
#define GAS __attribute__((address_space(1)))
#define DI __device__ __forceinline__
#define MFMA(a, b, c) __builtin_amdgcn_mfma_f32_32x32x16_bf16((a), (b), (c), 0, 0, 0)
#define SBAR() __builtin_amdgcn_sched_barrier(0)
#define RLX_AGENT __ATOMIC_RELAXED, __HIP_MEMORY_SCOPE_AGENT
#define BAR() asm volatile("s_waitcnt lgkmcnt(0)\n\ts_barrier" ::: "memory")

#define MAP_HD(b) (((b) & 7) * 2 + (((b) >> 3) & 1))
#define MAP_QT(b) ((b) >> 4)
constexpr int SEQ = 2048, DM = 448, NH = 16, DH = 28, N3 = 1344;
constexpr float QSCALE = 0.18898223650461362f * 1.4426950408889634f;
constexpr float L_MIN = 8.673617379884035e-19f, L_MAX = 1.152921504606846976e18f;
constexpr unsigned VROW = 456 * 16, TS = 4 * VROW;
constexpr unsigned WS_K = 0, WS_V = 2u << 20, WS_O = 4u << 20, WS_F = 6u << 20;
constexpr unsigned POISON = 0xffffffffu;
constexpr unsigned F_G = 0, F_R = 4096, F_P = 8192, F_KV = 131072;
constexpr unsigned L_WQ = 0, L_RING = 28672, STG = 24576;
constexpr unsigned L_QX = 131072, L_Q2 = L_QX + 8192, L_GB = 152064;
constexpr unsigned L_WO = 0, L_CB = 114688;
constexpr unsigned LDS_BYTES = 163840;


DI unsigned rfl(unsigned x) { return __builtin_amdgcn_readfirstlane(x); }
DI unsigned cvtpk(float lo, float hi) { f32x2 v = {lo, hi}; bf16x2_t b = __builtin_convertvector(v, bf16x2_t); return __builtin_bit_cast(unsigned, b); }
DI u32x4 pack8(const f32x16& x, int s) {
    return (u32x4){cvtpk(x[8 * s], x[8 * s + 1]), cvtpk(x[8 * s + 2], x[8 * s + 3]), cvtpk(x[8 * s + 4], x[8 * s + 5]), cvtpk(x[8 * s + 6], x[8 * s + 7])};
}
DI bf16x8 asbf(u32x4 x) { return __builtin_bit_cast(bf16x8, x); }
DI s16x4 vtr(LAS const char* p) { return __builtin_bit_cast(s16x4, __builtin_amdgcn_ds_read_tr16_b64_v4i16((LAS v4i16_t*)p)); }
DI bf16x8 cat(s16x4 lo, s16x4 hi) { return __builtin_shufflevector(lo, hi, 0, 1, 2, 3, 4, 5, 6, 7); }
DI float half_lo(float x) { auto r = __builtin_amdgcn_permlane32_swap(__float_as_uint(x), __float_as_uint(x), false, false); return __uint_as_float(r[0]); }
DI float half_hi(float x) { auto r = __builtin_amdgcn_permlane32_swap(__float_as_uint(x), __float_as_uint(x), false, false); return __uint_as_float(r[1]); }
DI void dma16(unsigned lds_dst, unsigned voff, __amdgpu_buffer_rsrc_t rs, unsigned soff) {
    asm volatile("s_mov_b32 m0, %0\n\ts_nop 0\n\tbuffer_load_dwordx4 %1, %2, %3 offen lds" ::"s"(lds_dst), "v"(voff), "s"(rs), "s"(soff) : "m0", "memory");
}
DI void st16_wt(u32x4 v, __amdgpu_buffer_rsrc_t rs, unsigned voff, unsigned soff) { __builtin_amdgcn_raw_buffer_store_b128(v, rs, voff, soff, 16); }
DI u32x4 ld16_wt(__amdgpu_buffer_rsrc_t rs, unsigned voff, unsigned soff) { return __builtin_amdgcn_raw_buffer_load_b128(rs, voff, soff, 16); }
DI __amdgpu_buffer_rsrc_t mkrs(const void* p, unsigned bytes) { return __builtin_amdgcn_make_buffer_rsrc((void*)p, (short)0, (int)bytes, 0x00020000); }
DI unsigned poll_tags(const GAS u64* g, unsigned tag) {
    u64 v = __hip_atomic_load(g, RLX_AGENT);
#pragma nounroll
    for (unsigned spins = 0; spins < (1u << 16); ++spins) {
        if (__all((unsigned)v == tag)) break;
        if ((unsigned)v != tag) v = __hip_atomic_load(g, RLX_AGENT);
    }
    __builtin_amdgcn_fence(__ATOMIC_ACQUIRE, "wavefront");
    return (unsigned)(v >> 32);
}
DI unsigned touch(const GAS char* p) { unsigned t; asm volatile("global_load_dword %0, %1, off" : "=v"(t) : "v"(p) : "memory"); return t; }

template <bool ISK>
DI void kv_gemm(LAS char* lds, unsigned w, int lane, bf16x8 (&xf)[7][4], f32x16& a0, f32x16& a1) {
    const int r = lane & 31, h = lane >> 5, i16 = lane & 15, q4 = i16 >> 2, p4 = i16 & 3, g16 = (lane >> 4) & 1;
    const int pc = ISK ? (((p4 & 1) << 1) | (p4 >> 1)) : p4;
    LAS const char* xb = lds + L_RING + w * 4096 + r * 128;
    LAS const char* wb0 = lds + L_RING + 16384 + (8 * h + q4) * 128 + ((q4 >> 1) * 64) + g16 * 32 + pc * 8;
    LAS const char* wb1 = lds + L_RING + 16384 + (8 * h + q4) * 128 + (((q4 >> 1) ^ 1) * 64) + g16 * 32 + pc * 8;
    const int xs = (r >> 1) & 7;
    bf16x8 wf[2][4][2];
#define RD(i) do { const int so = ((i) % 5) * STG; _Pragma("unroll") for (int ks = 0; ks < 4; ++ks) {                          \
        xf[i][ks] = *(LAS const bf16x8*)(xb + so + (((2 * ks + h) ^ xs) * 16));                                                \
        wf[(i) & 1][ks][0] = cat(vtr(wb0 + so + ks * 2048), vtr(wb0 + so + ks * 2048 + 512));                                  \
        wf[(i) & 1][ks][1] = cat(vtr(wb1 + so + ks * 2048), vtr(wb1 + so + ks * 2048 + 512)); } } while (0)
#define MM(i) do { _Pragma("unroll") for (int ks = 0; ks < 4; ++ks) {                                                          \
        if (ISK) { a0 = MFMA(wf[(i) & 1][ks][0], xf[i][ks], a0); a1 = MFMA(wf[(i) & 1][ks][1], xf[i][ks], a1); }               \
        else     { a0 = MFMA(xf[i][ks], wf[(i) & 1][ks][0], a0); a1 = MFMA(xf[i][ks], wf[(i) & 1][ks][1], a1); } } } while (0)
    BAR(); RD(0);
#pragma unroll
    for (int i = 0; i < 5; ++i) { BAR(); RD(i + 1); SBAR(); MM(i); SBAR(); }
    MM(5); SBAR();
    BAR(); RD(6); SBAR();
#pragma unroll
    for (int ks = 0; ks < 4; ++ks) a0 = ISK ? MFMA(wf[0][ks][0], xf[6][ks], a0) : MFMA(xf[6][ks], wf[0][ks][0], a0);
    SBAR();
#pragma unroll
    for (int ks = 0; ks < 4; ++ks) a1 = ISK ? MFMA(wf[0][ks][1], xf[6][ks], a1) : MFMA(xf[6][ks], wf[0][ks][1], a1);
    SBAR();
#undef RD
#undef MM
}

DI void stage_dma(unsigned mw, unsigned mx, unsigned sw, unsigned sx, unsigned wvo, unsigned xvo0, unsigned xvo1, __amdgpu_buffer_rsrc_t rsW,
                  __amdgpu_buffer_rsrc_t rsX) {
    unsigned t;
    asm volatile("s_mov_b32 m0, %1\n\ts_add_u32 %0, %3, 21504\n\tbuffer_load_dwordx4 %5, %8, %3 offen lds\n\t"
                 "s_add_u32 m0, m0, 1024\n\ts_nop 0\n\tbuffer_load_dwordx4 %5, %8, %0 offen lds\n\t"
                 "s_mov_b32 m0, %2\n\ts_add_u32 %0, %4, 7168\n\tbuffer_load_dwordx4 %6, %9, %4 offen lds\n\t"
                 "s_add_u32 m0, m0, 1024\n\ts_nop 0\n\tbuffer_load_dwordx4 %7, %9, %0 offen lds\n\t"
                 "s_add_u32 m0, m0, 1024\n\ts_add_u32 %0, %4, 14336\n\tbuffer_load_dwordx4 %6, %9, %0 offen lds\n\t"
                 "s_add_u32 m0, m0, 1024\n\ts_add_u32 %0, %4, 21504\n\tbuffer_load_dwordx4 %7, %9, %0 offen lds"
                 : "=&s"(t) : "s"(mw), "s"(mx), "s"(sw), "s"(sx), "v"(wvo), "v"(xvo0), "v"(xvo1), "s"(rsW), "s"(rsX) : "m0", "scc", "memory");
}

__global__ __launch_bounds__(512, 2) void mha_kernel(const char* __restrict__ x, const char* __restrict__ wqkv, const char* __restrict__ wo,
                                                     char* __restrict__ out, char* __restrict__ ws) {
    extern __shared__ __attribute__((aligned(16))) char lds_[];
    LAS char* lds = (LAS char*)lds_;
    const unsigned wv = rfl(threadIdx.x >> 6), w = wv & 3;
    const unsigned lds0 = (unsigned)(uintptr_t)lds_;
    GAS char* wsg = (GAS char*)ws;
#define DECODE()                                                                                                              \
    unsigned b = blockIdx.x, lane = threadIdx.x & 63;                                                                        \
    asm volatile("" : "+s"(b)); asm volatile("" : "+v"(lane));                                                                \
    const unsigned hd = MAP_HD(b), qt = MAP_QT(b);                                                                            \
    const unsigned ch = hd < 14 ? hd : hd - 2;         \
    const unsigned rot = (hd + qt) % 7;
#define DECODE2()                                                                                                             \
    const bool isK = ch < 7;                                                                                                  \
    const unsigned g0 = (7 * hd) >> 1;                                                  \
    const unsigned cg = (b >> 3) & 3, q32 = (b & 7) * 8 + (b >> 5);     \
    unsigned kbi[7];                                                                                                          \
    _Pragma("unroll") for (int i = 0; i < 7; ++i) { const unsigned k = rot + i; kbi[i] = k >= 7 ? k - 7 : k; }               \
    const __amdgpu_buffer_rsrc_t rsOI = mkrs(ws + WS_O, 2u << 20);

    if (__builtin_expect(wv >= 4, 1)) {
        DECODE();
        const unsigned xrow = qt * 128 + w * 32 + (lane >> 3);
        const unsigned xvo0 = xrow * (DM * 2) + (((lane & 7) ^ (lane >> 4)) * 16), xvo1 = xvo0 ^ 64;
        const unsigned wvo = (16 * w + (lane >> 3)) * (N3 * 2) + 896 + ch * 128 + (((lane & 7) ^ (((lane >> 4) & 1) << 2)) * 16);
        const __amdgpu_buffer_rsrc_t rsW = mkrs(wqkv, DM * N3 * 2), rsX = mkrs(x, SEQ * DM * 2);
        stage_dma(lds0 + L_RING + 16384 + w * 2048, lds0 + L_RING + w * 4096, rot * (64 * N3 * 2), rot * 128, wvo, xvo0, xvo1, rsW, rsX);
        DECODE2();
        const __amdgpu_buffer_rsrc_t rsF = mkrs(ws + WS_F, 4096);
#define STAGE(i) stage_dma(lds0 + L_RING + ((i) % 5) * STG + 16384 + w * 2048, lds0 + L_RING + ((i) % 5) * STG + w * 4096, kbi[i] * (64 * N3 * 2), kbi[i] * 128, \
                           wvo, xvo0, xvo1, rsW, rsX)
#define VBAR(NV) asm volatile("s_waitcnt vmcnt(" #NV ")\n\ts_barrier" ::: "memory")
        STAGE(1); STAGE(2);
        VBAR(12); STAGE(3);
        VBAR(12); STAGE(4);
        VBAR(12); STAGE(5);
        VBAR(12); STAGE(6);
        VBAR(12);
        unsigned gen;
        { const unsigned vo = F_G; asm volatile("buffer_load_dword %0, %1, %2, 0 offen sc1" : "=v"(gen) : "v"(vo), "s"(rsF) : "memory"); }
        {
            const unsigned qvo = (16 * w + (lane >> 2)) * (N3 * 2) + g0 * 16 + (lane & 3) * 16;
#pragma unroll
            for (int j = 0; j < 7; ++j) dma16(lds0 + L_WQ + (16 * w + 64 * kbi[j]) * 64, qvo, rsW, kbi[j] * 64 * (N3 * 2));
        }
        VBAR(14); VBAR(8);
        asm volatile("s_waitcnt vmcnt(7)" : "+v"(gen)::"memory");
        unsigned tag = rfl(gen);
        { const unsigned t = (tag ^ (tag >> 15)) * 0x2c1b3c6du + 0x9e3779b9u; tag = (t == tag) ? t + 1 : t; }
        const unsigned hh = lane & 15, wh = (lane >> 4) & 1;
        const unsigned c = isK ? (((7 * hh >> 1) + 3 * wh) >> 3) : 7 + ((28 * hh + 27 * wh) >> 6);
        GAS u64* fp = (GAS u64*)(wsg + WS_F + (lane < 32 ? F_KV + ((hh * 4 + (isK ? 0 : 2) + wh) * 16 + qt) * 128 : F_R + (qt * 16 + hd) * 8));
        const bool fon = w == 0 && ((lane < 32 && c == hd) || lane == 32);
        asm volatile("s_waitcnt lgkmcnt(0)\n\ts_barrier" : "+v"(fp)::"memory");
        if (w == 0) {
            if (fon) __hip_atomic_store(fp, ((u64)*(LAS const unsigned*)(lds + L_GB) << 32) | tag, RLX_AGENT);
            asm volatile("s_waitcnt vmcnt(1)" ::: "memory");
        } else {
            asm volatile("s_waitcnt vmcnt(0)" ::: "memory");
        }
        asm volatile("s_waitcnt lgkmcnt(0)\n\ts_barrier" ::: "memory");
        LAS unsigned* ord = (LAS unsigned*)(lds + L_CB + w * 12288 + (w == 2 ? 2048 : 0));
        if (lane < 5) ord[lane] = lane == 4 ? 0x40000000u : 0xffffffffu;
        {
            const unsigned tt = w + 4 * ((((lane >> 2) & 3) + (qt >> 2)) & 3);
            const GAS u64* fl = (const GAS u64*)(wsg + WS_F + F_KV + hd * 8192 + ((lane & 3) * 16 + tt) * 128);
            u64 v = __hip_atomic_load(fl, RLX_AGENT);
            unsigned done = 0;
#pragma nounroll
            for (unsigned spins = 0; done < 4; ++spins) {
                const unsigned miss = spins < (1u << 16) ? (unsigned)__ballot((unsigned)v != tag) & 0xffffu : 0u;
                const unsigned n = miss ? __builtin_ctz(miss) >> 2 : 4;
                if (n > done) {
                    __builtin_amdgcn_fence(__ATOMIC_ACQUIRE, "wavefront");
                    if (lane < 4 * n && (lane & 3) == 0) ord[lane >> 2] = tt * (4 * TS);
                    if (done == 0) {
                        const unsigned g = (unsigned)(v >> 32);
                        if (w == (qt & 3) && lane == 0) *(LAS unsigned*)(lds + L_GB + 16) = max(g, (unsigned)__builtin_amdgcn_readlane((int)g, 1));
                        BAR();
                        asm volatile("s_barrier" ::: "memory");
                    }
                    done = n;
                }
                if ((unsigned)v != tag) v = __hip_atomic_load(fl, RLX_AGENT);
            }
        }
        st16_wt((u32x4){POISON, POISON, POISON, POISON}, rsOI, lane * 16, ((qt * 4 + w) * NH + hd) * 2048);
        st16_wt((u32x4){POISON, POISON, POISON, POISON}, rsOI, lane * 16 + 1024, ((qt * 4 + w) * NH + hd) * 2048);
        asm volatile("s_waitcnt vmcnt(0)" ::: "memory");
        if (lane == 0) __hip_atomic_store((GAS u64*)(wsg + WS_F + F_P + ((qt * 4 + w) * 16 + hd) * 8), (u64)tag, RLX_AGENT);
        if (w == 1 && b == 0) {
            const GAS u64* fr = (const GAS u64*)(wsg + WS_F + F_R) + lane;
#pragma nounroll
            for (unsigned spins = 0; spins < (1u << 16); ++spins) {
                const u64 v0 = __hip_atomic_load(fr, RLX_AGENT), v1 = __hip_atomic_load(fr + 64, RLX_AGENT);
                const u64 v2 = __hip_atomic_load(fr + 128, RLX_AGENT), v3 = __hip_atomic_load(fr + 192, RLX_AGENT);
                if (__all((unsigned)v0 == tag && (unsigned)v1 == tag && (unsigned)v2 == tag && (unsigned)v3 == tag)) break;
                __builtin_amdgcn_s_sleep(4);
            }
            if (lane == 0) __hip_atomic_store((GAS unsigned*)(wsg + WS_F + F_G), tag, RLX_AGENT);
        }
        {
            const __amdgpu_buffer_rsrc_t rsO = mkrs(wo, DM * DM * 2);
            const unsigned vow = (4 * w + (lane >> 4)) * (DM * 2) + cg * 256 + ((((lane >> 2) & 3) ^ (lane >> 4)) * 64) + (lane & 3) * 16;
#pragma unroll 1
            for (unsigned j = 0; j < 28; ++j) { dma16(lds0 + L_WO + (4 * j + w) * 1024, vow, rsO, j * 14336); __builtin_amdgcn_s_sleep(1); }
        }
        (void)poll_tags((const GAS u64*)(wsg + WS_F + F_P + q32 * 128) + w * 4 + (lane & 3), tag);
        VBAR(0);
        asm volatile("s_barrier" ::: "memory");
        {
            unsigned pend = 15;
#pragma nounroll
            for (unsigned spins = 0; pend && spins < (1u << 16); ++spins) {
                u32x4 t[8];
                asm volatile("" ::: "memory");
#pragma unroll
                for (int j = 0; j < 4; ++j)
                    if (pend & (1u << j)) {
                        t[2 * j] = ld16_wt(rsOI, lane * 16 + j * 2048, q32 * (NH * 2048) + w * 8192);
                        t[2 * j + 1] = ld16_wt(rsOI, lane * 16 + j * 2048 + 1024, q32 * (NH * 2048) + w * 8192);
                    }
#pragma unroll
                for (int j = 0; j < 4; ++j)
                    if (pend & (1u << j)) {
                        const u32x4 u = t[2 * j], v = t[2 * j + 1];
                        *(LAS u32x4*)(lds + L_CB + (w * 8 + 2 * j) * 1024 + lane * 16) = u;
                        *(LAS u32x4*)(lds + L_CB + (w * 8 + 2 * j + 1) * 1024 + lane * 16) = v;
                        const unsigned m = max(max(max(u[0], u[1]), max(u[2], u[3])), max(max(v[0], v[1]), max(v[2], v[3])));
                        if (__all(m != POISON)) pend &= ~(1u << j);
                    }
            }
        }
        BAR();
        return;
    }

    DECODE(); DECODE2();
    const int r = lane & 31, h = lane >> 5, i16 = lane & 15, q4 = i16 >> 2, p4 = i16 & 3, g16 = (lane >> 4) & 1;
    f32x16 a0 = {}, a1 = {};
    bf16x8 xf[7][4];
    const unsigned kt = qt * 4 + w;
    unsigned tch1;
    {
        unsigned long long pc0;
        unsigned len;
        asm volatile("s_getpc_b64 %0\nMHA_PF%=:\n\ts_mov_b32 %1, MHA_CODE_END - MHA_PF%=" : "=s"(pc0), "=s"(len));
        tch1 = touch((const GAS char*)(pc0 & ~3ull) + min((((b >> 3) & 3) * 4 + w) * 1024 + (lane & 7) * 128, len));
    }
    if (threadIdx.x == 0) *(LAS unsigned*)(lds + L_GB) = 0;
    unsigned vok = WS_K + (g0 + h) * 512 + r * 16, vov = r <= DH ? WS_V + h * VROW + (r < DH ? DH * hd + r : 448) * 16 : 0x70000000u;
    const __amdgpu_buffer_rsrc_t rsKVr = mkrs(ws, 4u << 20);
    unsigned q2a = lds0 + L_Q2 + r * 16, kba = lds0 + L_GB + 16, ordn = lds0 + L_CB + w * 12288 + (w == 2 ? 2048 : 0) + 4, puta = lds0 + L_CB + w * 12288 + lane * 16;
    unsigned qxa[4];
#pragma unroll
    for (int i = 0; i < 4; ++i) qxa[i] = lds0 + L_QX + ((w + 1 + i) & 3) * 2048 + lane * 16;
    LAS const char* qb = lds + L_WQ + (8 * h + q4) * 64 + g16 * 32 + (((p4 & 1) << 1) | (p4 >> 1)) * 8;
    asm volatile("" : "+v"(vok), "+v"(vov), "+v"(q2a), "+v"(kba), "+v"(ordn), "+v"(puta), "+v"(qxa[0]), "+v"(qxa[1]), "+v"(qxa[2]), "+v"(qxa[3]), "+v"(qb));
    const __amdgpu_buffer_rsrc_t rsKV = mkrs(ws, hd < 14 ? 4u << 20 : 0u);
    if (isK) {
        unsigned vo = WS_K + (8 * ch + h) * 512 + r * 16;
        asm volatile("" : "+v"(vo));
        kv_gemm<true>(lds, w, lane, xf, a0, a1);
        st16_wt(pack8(a0, 0), rsKV, vo, kt * TS); st16_wt(pack8(a0, 1), rsKV, vo + 1024, kt * TS);
        st16_wt(pack8(a1, 0), rsKV, vo + 2048, kt * TS); st16_wt(pack8(a1, 1), rsKV, vo + 3072, kt * TS);
        SBAR();
        float s0 = 0.f, s1 = 0.f, s2 = 0.f, s3 = 0.f;
#pragma unroll
        for (int j = 0; j < 8; ++j) { s0 += a0[j] * a0[j]; s1 += a0[8 + j] * a0[8 + j]; s2 += a1[j] * a1[j]; s3 += a1[8 + j] * a1[8 + j]; }
        asm volatile("ds_max_u32 %0, %1" ::"v"(lds0 + L_GB), "v"(max(max(__float_as_uint(s0), __float_as_uint(s1)), max(__float_as_uint(s2), __float_as_uint(s3)))) : "memory");
    } else {
        unsigned v0 = WS_V + h * VROW + (64 * (ch - 7) + r) * 16, v2 = v0 + 2 * VROW;
        if (lane < 32)
            st16_wt((u32x4){0x3f803f80u, 0x3f803f80u, 0x3f803f80u, 0x3f803f80u}, rsKV, WS_V + (lane >> 3) * VROW + (448 + (lane & 7)) * 16, kt * TS);
        asm volatile("" : "+v"(v0), "+v"(v2));
        kv_gemm<false>(lds, w, lane, xf, a0, a1);
        st16_wt(pack8(a0, 0), rsKV, v0, kt * TS); st16_wt(pack8(a0, 1), rsKV, v2, kt * TS);
        st16_wt(pack8(a1, 0), rsKV, v0 + 512, kt * TS); st16_wt(pack8(a1, 1), rsKV, v2 + 512, kt * TS);
        SBAR();
    }
    asm volatile("s_waitcnt vmcnt(0) lgkmcnt(0)" : "+v"(tch1)::"memory");
    asm volatile("s_barrier" ::: "memory");
    asm volatile("s_barrier" ::: "memory");
    f32x16 aq = {};
    {
        bf16x8 wq[3][4];
#define RDQ(i) do { LAS const char* qk = qb + kbi[i] * 4096; _Pragma("unroll") for (int ks = 0; ks < 4; ++ks)                  \
            wq[(i) % 3][ks] = cat(vtr(qk + ks * 1024), vtr(qk + ks * 1024 + 256)); } while (0)
        RDQ(0); RDQ(1); SBAR();
#pragma unroll
        for (int i = 0; i < 7; ++i) {
            if (i + 2 < 7) RDQ(i + 2);
            SBAR();
#pragma unroll
            for (int ks = 0; ks < 4; ++ks) aq = MFMA(wq[i % 3][ks], xf[i][ks], aq);
            SBAR();
        }
#undef RDQ
    }
    {
        const bool zlo = (hd & 1) && !h, zhi = !(hd & 1) && h;
        float qs[4] = {0.f, 0.f, 0.f, 0.f};
#pragma unroll
        for (int i = 0; i < 16; ++i) {
            aq[i] *= QSCALE;
            if (i < 4) aq[i] = zlo ? 0.f : aq[i];
            if (i >= 12) aq[i] = zhi ? 0.f : aq[i];
            qs[i & 3] += aq[i] * aq[i];
        }
        *(LAS u32x4*)(lds + (qxa[3] - lds0)) = pack8(aq, 0);
        *(LAS u32x4*)(lds + (qxa[3] - lds0) + 1024) = pack8(aq, 1);
        float q2 = (qs[0] + qs[1]) + (qs[2] + qs[3]);
        q2 = half_lo(q2) + half_hi(q2);
        *(LAS float*)(lds + (q2a - lds0) + w * 4) = q2;
    }

    f32x16 acc;
    u32x4 qo0, qo1;
    {
        unsigned sb = (w + (qt & 12)) * (4 * TS), cnt = 4;
        asm volatile(
        "s_setprio 2\n\t"
        "v_mov_b64 v[0:1], 0\n\t"
        "v_mov_b64 v[2:3], 0\n\t"
        "v_mov_b64 v[4:5], 0\n\t"
        "v_mov_b64 v[6:7], 0\n\t"
        "v_mov_b64 v[8:9], 0\n\t"
        "v_mov_b64 v[10:11], 0\n\t"
        "v_mov_b64 v[12:13], 0\n\t"
        "v_mov_b64 v[14:15], 0\n\t"
        "v_mov_b64 v[16:17], 0\n\t"
        "v_mov_b64 v[18:19], 0\n\t"
        "v_mov_b64 v[20:21], 0\n\t"
        "v_mov_b64 v[22:23], 0\n\t"
        "v_mov_b64 v[24:25], 0\n\t"
        "v_mov_b64 v[26:27], 0\n\t"
        "v_mov_b64 v[28:29], 0\n\t"
        "v_mov_b64 v[30:31], 0\n\t"
        "v_mov_b64 v[32:33], 0\n\t"
        "v_mov_b64 v[34:35], 0\n\t"
        "v_mov_b64 v[36:37], 0\n\t"
        "v_mov_b64 v[38:39], 0\n\t"
        "v_mov_b64 v[40:41], 0\n\t"
        "v_mov_b64 v[42:43], 0\n\t"
        "v_mov_b64 v[44:45], 0\n\t"
        "v_mov_b64 v[46:47], 0\n\t"
        "v_mov_b64 v[48:49], 0\n\t"
        "v_mov_b64 v[50:51], 0\n\t"
        "v_mov_b64 v[52:53], 0\n\t"
        "v_mov_b64 v[54:55], 0\n\t"
        "v_mov_b64 v[56:57], 0\n\t"
        "v_mov_b64 v[58:59], 0\n\t"
        "v_mov_b64 v[60:61], 0\n\t"
        "v_mov_b64 v[62:63], 0\n\t"
        "v_mov_b64 v[152:153], 0\n\t"
        "v_mov_b64 v[154:155], 0\n\t"
        "v_mov_b64 v[156:157], 0\n\t"
        "v_mov_b64 v[158:159], 0\n\t"
        "v_mov_b64 v[216:217], 0\n\t"
        "v_mov_b64 v[218:219], 0\n\t"
        "v_mov_b64 v[220:221], 0\n\t"
        "v_mov_b64 v[222:223], 0\n\t"
        "s_waitcnt lgkmcnt(0)\n\t"
        "s_barrier\n\t"
        "buffer_load_dwordx4 v[160:163], v224, %[rs], %[sb] offen sc1\n\t"
        "buffer_load_dwordx4 v[164:167], v224, %[rs], %[sb] offen offset:1024 sc1\n\t"
        "buffer_load_dwordx4 v[168:171], v225, %[rs], %[sb] offen sc1\n\t"
        "buffer_load_dwordx4 v[172:175], v226, %[rs], %[sb] offen sc1\n\t"
        "ds_read_b128 v[80:83], v236\n\t"
        "ds_read_b32 v237, v237\n\t"
        "ds_read_b128 v[112:115], v241\n\t"
        "ds_read_b128 v[116:119], v241 offset:1024\n\t"
        "ds_read_b128 v[120:123], v242\n\t"
        "ds_read_b128 v[124:127], v242 offset:1024\n\t"
        "ds_read_b128 v[128:131], v243\n\t"
        "ds_read_b128 v[132:135], v243 offset:1024\n\t"
        "ds_read_b128 v[136:139], v244\n\t"
        "ds_read_b128 v[140:143], v244 offset:1024\n\t"
        "s_waitcnt lgkmcnt(0)\n\t"
        "s_barrier\n\t"
        "v_max3_f32 v236, v80, v81, v82\n\t"
        "v_max_f32 v236, v236, v83\n\t"
        "v_mul_f32 v236, v236, v237\n\t"
        "v_sqrt_f32 v236, v236\n\t"
        "s_nop 0\n\t"
        "v_mul_f32 v96, -0.5, v236\n\t"
        "v_mov_b32 v97, v96\n\t"
        "v_mov_b64 v[98:99], v[96:97]\n\t"
        "v_mov_b64 v[100:101], v[96:97]\n\t"
        "v_mov_b64 v[102:103], v[96:97]\n\t"
        "v_mov_b64 v[104:105], v[96:97]\n\t"
        "v_mov_b64 v[106:107], v[96:97]\n\t"
        "v_mov_b64 v[108:109], v[96:97]\n\t"
        "v_mov_b64 v[110:111], v[96:97]\n\t"
        "buffer_load_dwordx4 v[176:179], v227, %[rs], %[sb] offen sc1\n\t"
        "buffer_load_dwordx4 v[180:183], v227, %[rs], %[sb] offen offset:1024 sc1\n\t"
        "buffer_load_dwordx4 v[184:187], v228, %[rs], %[sb] offen sc1\n\t"
        "buffer_load_dwordx4 v[188:191], v229, %[rs], %[sb] offen sc1\n\t"
        "s_waitcnt vmcnt(4)\n\t"
        "v_mfma_f32_32x32x16_bf16 v[64:79], v[160:163], v[112:115], v[96:111]\n\t"
        "v_mfma_f32_32x32x16_bf16 v[64:79], v[164:167], v[116:119], v[64:79]\n\t"
        "buffer_load_dwordx4 v[192:195], v230, %[rs], %[sb] offen sc1\n\t"
        "buffer_load_dwordx4 v[196:199], v230, %[rs], %[sb] offen offset:1024 sc1\n\t"
        "buffer_load_dwordx4 v[200:203], v231, %[rs], %[sb] offen sc1\n\t"
        "buffer_load_dwordx4 v[204:207], v232, %[rs], %[sb] offen sc1\n\t"
        "s_nop 9\n\t"
        "LOOP%=:\n\t"
        "v_mfma_f32_32x32x16_bf16 v[80:95], v[160:163], v[120:123], v[96:111]\n\t"
        "v_exp_f32 v64, v64\n\t"
        "v_exp_f32 v65, v65\n\t"
        "v_exp_f32 v66, v66\n\t"
        "v_exp_f32 v67, v67\n\t"
        "v_mfma_f32_32x32x16_bf16 v[80:95], v[164:167], v[124:127], v[80:95]\n\t"
        "v_exp_f32 v68, v68\n\t"
        "v_exp_f32 v69, v69\n\t"
        "v_exp_f32 v70, v70\n\t"
        "v_exp_f32 v71, v71\n\t"
        "v_cvt_pk_bf16_f32 v144, v64, v65\n\t"
        "v_cvt_pk_bf16_f32 v145, v66, v67\n\t"
        "v_mfma_f32_32x32x16_bf16 v[48:63], v[216:219], v[152:155], v[48:63]\n\t"
        "v_exp_f32 v72, v72\n\t"
        "v_exp_f32 v73, v73\n\t"
        "v_exp_f32 v74, v74\n\t"
        "v_exp_f32 v75, v75\n\t"
        "v_cvt_pk_bf16_f32 v146, v68, v69\n\t"
        "v_cvt_pk_bf16_f32 v147, v70, v71\n\t"
        "v_mfma_f32_32x32x16_bf16 v[48:63], v[220:223], v[156:159], v[48:63]\n\t"
        "v_exp_f32 v76, v76\n\t"
        "v_exp_f32 v77, v77\n\t"
        "v_exp_f32 v78, v78\n\t"
        "v_exp_f32 v79, v79\n\t"
        "v_cvt_pk_bf16_f32 v148, v72, v73\n\t"
        "v_cvt_pk_bf16_f32 v149, v74, v75\n\t"
        "v_cvt_pk_bf16_f32 v150, v76, v77\n\t"
        "v_cvt_pk_bf16_f32 v151, v78, v79\n\t"
        "v_mfma_f32_32x32x16_bf16 v[64:79], v[160:163], v[128:131], v[96:111]\n\t"
        "buffer_load_dwordx4 v[208:211], v233, %[rs], %[sb] offen sc1\n\t"
        "buffer_load_dwordx4 v[212:215], v233, %[rs], %[sb] offen offset:1024 sc1\n\t"
        "buffer_load_dwordx4 v[216:219], v234, %[rs], %[sb] offen sc1\n\t"
        "buffer_load_dwordx4 v[220:223], v235, %[rs], %[sb] offen sc1\n\t"
        "v_exp_f32 v80, v80\n\t"
        "v_exp_f32 v81, v81\n\t"
        "v_exp_f32 v82, v82\n\t"
        "v_exp_f32 v83, v83\n\t"
        "v_mfma_f32_32x32x16_bf16 v[64:79], v[164:167], v[132:135], v[64:79]\n\t"
        "v_exp_f32 v84, v84\n\t"
        "v_exp_f32 v85, v85\n\t"
        "v_exp_f32 v86, v86\n\t"
        "v_exp_f32 v87, v87\n\t"
        "v_cvt_pk_bf16_f32 v152, v80, v81\n\t"
        "v_cvt_pk_bf16_f32 v153, v82, v83\n\t"
        "v_mfma_f32_32x32x16_bf16 v[0:15], v[168:171], v[144:147], v[0:15]\n\t"
        "v_exp_f32 v88, v88\n\t"
        "v_exp_f32 v89, v89\n\t"
        "v_exp_f32 v90, v90\n\t"
        "v_exp_f32 v91, v91\n\t"
        "v_cvt_pk_bf16_f32 v154, v84, v85\n\t"
        "v_cvt_pk_bf16_f32 v155, v86, v87\n\t"
        "v_mfma_f32_32x32x16_bf16 v[0:15], v[172:175], v[148:151], v[0:15]\n\t"
        "v_exp_f32 v92, v92\n\t"
        "v_exp_f32 v93, v93\n\t"
        "v_exp_f32 v94, v94\n\t"
        "v_exp_f32 v95, v95\n\t"
        "v_cvt_pk_bf16_f32 v156, v88, v89\n\t"
        "v_cvt_pk_bf16_f32 v157, v90, v91\n\t"
        "v_cvt_pk_bf16_f32 v158, v92, v93\n\t"
        "v_cvt_pk_bf16_f32 v159, v94, v95\n\t"
        "v_mfma_f32_32x32x16_bf16 v[80:95], v[160:163], v[136:139], v[96:111]\n\t"
        "ds_read_b32 v239, v238\n\t"
        "v_exp_f32 v64, v64\n\t"
        "v_exp_f32 v65, v65\n\t"
        "v_exp_f32 v66, v66\n\t"
        "v_exp_f32 v67, v67\n\t"
        "v_mfma_f32_32x32x16_bf16 v[80:95], v[164:167], v[140:143], v[80:95]\n\t"
        "v_exp_f32 v68, v68\n\t"
        "v_exp_f32 v69, v69\n\t"
        "v_exp_f32 v70, v70\n\t"
        "v_exp_f32 v71, v71\n\t"
        "v_cvt_pk_bf16_f32 v144, v64, v65\n\t"
        "v_cvt_pk_bf16_f32 v145, v66, v67\n\t"
        "v_mfma_f32_32x32x16_bf16 v[16:31], v[168:171], v[152:155], v[16:31]\n\t"
        "v_exp_f32 v72, v72\n\t"
        "v_exp_f32 v73, v73\n\t"
        "v_exp_f32 v74, v74\n\t"
        "v_exp_f32 v75, v75\n\t"
        "v_cvt_pk_bf16_f32 v146, v68, v69\n\t"
        "v_cvt_pk_bf16_f32 v147, v70, v71\n\t"
        "v_mfma_f32_32x32x16_bf16 v[16:31], v[172:175], v[156:159], v[16:31]\n\t"
        "v_exp_f32 v76, v76\n\t"
        "v_exp_f32 v77, v77\n\t"
        "v_exp_f32 v78, v78\n\t"
        "v_exp_f32 v79, v79\n\t"
        "v_cvt_pk_bf16_f32 v148, v72, v73\n\t"
        "v_cvt_pk_bf16_f32 v149, v74, v75\n\t"
        "v_cvt_pk_bf16_f32 v150, v76, v77\n\t"
        "v_cvt_pk_bf16_f32 v151, v78, v79\n\t"
        "s_waitcnt vmcnt(8)\n\t"
        "v_mfma_f32_32x32x16_bf16 v[64:79], v[176:179], v[112:115], v[96:111]\n\t"
        "v_exp_f32 v80, v80\n\t"
        "v_exp_f32 v81, v81\n\t"
        "v_exp_f32 v82, v82\n\t"
        "v_exp_f32 v83, v83\n\t"
        "v_mfma_f32_32x32x16_bf16 v[64:79], v[180:183], v[116:119], v[64:79]\n\t"
        "v_exp_f32 v84, v84\n\t"
        "v_exp_f32 v85, v85\n\t"
        "v_exp_f32 v86, v86\n\t"
        "v_exp_f32 v87, v87\n\t"
        "v_cvt_pk_bf16_f32 v152, v80, v81\n\t"
        "v_cvt_pk_bf16_f32 v153, v82, v83\n\t"
        "v_mfma_f32_32x32x16_bf16 v[32:47], v[168:171], v[144:147], v[32:47]\n\t"
        "v_exp_f32 v88, v88\n\t"
        "v_exp_f32 v89, v89\n\t"
        "v_exp_f32 v90, v90\n\t"
        "v_exp_f32 v91, v91\n\t"
        "v_cvt_pk_bf16_f32 v154, v84, v85\n\t"
        "v_cvt_pk_bf16_f32 v155, v86, v87\n\t"
        "v_mfma_f32_32x32x16_bf16 v[32:47], v[172:175], v[148:151], v[32:47]\n\t"
        "v_exp_f32 v92, v92\n\t"
        "v_exp_f32 v93, v93\n\t"
        "v_exp_f32 v94, v94\n\t"
        "v_exp_f32 v95, v95\n\t"
        "v_cvt_pk_bf16_f32 v156, v88, v89\n\t"
        "v_cvt_pk_bf16_f32 v157, v90, v91\n\t"
        "v_cvt_pk_bf16_f32 v158, v92, v93\n\t"
        "v_cvt_pk_bf16_f32 v159, v94, v95\n\t"
        "v_mfma_f32_32x32x16_bf16 v[80:95], v[176:179], v[120:123], v[96:111]\n\t"
        "CHK%=:\n\t"
        "s_waitcnt lgkmcnt(0)\n\t"
        "v_readfirstlane_b32 %[sb], v239\n\t"
        "ds_read_b32 v239, v238\n\t"
        "s_cmp_eq_u32 %[sb], -1\n\t"
        "s_cbranch_scc1 CHK%=\n\t"
        "v_add_u32 v238, 4, v238\n\t"
        "v_exp_f32 v64, v64\n\t"
        "v_exp_f32 v65, v65\n\t"
        "v_exp_f32 v66, v66\n\t"
        "v_exp_f32 v67, v67\n\t"
        "v_mfma_f32_32x32x16_bf16 v[80:95], v[180:183], v[124:127], v[80:95]\n\t"
        "v_exp_f32 v68, v68\n\t"
        "v_exp_f32 v69, v69\n\t"
        "v_exp_f32 v70, v70\n\t"
        "v_exp_f32 v71, v71\n\t"
        "v_cvt_pk_bf16_f32 v144, v64, v65\n\t"
        "v_cvt_pk_bf16_f32 v145, v66, v67\n\t"
        "v_mfma_f32_32x32x16_bf16 v[48:63], v[168:171], v[152:155], v[48:63]\n\t"
        "v_exp_f32 v72, v72\n\t"
        "v_exp_f32 v73, v73\n\t"
        "v_exp_f32 v74, v74\n\t"
        "v_exp_f32 v75, v75\n\t"
        "v_cvt_pk_bf16_f32 v146, v68, v69\n\t"
        "v_cvt_pk_bf16_f32 v147, v70, v71\n\t"
        "v_mfma_f32_32x32x16_bf16 v[48:63], v[172:175], v[156:159], v[48:63]\n\t"
        "v_exp_f32 v76, v76\n\t"
        "v_exp_f32 v77, v77\n\t"
        "v_exp_f32 v78, v78\n\t"
        "v_exp_f32 v79, v79\n\t"
        "v_cvt_pk_bf16_f32 v148, v72, v73\n\t"
        "v_cvt_pk_bf16_f32 v149, v74, v75\n\t"
        "v_cvt_pk_bf16_f32 v150, v76, v77\n\t"
        "v_cvt_pk_bf16_f32 v151, v78, v79\n\t"
        "v_mfma_f32_32x32x16_bf16 v[64:79], v[176:179], v[128:131], v[96:111]\n\t"
        "buffer_load_dwordx4 v[160:163], v224, %[rs], %[sb] offen sc1\n\t"
        "buffer_load_dwordx4 v[164:167], v224, %[rs], %[sb] offen offset:1024 sc1\n\t"
        "buffer_load_dwordx4 v[168:171], v225, %[rs], %[sb] offen sc1\n\t"
        "buffer_load_dwordx4 v[172:175], v226, %[rs], %[sb] offen sc1\n\t"
        "v_exp_f32 v80, v80\n\t"
        "v_exp_f32 v81, v81\n\t"
        "v_exp_f32 v82, v82\n\t"
        "v_exp_f32 v83, v83\n\t"
        "v_mfma_f32_32x32x16_bf16 v[64:79], v[180:183], v[132:135], v[64:79]\n\t"
        "v_exp_f32 v84, v84\n\t"
        "v_exp_f32 v85, v85\n\t"
        "v_exp_f32 v86, v86\n\t"
        "v_exp_f32 v87, v87\n\t"
        "v_cvt_pk_bf16_f32 v152, v80, v81\n\t"
        "v_cvt_pk_bf16_f32 v153, v82, v83\n\t"
        "v_mfma_f32_32x32x16_bf16 v[0:15], v[184:187], v[144:147], v[0:15]\n\t"
        "v_exp_f32 v88, v88\n\t"
        "v_exp_f32 v89, v89\n\t"
        "v_exp_f32 v90, v90\n\t"
        "v_exp_f32 v91, v91\n\t"
        "v_cvt_pk_bf16_f32 v154, v84, v85\n\t"
        "v_cvt_pk_bf16_f32 v155, v86, v87\n\t"
        "v_mfma_f32_32x32x16_bf16 v[0:15], v[188:191], v[148:151], v[0:15]\n\t"
        "v_exp_f32 v92, v92\n\t"
        "v_exp_f32 v93, v93\n\t"
        "v_exp_f32 v94, v94\n\t"
        "v_exp_f32 v95, v95\n\t"
        "v_cvt_pk_bf16_f32 v156, v88, v89\n\t"
        "v_cvt_pk_bf16_f32 v157, v90, v91\n\t"
        "v_cvt_pk_bf16_f32 v158, v92, v93\n\t"
        "v_cvt_pk_bf16_f32 v159, v94, v95\n\t"
        "v_mfma_f32_32x32x16_bf16 v[80:95], v[176:179], v[136:139], v[96:111]\n\t"
        "v_exp_f32 v64, v64\n\t"
        "v_exp_f32 v65, v65\n\t"
        "v_exp_f32 v66, v66\n\t"
        "v_exp_f32 v67, v67\n\t"
        "v_mfma_f32_32x32x16_bf16 v[80:95], v[180:183], v[140:143], v[80:95]\n\t"
        "v_exp_f32 v68, v68\n\t"
        "v_exp_f32 v69, v69\n\t"
        "v_exp_f32 v70, v70\n\t"
        "v_exp_f32 v71, v71\n\t"
        "v_cvt_pk_bf16_f32 v144, v64, v65\n\t"
        "v_cvt_pk_bf16_f32 v145, v66, v67\n\t"
        "v_mfma_f32_32x32x16_bf16 v[16:31], v[184:187], v[152:155], v[16:31]\n\t"
        "v_exp_f32 v72, v72\n\t"
        "v_exp_f32 v73, v73\n\t"
        "v_exp_f32 v74, v74\n\t"
        "v_exp_f32 v75, v75\n\t"
        "v_cvt_pk_bf16_f32 v146, v68, v69\n\t"
        "v_cvt_pk_bf16_f32 v147, v70, v71\n\t"
        "v_mfma_f32_32x32x16_bf16 v[16:31], v[188:191], v[156:159], v[16:31]\n\t"
        "v_exp_f32 v76, v76\n\t"
        "v_exp_f32 v77, v77\n\t"
        "v_exp_f32 v78, v78\n\t"
        "v_exp_f32 v79, v79\n\t"
        "v_cvt_pk_bf16_f32 v148, v72, v73\n\t"
        "v_cvt_pk_bf16_f32 v149, v74, v75\n\t"
        "v_cvt_pk_bf16_f32 v150, v76, v77\n\t"
        "v_cvt_pk_bf16_f32 v151, v78, v79\n\t"
        "s_waitcnt vmcnt(8)\n\t"
        "v_mfma_f32_32x32x16_bf16 v[64:79], v[192:195], v[112:115], v[96:111]\n\t"
        "v_exp_f32 v80, v80\n\t"
        "v_exp_f32 v81, v81\n\t"
        "v_exp_f32 v82, v82\n\t"
        "v_exp_f32 v83, v83\n\t"
        "v_mfma_f32_32x32x16_bf16 v[64:79], v[196:199], v[116:119], v[64:79]\n\t"
        "v_exp_f32 v84, v84\n\t"
        "v_exp_f32 v85, v85\n\t"
        "v_exp_f32 v86, v86\n\t"
        "v_exp_f32 v87, v87\n\t"
        "v_cvt_pk_bf16_f32 v152, v80, v81\n\t"
        "v_cvt_pk_bf16_f32 v153, v82, v83\n\t"
        "v_mfma_f32_32x32x16_bf16 v[32:47], v[184:187], v[144:147], v[32:47]\n\t"
        "v_exp_f32 v88, v88\n\t"
        "v_exp_f32 v89, v89\n\t"
        "v_exp_f32 v90, v90\n\t"
        "v_exp_f32 v91, v91\n\t"
        "v_cvt_pk_bf16_f32 v154, v84, v85\n\t"
        "v_cvt_pk_bf16_f32 v155, v86, v87\n\t"
        "v_mfma_f32_32x32x16_bf16 v[32:47], v[188:191], v[148:151], v[32:47]\n\t"
        "v_exp_f32 v92, v92\n\t"
        "v_exp_f32 v93, v93\n\t"
        "v_exp_f32 v94, v94\n\t"
        "v_exp_f32 v95, v95\n\t"
        "v_cvt_pk_bf16_f32 v156, v88, v89\n\t"
        "v_cvt_pk_bf16_f32 v157, v90, v91\n\t"
        "v_cvt_pk_bf16_f32 v158, v92, v93\n\t"
        "v_cvt_pk_bf16_f32 v159, v94, v95\n\t"
        "v_mfma_f32_32x32x16_bf16 v[80:95], v[192:195], v[120:123], v[96:111]\n\t"
        "v_exp_f32 v64, v64\n\t"
        "v_exp_f32 v65, v65\n\t"
        "v_exp_f32 v66, v66\n\t"
        "v_exp_f32 v67, v67\n\t"
        "v_mfma_f32_32x32x16_bf16 v[80:95], v[196:199], v[124:127], v[80:95]\n\t"
        "v_exp_f32 v68, v68\n\t"
        "v_exp_f32 v69, v69\n\t"
        "v_exp_f32 v70, v70\n\t"
        "v_exp_f32 v71, v71\n\t"
        "v_cvt_pk_bf16_f32 v144, v64, v65\n\t"
        "v_cvt_pk_bf16_f32 v145, v66, v67\n\t"
        "v_mfma_f32_32x32x16_bf16 v[48:63], v[184:187], v[152:155], v[48:63]\n\t"
        "v_exp_f32 v72, v72\n\t"
        "v_exp_f32 v73, v73\n\t"
        "v_exp_f32 v74, v74\n\t"
        "v_exp_f32 v75, v75\n\t"
        "v_cvt_pk_bf16_f32 v146, v68, v69\n\t"
        "v_cvt_pk_bf16_f32 v147, v70, v71\n\t"
        "v_mfma_f32_32x32x16_bf16 v[48:63], v[188:191], v[156:159], v[48:63]\n\t"
        "v_exp_f32 v76, v76\n\t"
        "v_exp_f32 v77, v77\n\t"
        "v_exp_f32 v78, v78\n\t"
        "v_exp_f32 v79, v79\n\t"
        "v_cvt_pk_bf16_f32 v148, v72, v73\n\t"
        "v_cvt_pk_bf16_f32 v149, v74, v75\n\t"
        "v_cvt_pk_bf16_f32 v150, v76, v77\n\t"
        "v_cvt_pk_bf16_f32 v151, v78, v79\n\t"
        "v_mfma_f32_32x32x16_bf16 v[64:79], v[192:195], v[128:131], v[96:111]\n\t"
        "buffer_load_dwordx4 v[176:179], v227, %[rs], %[sb] offen sc1\n\t"
        "buffer_load_dwordx4 v[180:183], v227, %[rs], %[sb] offen offset:1024 sc1\n\t"
        "buffer_load_dwordx4 v[184:187], v228, %[rs], %[sb] offen sc1\n\t"
        "buffer_load_dwordx4 v[188:191], v229, %[rs], %[sb] offen sc1\n\t"
        "v_exp_f32 v80, v80\n\t"
        "v_exp_f32 v81, v81\n\t"
        "v_exp_f32 v82, v82\n\t"
        "v_exp_f32 v83, v83\n\t"
        "v_mfma_f32_32x32x16_bf16 v[64:79], v[196:199], v[132:135], v[64:79]\n\t"
        "v_exp_f32 v84, v84\n\t"
        "v_exp_f32 v85, v85\n\t"
        "v_exp_f32 v86, v86\n\t"
        "v_exp_f32 v87, v87\n\t"
        "v_cvt_pk_bf16_f32 v152, v80, v81\n\t"
        "v_cvt_pk_bf16_f32 v153, v82, v83\n\t"
        "v_mfma_f32_32x32x16_bf16 v[0:15], v[200:203], v[144:147], v[0:15]\n\t"
        "v_exp_f32 v88, v88\n\t"
        "v_exp_f32 v89, v89\n\t"
        "v_exp_f32 v90, v90\n\t"
        "v_exp_f32 v91, v91\n\t"
        "v_cvt_pk_bf16_f32 v154, v84, v85\n\t"
        "v_cvt_pk_bf16_f32 v155, v86, v87\n\t"
        "v_mfma_f32_32x32x16_bf16 v[0:15], v[204:207], v[148:151], v[0:15]\n\t"
        "v_exp_f32 v92, v92\n\t"
        "v_exp_f32 v93, v93\n\t"
        "v_exp_f32 v94, v94\n\t"
        "v_exp_f32 v95, v95\n\t"
        "v_cvt_pk_bf16_f32 v156, v88, v89\n\t"
        "v_cvt_pk_bf16_f32 v157, v90, v91\n\t"
        "v_cvt_pk_bf16_f32 v158, v92, v93\n\t"
        "v_cvt_pk_bf16_f32 v159, v94, v95\n\t"
        "v_mfma_f32_32x32x16_bf16 v[80:95], v[192:195], v[136:139], v[96:111]\n\t"
        "v_exp_f32 v64, v64\n\t"
        "v_exp_f32 v65, v65\n\t"
        "v_exp_f32 v66, v66\n\t"
        "v_exp_f32 v67, v67\n\t"
        "v_mfma_f32_32x32x16_bf16 v[80:95], v[196:199], v[140:143], v[80:95]\n\t"
        "v_exp_f32 v68, v68\n\t"
        "v_exp_f32 v69, v69\n\t"
        "v_exp_f32 v70, v70\n\t"
        "v_exp_f32 v71, v71\n\t"
        "v_cvt_pk_bf16_f32 v144, v64, v65\n\t"
        "v_cvt_pk_bf16_f32 v145, v66, v67\n\t"
        "v_mfma_f32_32x32x16_bf16 v[16:31], v[200:203], v[152:155], v[16:31]\n\t"
        "v_exp_f32 v72, v72\n\t"
        "v_exp_f32 v73, v73\n\t"
        "v_exp_f32 v74, v74\n\t"
        "v_exp_f32 v75, v75\n\t"
        "v_cvt_pk_bf16_f32 v146, v68, v69\n\t"
        "v_cvt_pk_bf16_f32 v147, v70, v71\n\t"
        "v_mfma_f32_32x32x16_bf16 v[16:31], v[204:207], v[156:159], v[16:31]\n\t"
        "v_exp_f32 v76, v76\n\t"
        "v_exp_f32 v77, v77\n\t"
        "v_exp_f32 v78, v78\n\t"
        "v_exp_f32 v79, v79\n\t"
        "v_cvt_pk_bf16_f32 v148, v72, v73\n\t"
        "v_cvt_pk_bf16_f32 v149, v74, v75\n\t"
        "v_cvt_pk_bf16_f32 v150, v76, v77\n\t"
        "v_cvt_pk_bf16_f32 v151, v78, v79\n\t"
        "s_waitcnt vmcnt(8)\n\t"
        "v_mfma_f32_32x32x16_bf16 v[64:79], v[208:211], v[112:115], v[96:111]\n\t"
        "v_exp_f32 v80, v80\n\t"
        "v_exp_f32 v81, v81\n\t"
        "v_exp_f32 v82, v82\n\t"
        "v_exp_f32 v83, v83\n\t"
        "v_mfma_f32_32x32x16_bf16 v[64:79], v[212:215], v[116:119], v[64:79]\n\t"
        "v_exp_f32 v84, v84\n\t"
        "v_exp_f32 v85, v85\n\t"
        "v_exp_f32 v86, v86\n\t"
        "v_exp_f32 v87, v87\n\t"
        "v_cvt_pk_bf16_f32 v152, v80, v81\n\t"
        "v_cvt_pk_bf16_f32 v153, v82, v83\n\t"
        "v_mfma_f32_32x32x16_bf16 v[32:47], v[200:203], v[144:147], v[32:47]\n\t"
        "v_exp_f32 v88, v88\n\t"
        "v_exp_f32 v89, v89\n\t"
        "v_exp_f32 v90, v90\n\t"
        "v_exp_f32 v91, v91\n\t"
        "v_cvt_pk_bf16_f32 v154, v84, v85\n\t"
        "v_cvt_pk_bf16_f32 v155, v86, v87\n\t"
        "v_mfma_f32_32x32x16_bf16 v[32:47], v[204:207], v[148:151], v[32:47]\n\t"
        "v_exp_f32 v92, v92\n\t"
        "v_exp_f32 v93, v93\n\t"
        "v_exp_f32 v94, v94\n\t"
        "v_exp_f32 v95, v95\n\t"
        "v_cvt_pk_bf16_f32 v156, v88, v89\n\t"
        "v_cvt_pk_bf16_f32 v157, v90, v91\n\t"
        "v_cvt_pk_bf16_f32 v158, v92, v93\n\t"
        "v_cvt_pk_bf16_f32 v159, v94, v95\n\t"
        "v_mfma_f32_32x32x16_bf16 v[80:95], v[208:211], v[120:123], v[96:111]\n\t"
        "v_exp_f32 v64, v64\n\t"
        "v_exp_f32 v65, v65\n\t"
        "v_exp_f32 v66, v66\n\t"
        "v_exp_f32 v67, v67\n\t"
        "v_mfma_f32_32x32x16_bf16 v[80:95], v[212:215], v[124:127], v[80:95]\n\t"
        "v_exp_f32 v68, v68\n\t"
        "v_exp_f32 v69, v69\n\t"
        "v_exp_f32 v70, v70\n\t"
        "v_exp_f32 v71, v71\n\t"
        "v_cvt_pk_bf16_f32 v144, v64, v65\n\t"
        "v_cvt_pk_bf16_f32 v145, v66, v67\n\t"
        "v_mfma_f32_32x32x16_bf16 v[48:63], v[200:203], v[152:155], v[48:63]\n\t"
        "v_exp_f32 v72, v72\n\t"
        "v_exp_f32 v73, v73\n\t"
        "v_exp_f32 v74, v74\n\t"
        "v_exp_f32 v75, v75\n\t"
        "v_cvt_pk_bf16_f32 v146, v68, v69\n\t"
        "v_cvt_pk_bf16_f32 v147, v70, v71\n\t"
        "v_mfma_f32_32x32x16_bf16 v[48:63], v[204:207], v[156:159], v[48:63]\n\t"
        "v_exp_f32 v76, v76\n\t"
        "v_exp_f32 v77, v77\n\t"
        "v_exp_f32 v78, v78\n\t"
        "v_exp_f32 v79, v79\n\t"
        "v_cvt_pk_bf16_f32 v148, v72, v73\n\t"
        "v_cvt_pk_bf16_f32 v149, v74, v75\n\t"
        "v_cvt_pk_bf16_f32 v150, v76, v77\n\t"
        "v_cvt_pk_bf16_f32 v151, v78, v79\n\t"
        "v_mfma_f32_32x32x16_bf16 v[64:79], v[208:211], v[128:131], v[96:111]\n\t"
        "buffer_load_dwordx4 v[192:195], v230, %[rs], %[sb] offen sc1\n\t"
        "buffer_load_dwordx4 v[196:199], v230, %[rs], %[sb] offen offset:1024 sc1\n\t"
        "buffer_load_dwordx4 v[200:203], v231, %[rs], %[sb] offen sc1\n\t"
        "buffer_load_dwordx4 v[204:207], v232, %[rs], %[sb] offen sc1\n\t"
        "s_sub_u32 %[cnt], %[cnt], 1\n\t"
        "s_cmp_eq_u32 %[cnt], 0\n\t"
        "v_exp_f32 v80, v80\n\t"
        "v_exp_f32 v81, v81\n\t"
        "v_exp_f32 v82, v82\n\t"
        "v_exp_f32 v83, v83\n\t"
        "v_mfma_f32_32x32x16_bf16 v[64:79], v[212:215], v[132:135], v[64:79]\n\t"
        "v_exp_f32 v84, v84\n\t"
        "v_exp_f32 v85, v85\n\t"
        "v_exp_f32 v86, v86\n\t"
        "v_exp_f32 v87, v87\n\t"
        "v_cvt_pk_bf16_f32 v152, v80, v81\n\t"
        "v_cvt_pk_bf16_f32 v153, v82, v83\n\t"
        "v_mfma_f32_32x32x16_bf16 v[0:15], v[216:219], v[144:147], v[0:15]\n\t"
        "v_exp_f32 v88, v88\n\t"
        "v_exp_f32 v89, v89\n\t"
        "v_exp_f32 v90, v90\n\t"
        "v_exp_f32 v91, v91\n\t"
        "v_cvt_pk_bf16_f32 v154, v84, v85\n\t"
        "v_cvt_pk_bf16_f32 v155, v86, v87\n\t"
        "v_mfma_f32_32x32x16_bf16 v[0:15], v[220:223], v[148:151], v[0:15]\n\t"
        "v_exp_f32 v92, v92\n\t"
        "v_exp_f32 v93, v93\n\t"
        "v_exp_f32 v94, v94\n\t"
        "v_exp_f32 v95, v95\n\t"
        "v_cvt_pk_bf16_f32 v156, v88, v89\n\t"
        "v_cvt_pk_bf16_f32 v157, v90, v91\n\t"
        "v_cvt_pk_bf16_f32 v158, v92, v93\n\t"
        "v_cvt_pk_bf16_f32 v159, v94, v95\n\t"
        "s_cbranch_scc1 TAIL%=\n\t"
        "v_mfma_f32_32x32x16_bf16 v[80:95], v[208:211], v[136:139], v[96:111]\n\t"
        "v_exp_f32 v64, v64\n\t"
        "v_exp_f32 v65, v65\n\t"
        "v_exp_f32 v66, v66\n\t"
        "v_exp_f32 v67, v67\n\t"
        "v_mfma_f32_32x32x16_bf16 v[80:95], v[212:215], v[140:143], v[80:95]\n\t"
        "v_exp_f32 v68, v68\n\t"
        "v_exp_f32 v69, v69\n\t"
        "v_exp_f32 v70, v70\n\t"
        "v_exp_f32 v71, v71\n\t"
        "v_cvt_pk_bf16_f32 v144, v64, v65\n\t"
        "v_cvt_pk_bf16_f32 v145, v66, v67\n\t"
        "v_mfma_f32_32x32x16_bf16 v[16:31], v[216:219], v[152:155], v[16:31]\n\t"
        "v_exp_f32 v72, v72\n\t"
        "v_exp_f32 v73, v73\n\t"
        "v_exp_f32 v74, v74\n\t"
        "v_exp_f32 v75, v75\n\t"
        "v_cvt_pk_bf16_f32 v146, v68, v69\n\t"
        "v_cvt_pk_bf16_f32 v147, v70, v71\n\t"
        "v_mfma_f32_32x32x16_bf16 v[16:31], v[220:223], v[156:159], v[16:31]\n\t"
        "v_exp_f32 v76, v76\n\t"
        "v_exp_f32 v77, v77\n\t"
        "v_exp_f32 v78, v78\n\t"
        "v_exp_f32 v79, v79\n\t"
        "v_cvt_pk_bf16_f32 v148, v72, v73\n\t"
        "v_cvt_pk_bf16_f32 v149, v74, v75\n\t"
        "v_cvt_pk_bf16_f32 v150, v76, v77\n\t"
        "v_cvt_pk_bf16_f32 v151, v78, v79\n\t"
        "s_waitcnt vmcnt(8)\n\t"
        "v_mfma_f32_32x32x16_bf16 v[64:79], v[160:163], v[112:115], v[96:111]\n\t"
        "v_exp_f32 v80, v80\n\t"
        "v_exp_f32 v81, v81\n\t"
        "v_exp_f32 v82, v82\n\t"
        "v_exp_f32 v83, v83\n\t"
        "v_mfma_f32_32x32x16_bf16 v[64:79], v[164:167], v[116:119], v[64:79]\n\t"
        "v_exp_f32 v84, v84\n\t"
        "v_exp_f32 v85, v85\n\t"
        "v_exp_f32 v86, v86\n\t"
        "v_exp_f32 v87, v87\n\t"
        "v_cvt_pk_bf16_f32 v152, v80, v81\n\t"
        "v_cvt_pk_bf16_f32 v153, v82, v83\n\t"
        "v_mfma_f32_32x32x16_bf16 v[32:47], v[216:219], v[144:147], v[32:47]\n\t"
        "v_exp_f32 v88, v88\n\t"
        "v_exp_f32 v89, v89\n\t"
        "v_exp_f32 v90, v90\n\t"
        "v_exp_f32 v91, v91\n\t"
        "v_cvt_pk_bf16_f32 v154, v84, v85\n\t"
        "v_cvt_pk_bf16_f32 v155, v86, v87\n\t"
        "v_mfma_f32_32x32x16_bf16 v[32:47], v[220:223], v[148:151], v[32:47]\n\t"
        "v_exp_f32 v92, v92\n\t"
        "v_exp_f32 v93, v93\n\t"
        "v_exp_f32 v94, v94\n\t"
        "v_exp_f32 v95, v95\n\t"
        "v_cvt_pk_bf16_f32 v156, v88, v89\n\t"
        "v_cvt_pk_bf16_f32 v157, v90, v91\n\t"
        "v_cvt_pk_bf16_f32 v158, v92, v93\n\t"
        "v_cvt_pk_bf16_f32 v159, v94, v95\n\t"
        "s_branch LOOP%=\n\t"
        "TAIL%=:\n\t"
        "v_mfma_f32_32x32x16_bf16 v[80:95], v[208:211], v[136:139], v[96:111]\n\t"
        "v_exp_f32 v64, v64\n\t"
        "v_exp_f32 v65, v65\n\t"
        "v_exp_f32 v66, v66\n\t"
        "v_exp_f32 v67, v67\n\t"
        "v_mfma_f32_32x32x16_bf16 v[80:95], v[212:215], v[140:143], v[80:95]\n\t"
        "ds_write_b128 v240, v[0:3] offset:0\n\t"
        "ds_write_b128 v240, v[4:7] offset:1024\n\t"
        "ds_write_b128 v240, v[8:11] offset:2048\n\t"
        "ds_write_b128 v240, v[12:15] offset:3072\n\t"
        "v_exp_f32 v68, v68\n\t"
        "v_exp_f32 v69, v69\n\t"
        "v_exp_f32 v70, v70\n\t"
        "v_exp_f32 v71, v71\n\t"
        "v_cvt_pk_bf16_f32 v144, v64, v65\n\t"
        "v_cvt_pk_bf16_f32 v145, v66, v67\n\t"
        "v_mfma_f32_32x32x16_bf16 v[16:31], v[216:219], v[152:155], v[16:31]\n\t"
        "v_exp_f32 v72, v72\n\t"
        "v_exp_f32 v73, v73\n\t"
        "v_exp_f32 v74, v74\n\t"
        "v_exp_f32 v75, v75\n\t"
        "v_cvt_pk_bf16_f32 v146, v68, v69\n\t"
        "v_cvt_pk_bf16_f32 v147, v70, v71\n\t"
        "v_mfma_f32_32x32x16_bf16 v[16:31], v[220:223], v[156:159], v[16:31]\n\t"
        "v_exp_f32 v76, v76\n\t"
        "v_exp_f32 v77, v77\n\t"
        "v_exp_f32 v78, v78\n\t"
        "v_exp_f32 v79, v79\n\t"
        "v_cvt_pk_bf16_f32 v148, v72, v73\n\t"
        "v_cvt_pk_bf16_f32 v149, v74, v75\n\t"
        "v_cvt_pk_bf16_f32 v150, v76, v77\n\t"
        "v_cvt_pk_bf16_f32 v151, v78, v79\n\t"
        "v_exp_f32 v80, v80\n\t"
        "v_exp_f32 v81, v81\n\t"
        "v_exp_f32 v82, v82\n\t"
        "v_exp_f32 v83, v83\n\t"
        "v_exp_f32 v84, v84\n\t"
        "v_exp_f32 v85, v85\n\t"
        "v_exp_f32 v86, v86\n\t"
        "v_exp_f32 v87, v87\n\t"
        "v_cvt_pk_bf16_f32 v152, v80, v81\n\t"
        "v_cvt_pk_bf16_f32 v153, v82, v83\n\t"
        "v_mfma_f32_32x32x16_bf16 v[32:47], v[216:219], v[144:147], v[32:47]\n\t"
        "ds_write_b128 v240, v[16:19] offset:4096\n\t"
        "ds_write_b128 v240, v[20:23] offset:5120\n\t"
        "ds_write_b128 v240, v[24:27] offset:6144\n\t"
        "ds_write_b128 v240, v[28:31] offset:7168\n\t"
        "v_exp_f32 v88, v88\n\t"
        "v_exp_f32 v89, v89\n\t"
        "v_exp_f32 v90, v90\n\t"
        "v_exp_f32 v91, v91\n\t"
        "v_cvt_pk_bf16_f32 v154, v84, v85\n\t"
        "v_cvt_pk_bf16_f32 v155, v86, v87\n\t"
        "v_mfma_f32_32x32x16_bf16 v[32:47], v[220:223], v[148:151], v[32:47]\n\t"
        "v_exp_f32 v92, v92\n\t"
        "v_exp_f32 v93, v93\n\t"
        "v_exp_f32 v94, v94\n\t"
        "v_exp_f32 v95, v95\n\t"
        "v_cvt_pk_bf16_f32 v156, v88, v89\n\t"
        "v_cvt_pk_bf16_f32 v157, v90, v91\n\t"
        "v_cvt_pk_bf16_f32 v158, v92, v93\n\t"
        "v_cvt_pk_bf16_f32 v159, v94, v95\n\t"
        "s_nop 1\n\t"
        "v_mfma_f32_32x32x16_bf16 v[48:63], v[216:219], v[152:155], v[48:63]\n\t"
        "v_mfma_f32_32x32x16_bf16 v[48:63], v[220:223], v[156:159], v[48:63]\n\t"
        "s_nop 3\n\t"
        "ds_write_b128 v240, v[32:35] offset:8192\n\t"
        "ds_write_b128 v240, v[36:39] offset:9216\n\t"
        "ds_write_b128 v240, v[40:43] offset:10240\n\t"
        "ds_write_b128 v240, v[44:47] offset:11264\n\t"
        "s_waitcnt vmcnt(0) lgkmcnt(0)\n\t"
        "s_barrier\n\t"
        "s_nop 7\n\t"
        : "={v[48:63]}"(acc), [sb] "+s"(sb), [cnt] "+s"(cnt), "+{v236}"(q2a), "+{v237}"(kba), "+{v238}"(ordn), "={v[136:139]}"(qo0), "={v[140:143]}"(qo1)
        : "{v224}"(vok + 0 * TS), "{v225}"(vov + 0 * TS), "{v226}"(vov + 2 * VROW + 0 * TS), "{v227}"(vok + 1 * TS), "{v228}"(vov + 1 * TS), "{v229}"(vov + 2 * VROW + 1 * TS), "{v230}"(vok + 2 * TS), "{v231}"(vov + 2 * TS), "{v232}"(vov + 2 * VROW + 2 * TS), "{v233}"(vok + 3 * TS), "{v234}"(vov + 3 * TS), "{v235}"(vov + 2 * VROW + 3 * TS),
          "{v240}"(puta), "{v241}"(qxa[0]), "{v242}"(qxa[1]), "{v243}"(qxa[2]), "{v244}"(qxa[3]), [rs] "s"(rsKVr)
        : "v0", "v1", "v2", "v3", "v4", "v5", "v6", "v7", "v8", "v9", "v10", "v11", "v12", "v13", "v14", "v15", "v16", "v17", "v18", "v19", "v20", "v21", "v22", "v23", "v24", "v25", "v26", "v27", "v28", "v29", "v30", "v31", "v32", "v33", "v34", "v35", "v36", "v37", "v38", "v39", "v40", "v41", "v42", "v43", "v44", "v45", "v46", "v47", "v64", "v65", "v66", "v67", "v68", "v69", "v70", "v71", "v72", "v73", "v74", "v75", "v76", "v77", "v78", "v79", "v80", "v81", "v82", "v83", "v84", "v85", "v86", "v87", "v88", "v89", "v90", "v91", "v92", "v93", "v94", "v95", "v96", "v97", "v98", "v99", "v100", "v101", "v102", "v103", "v104", "v105", "v106", "v107", "v108", "v109", "v110", "v111", "v112", "v113", "v114", "v115", "v116", "v117", "v118", "v119", "v120", "v121", "v122", "v123", "v124", "v125", "v126", "v127", "v128", "v129", "v130", "v131", "v132", "v133", "v134", "v135", "v144", "v145", "v146", "v147", "v148", "v149", "v150", "v151", "v152", "v153", "v154", "v155", "v156", "v157", "v158", "v159", "v160", "v161", "v162", "v163", "v164", "v165", "v166", "v167", "v168", "v169", "v170", "v171", "v172", "v173", "v174", "v175", "v176", "v177", "v178", "v179", "v180", "v181", "v182", "v183", "v184", "v185", "v186", "v187", "v188", "v189", "v190", "v191", "v192", "v193", "v194", "v195", "v196", "v197", "v198", "v199", "v200", "v201", "v202", "v203", "v204", "v205", "v206", "v207", "v208", "v209", "v210", "v211", "v212", "v213", "v214", "v215", "v216", "v217", "v218", "v219", "v220", "v221", "v222", "v223", "v239", "scc", "memory");
    }
#pragma unroll
    for (int s = 0; s < 3; ++s) {
        LAS const char* sp = lds + L_CB + (((w - 1 - s) & 3) * 3 + s) * 4096 + lane * 16;
#pragma unroll
        for (int g = 0; g < 4; ++g) {
            const f32x4 v = *(LAS const f32x4*)(sp + g * 1024);
            acc[4 * g] += v[0]; acc[4 * g + 1] += v[1]; acc[4 * g + 2] += v[2]; acc[4 * g + 3] += v[3];
        }
    }
    float l = half_hi(acc[12]);
    if (__builtin_expect(__any(!(l >= L_MIN && l <= L_MAX)), 0)) {
        const f32x16 zero = {};
        float m = -__builtin_inff();
#pragma unroll 1
        for (int t = 0; t < 64; ++t) {
            f32x16 s = MFMA(asbf(ld16_wt(rsKVr, vok, t * TS)), asbf(qo0), zero);
            s = MFMA(asbf(ld16_wt(rsKVr, vok + 1024, t * TS)), asbf(qo1), s);
#pragma unroll
            for (int i = 0; i < 16; ++i) m = __builtin_fmaxf(m, s[i]);
        }
        m = __builtin_fmaxf(half_lo(m), half_hi(m));
        acc = zero;
#pragma unroll 1
        for (int t = 0; t < 64; ++t) {
            f32x16 s = MFMA(asbf(ld16_wt(rsKVr, vok, t * TS)), asbf(qo0), zero);
            s = MFMA(asbf(ld16_wt(rsKVr, vok + 1024, t * TS)), asbf(qo1), s);
#pragma unroll
            for (int i = 0; i < 16; ++i) s[i] = __builtin_amdgcn_exp2f(s[i] - m);
            acc = MFMA(asbf(ld16_wt(rsKVr, vov, t * TS)), asbf(pack8(s, 0)), acc);
            acc = MFMA(asbf(ld16_wt(rsKVr, vov + 2 * VROW, t * TS)), asbf(pack8(s, 1)), acc);
        }
        l = half_hi(acc[12]);
    }
    {
        const float rl = __builtin_amdgcn_rcpf(l);
#pragma unroll
        for (int i = 0; i < 16; ++i) acc[i] = (i >= 12 && h) ? 0.f : acc[i] * rl;
        const unsigned so = (kt * NH + hd) * 2048;
        u32x4 p0 = pack8(acc, 0), p1 = pack8(acc, 1);
#pragma unroll
        for (int i = 0; i < 4; ++i) { p0[i] = min(p0[i], POISON - 1); p1[i] = min(p1[i], POISON - 1); }
        st16_wt(p0, rsOI, lane * 16, so);
        st16_wt(p1, rsOI, lane * 16 + 1024, so);
    }
    BAR();

    bf16x8 wf[32];
    LAS const char* ob = lds + L_CB + lane * 16;
    asm volatile("" : "+v"(ob));
    {
        LAS const char* wb = lds + L_WO + (4 * h + q4) * 256 + ((w ^ q4) * 64) + g16 * 32 + p4 * 8;
#pragma unroll
        for (int k = 0; k < 32; ++k) {
            const int ro = ((k >> 1) * DH + (k & 1) * 16) * 256;
            wf[k] = cat(vtr(wb + ro), vtr(wb + ro + 2048 - (k == 31 ? h * 1024 : 0)));
        }
    }
    asm volatile("s_waitcnt lgkmcnt(0)\n\ts_barrier\nMHA_CODE_END:" ::: "memory");
    LAS char* tb = lds + L_CB + 32768;
    if (cg < 3 || w < 2) {
        f32x16 c = {};
        bf16x8 of[3][4];
#define RDO(i) do { _Pragma("unroll") for (int j = 0; j < 4; ++j) of[(i) % 3][j] = *(LAS const bf16x8*)(ob + ((i) * 4 + j) * 1024); } while (0)
        RDO(0); RDO(1); SBAR();
#pragma unroll
        for (int i = 0; i < 8; ++i) {
            if (i + 2 < 8) RDO(i + 2);
            SBAR();
#pragma unroll
            for (int j = 0; j < 4; ++j) c = MFMA(wf[i * 4 + j], of[i % 3][j], c);
            SBAR();
        }
#undef RDO
#pragma unroll
        for (int g = 0; g < 4; ++g) {
            typedef unsigned u32x2 __attribute__((ext_vector_type(2)));
            *(LAS u32x2*)(tb + r * 272 + w * 64 + g * 16 + h * 8) = (u32x2){cvtpk(c[4 * g], c[4 * g + 1]), cvtpk(c[4 * g + 2], c[4 * g + 3])};
        }
    }
    BAR();
    {
        const unsigned rr = w * 8 + (lane >> 4), cc = (lane & 15) * 16;
        GAS char* o2 = (GAS char*)out + (size_t)(q32 * 32 + rr) * (DM * 2) + cg * 256 + cc;
        const u32x4 v0 = *(LAS const u32x4*)(tb + rr * 272 + cc), v1 = *(LAS const u32x4*)(tb + (rr + 4) * 272 + cc);
        const __amdgpu_buffer_rsrc_t rsOut = mkrs(out, SEQ * DM * 2); const unsigned oo = (q32 * 32 + rr) * (DM * 2) + cg * 256 + cc;
        if (cg < 3 || cc < 128) { __builtin_amdgcn_raw_buffer_store_b128(v0, rsOut, oo, 0, 2); __builtin_amdgcn_raw_buffer_store_b128(v1, rsOut, oo + 4 * DM * 2, 0, 2); }
    }
}

extern "C" void kernel_launch(void* const* d_in, const int* in_sizes, int n_in, void* d_out, int out_size, void* d_ws, size_t ws_size,
                              hipStream_t stream) {
    (void)hipFuncSetAttribute((const void*)mha_kernel, hipFuncAttributeMaxDynamicSharedMemorySize, LDS_BYTES);
    mha_kernel<<<256, 512, LDS_BYTES, stream>>>((const char*)d_in[0], (const char*)d_in[1], (const char*)d_in[2], (char*)d_out, (char*)d_ws);
}
```

```cpp
#include <hip/hip_runtime.h>
#include <hip/hip_bf16.h>
#include <stdint.h>

typedef short bf16x8 __attribute__((ext_vector_type(8)));
typedef short s16x4 __attribute__((ext_vector_type(4)));
typedef float f32x16 __attribute__((ext_vector_type(16)));
typedef float f32x4 __attribute__((ext_vector_type(4)));
typedef float f32x2 __attribute__((ext_vector_type(2)));
typedef unsigned u32x4 __attribute__((ext_vector_type(4)));
typedef unsigned u32x2 __attribute__((ext_vector_type(2)));
typedef __bf16 bf16x2_t __attribute__((ext_vector_type(2)));
typedef short v4i16_t __attribute__((ext_vector_type(4)));
typedef unsigned long long u64;
#define LAS __attribute__((address_space(3)))
#define GAS __attribute__((address_space(1)))
#define DI __device__ __forceinline__
#define MFMA(a, b, c) __builtin_amdgcn_mfma_f32_32x32x16_bf16((a), (b), (c), 0, 0, 0)
#define SBAR() __builtin_amdgcn_sched_barrier(0)
#define RLX_AGENT __ATOMIC_RELAXED, __HIP_MEMORY_SCOPE_AGENT
#define BAR() asm volatile("s_waitcnt lgkmcnt(0)\n\ts_barrier" ::: "memory")

#define MAP_HD(b) (((b) & 7) * 2 + (((b) >> 3) & 1))
#define MAP_QT(b) ((b) >> 4)
constexpr int SEQ = 2048, DM = 448, NH = 16, DH = 28, N3 = 1344;
constexpr float QSCALE = 0.18898223650461362f * 1.4426950408889634f;
constexpr float L_MIN = 8.673617379884035e-19f, L_MAX = 1.152921504606846976e18f;
constexpr unsigned VROW = 456 * 16, TS = 4 * VROW;
constexpr unsigned WS_K = 0, WS_V = 2u << 20, WS_O = 4u << 20, WS_F = 6u << 20;
constexpr unsigned POISON = 0xffffffffu;
constexpr unsigned F_G = 0, F_R = 4096, F_P = 8192, F_KV = 131072;
constexpr unsigned L_WQ = 0, L_RING = 28672, STG = 24576;
constexpr unsigned L_QX = 131072, L_Q2 = L_QX + 8192, L_GB = 152064;
constexpr unsigned L_WO = 0, L_CB = 114688;
constexpr unsigned LDS_BYTES = 163840;


DI unsigned rfl(unsigned x) { return __builtin_amdgcn_readfirstlane(x); }
DI unsigned cvtpk(float lo, float hi) { f32x2 v = {lo, hi}; bf16x2_t b = __builtin_convertvector(v, bf16x2_t); return __builtin_bit_cast(unsigned, b); }
DI u32x4 pack8(const f32x16& x, int s) {
    return (u32x4){cvtpk(x[8 * s], x[8 * s + 1]), cvtpk(x[8 * s + 2], x[8 * s + 3]), cvtpk(x[8 * s + 4], x[8 * s + 5]), cvtpk(x[8 * s + 6], x[8 * s + 7])};
}
DI bf16x8 asbf(u32x4 x) { return __builtin_bit_cast(bf16x8, x); }
DI s16x4 vtr(LAS const char* p) { return __builtin_bit_cast(s16x4, __builtin_amdgcn_ds_read_tr16_b64_v4i16((LAS v4i16_t*)p)); }
DI bf16x8 cat(s16x4 lo, s16x4 hi) { return __builtin_shufflevector(lo, hi, 0, 1, 2, 3, 4, 5, 6, 7); }
DI float half_lo(float x) { auto r = __builtin_amdgcn_permlane32_swap(__float_as_uint(x), __float_as_uint(x), false, false); return __uint_as_float(r[0]); }
DI float half_hi(float x) { auto r = __builtin_amdgcn_permlane32_swap(__float_as_uint(x), __float_as_uint(x), false, false); return __uint_as_float(r[1]); }
DI void dma16(unsigned lds_dst, unsigned voff, __amdgpu_buffer_rsrc_t rs, unsigned soff) {
    asm volatile("s_mov_b32 m0, %0\n\ts_nop 0\n\tbuffer_load_dwordx4 %1, %2, %3 offen lds" ::"s"(lds_dst), "v"(voff), "s"(rs), "s"(soff) : "m0", "memory");
}
DI void st16_wt(u32x4 v, __amdgpu_buffer_rsrc_t rs, unsigned voff, unsigned soff) { __builtin_amdgcn_raw_buffer_store_b128(v, rs, voff, soff, 16); }
DI u32x4 ld16_wt(__amdgpu_buffer_rsrc_t rs, unsigned voff, unsigned soff) { return __builtin_amdgcn_raw_buffer_load_b128(rs, voff, soff, 16); }
DI __amdgpu_buffer_rsrc_t mkrs(const void* p, unsigned bytes) { return __builtin_amdgcn_make_buffer_rsrc((void*)p, (short)0, (int)bytes, 0x00020000); }
DI unsigned poll_tags(const GAS u64* g, unsigned tag) {
    u64 v = __hip_atomic_load(g, RLX_AGENT);
#pragma nounroll
    for (unsigned spins = 0; spins < (1u << 16); ++spins) {
        if (__all((unsigned)v == tag)) break;
        if ((unsigned)v != tag) v = __hip_atomic_load(g, RLX_AGENT);
    }
    __builtin_amdgcn_fence(__ATOMIC_ACQUIRE, "wavefront");
    return (unsigned)(v >> 32);
}
DI unsigned touch(const GAS char* p) { unsigned t; asm volatile("global_load_dword %0, %1, off" : "=v"(t) : "v"(p) : "memory"); return t; }

template <bool ISK>
DI void kv_gemm(LAS char* lds, unsigned w, int lane, bf16x8 (&xf)[7][4], f32x16& a0, f32x16& a1) {
    const int r = lane & 31, h = lane >> 5, i16 = lane & 15, q4 = i16 >> 2, p4 = i16 & 3, g16 = (lane >> 4) & 1;
    const int pc = ISK ? (((p4 & 1) << 1) | (p4 >> 1)) : p4;
    LAS const char* xb = lds + L_RING + w * 4096 + r * 128;
    LAS const char* wb0 = lds + L_RING + 16384 + (8 * h + q4) * 128 + ((q4 >> 1) * 64) + g16 * 32 + pc * 8;
    LAS const char* wb1 = lds + L_RING + 16384 + (8 * h + q4) * 128 + (((q4 >> 1) ^ 1) * 64) + g16 * 32 + pc * 8;
    const int xs = (r >> 1) & 7;
    bf16x8 wf[2][4][2];
#define RD(i) do { const int so = ((i) % 5) * STG; _Pragma("unroll") for (int ks = 0; ks < 4; ++ks) {                          \
        xf[i][ks] = *(LAS const bf16x8*)(xb + so + (((2 * ks + h) ^ xs) * 16));                                                \
        wf[(i) & 1][ks][0] = cat(vtr(wb0 + so + ks * 2048), vtr(wb0 + so + ks * 2048 + 512));                                  \
        wf[(i) & 1][ks][1] = cat(vtr(wb1 + so + ks * 2048), vtr(wb1 + so + ks * 2048 + 512)); } } while (0)
#define MM(i) do { _Pragma("unroll") for (int ks = 0; ks < 4; ++ks) {                                                          \
        if (ISK) { a0 = MFMA(wf[(i) & 1][ks][0], xf[i][ks], a0); a1 = MFMA(wf[(i) & 1][ks][1], xf[i][ks], a1); }               \
        else     { a0 = MFMA(xf[i][ks], wf[(i) & 1][ks][0], a0); a1 = MFMA(xf[i][ks], wf[(i) & 1][ks][1], a1); } } } while (0)
    BAR(); RD(0);
#pragma unroll
    for (int i = 0; i < 5; ++i) { BAR(); RD(i + 1); SBAR(); MM(i); SBAR(); }
    MM(5); SBAR();
    BAR(); RD(6); SBAR();
#pragma unroll
    for (int ks = 0; ks < 4; ++ks) a0 = ISK ? MFMA(wf[0][ks][0], xf[6][ks], a0) : MFMA(xf[6][ks], wf[0][ks][0], a0);
    SBAR();
#pragma unroll
    for (int ks = 0; ks < 4; ++ks) a1 = ISK ? MFMA(wf[0][ks][1], xf[6][ks], a1) : MFMA(xf[6][ks], wf[0][ks][1], a1);
    SBAR();
#undef RD
#undef MM
}

DI void stage_dma(unsigned mw, unsigned mx, unsigned sw, unsigned sx, unsigned wvo, unsigned xvo0, unsigned xvo1, __amdgpu_buffer_rsrc_t rsW,
                  __amdgpu_buffer_rsrc_t rsX) {
    unsigned t;
    asm volatile("s_mov_b32 m0, %1\n\ts_add_u32 %0, %3, 21504\n\tbuffer_load_dwordx4 %5, %8, %3 offen lds\n\t"
                 "s_add_u32 m0, m0, 1024\n\ts_nop 0\n\tbuffer_load_dwordx4 %5, %8, %0 offen lds\n\t"
                 "s_mov_b32 m0, %2\n\ts_add_u32 %0, %4, 7168\n\tbuffer_load_dwordx4 %6, %9, %4 offen lds\n\t"
                 "s_add_u32 m0, m0, 1024\n\ts_nop 0\n\tbuffer_load_dwordx4 %7, %9, %0 offen lds\n\t"
                 "s_add_u32 m0, m0, 1024\n\ts_add_u32 %0, %4, 14336\n\tbuffer_load_dwordx4 %6, %9, %0 offen lds\n\t"
                 "s_add_u32 m0, m0, 1024\n\ts_add_u32 %0, %4, 21504\n\tbuffer_load_dwordx4 %7, %9, %0 offen lds"
                 : "=&s"(t) : "s"(mw), "s"(mx), "s"(sw), "s"(sx), "v"(wvo), "v"(xvo0), "v"(xvo1), "s"(rsW), "s"(rsX) : "m0", "scc", "memory");
}

__global__ __launch_bounds__(512, 2) void mha_kernel(const char* __restrict__ x, const char* __restrict__ wqkv, const char* __restrict__ wo,
                                                     char* __restrict__ out, char* __restrict__ ws) {
    extern __shared__ __attribute__((aligned(16))) char lds_[];
    LAS char* lds = (LAS char*)lds_;
    const unsigned wv = rfl(threadIdx.x >> 6), w = wv & 3;
    const unsigned lds0 = (unsigned)(uintptr_t)lds_;
    GAS char* wsg = (GAS char*)ws;
#define DECODE()                                                                                                              \
    unsigned b = blockIdx.x, lane = threadIdx.x & 63;                                                                        \
    asm volatile("" : "+s"(b)); asm volatile("" : "+v"(lane));                                                                \
    const unsigned hd = MAP_HD(b), qt = MAP_QT(b);                                                                            \
    const unsigned ch = hd < 14 ? hd : hd - 2;         \
    const unsigned rot = (hd + qt) % 7;
#define DECODE2()                                                                                                             \
    const bool isK = ch < 7;                                                                                                  \
    const unsigned g0 = (7 * hd) >> 1;                                                  \
    const unsigned cg = (b >> 3) & 3, q32 = (b & 7) * 8 + (b >> 5);     \
    unsigned kbi[7];                                                                                                          \
    _Pragma("unroll") for (int i = 0; i < 7; ++i) { const unsigned k = rot + i; kbi[i] = k >= 7 ? k - 7 : k; }               \
    const __amdgpu_buffer_rsrc_t rsOI = mkrs(ws + WS_O, 2u << 20);

    if (__builtin_expect(wv >= 4, 1)) {
        DECODE();
        const unsigned xrow = qt * 128 + w * 32 + (lane >> 3);
        const unsigned xvo0 = xrow * (DM * 2) + (((lane & 7) ^ (lane >> 4)) * 16), xvo1 = xvo0 ^ 64;
        const unsigned wvo = (16 * w + (lane >> 3)) * (N3 * 2) + 896 + ch * 128 + (((lane & 7) ^ (((lane >> 4) & 1) << 2)) * 16);
        const __amdgpu_buffer_rsrc_t rsW = mkrs(wqkv, DM * N3 * 2), rsX = mkrs(x, SEQ * DM * 2);
        stage_dma(lds0 + L_RING + 16384 + w * 2048, lds0 + L_RING + w * 4096, rot * (64 * N3 * 2), rot * 128, wvo, xvo0, xvo1, rsW, rsX);
        DECODE2();
        const __amdgpu_buffer_rsrc_t rsF = mkrs(ws + WS_F, 4096);
#define STAGE(i) stage_dma(lds0 + L_RING + ((i) % 5) * STG + 16384 + w * 2048, lds0 + L_RING + ((i) % 5) * STG + w * 4096, kbi[i] * (64 * N3 * 2), kbi[i] * 128, \
                           wvo, xvo0, xvo1, rsW, rsX)
#define VBAR(NV) asm volatile("s_waitcnt vmcnt(" #NV ")\n\ts_barrier" ::: "memory")
        STAGE(1); STAGE(2);
        VBAR(12); STAGE(3);
        VBAR(12); STAGE(4);
        VBAR(12); STAGE(5);
        VBAR(12); STAGE(6);
        VBAR(12);
        unsigned gen;
        { const unsigned vo = F_G; asm volatile("buffer_load_dword %0, %1, %2, 0 offen sc1" : "=v"(gen) : "v"(vo), "s"(rsF) : "memory"); }
        {
            const unsigned qvo = (16 * w + (lane >> 2)) * (N3 * 2) + g0 * 16 + (lane & 3) * 16;
#pragma unroll
            for (int j = 0; j < 7; ++j) dma16(lds0 + L_WQ + (16 * w + 64 * kbi[j]) * 64, qvo, rsW, kbi[j] * 64 * (N3 * 2));
        }
        VBAR(14); VBAR(8);
        asm volatile("s_waitcnt vmcnt(7)" : "+v"(gen)::"memory");
        unsigned tag = rfl(gen);
        { const unsigned t = (tag ^ (tag >> 15)) * 0x2c1b3c6du + 0x9e3779b9u; tag = (t == tag) ? t + 1 : t; }
        const unsigned hh = lane & 15, wh = (lane >> 4) & 1;
        const unsigned c = isK ? (((7 * hh >> 1) + 3 * wh) >> 3) : 7 + ((28 * hh + 27 * wh) >> 6);
        GAS u64* fp = (GAS u64*)(wsg + WS_F + (lane < 32 ? F_KV + ((hh * 4 + (isK ? 0 : 2) + wh) * 16 + qt) * 128 : F_R + (qt * 16 + hd) * 8));
        const bool fon = w == 0 && ((lane < 32 && c == hd) || lane == 32);
        asm volatile("s_waitcnt lgkmcnt(0)\n\ts_barrier" : "+v"(fp)::"memory");
        if (w == 0) {
            if (fon) __hip_atomic_store(fp, ((u64)*(LAS const unsigned*)(lds + L_GB) << 32) | tag, RLX_AGENT);
            asm volatile("s_waitcnt vmcnt(1)" ::: "memory");
        } else {
            asm volatile("s_waitcnt vmcnt(0)" ::: "memory");
        }
        asm volatile("s_waitcnt lgkmcnt(0)\n\ts_barrier" ::: "memory");
        LAS unsigned* ord = (LAS unsigned*)(lds + L_CB + w * 12288 + (w == 2 ? 2048 : 0));
        if (lane < 5) ord[lane] = lane == 4 ? 0x40000000u : 0xffffffffu;
        {
            const unsigned tt = w + 4 * ((((lane >> 2) & 3) + (qt >> 2)) & 3);
            const GAS u64* fl = (const GAS u64*)(wsg + WS_F + F_KV + hd * 8192 + ((lane & 3) * 16 + tt) * 128);
            u64 v = __hip_atomic_load(fl, RLX_AGENT);
            unsigned done = 0;
#pragma nounroll
            for (unsigned spins = 0; done < 4; ++spins) {
                const unsigned miss = spins < (1u << 16) ? (unsigned)__ballot((unsigned)v != tag) & 0xffffu : 0u;
                const unsigned n = miss ? __builtin_ctz(miss) >> 2 : 4;
                if (n > done) {
                    __builtin_amdgcn_fence(__ATOMIC_ACQUIRE, "wavefront");
                    if (lane < 4 * n && (lane & 3) == 0) ord[lane >> 2] = tt * (4 * TS);
                    if (done == 0) {
                        const unsigned g = (unsigned)(v >> 32);
                        if (w == (qt & 3) && lane == 0) *(LAS unsigned*)(lds + L_GB + 16) = max(g, (unsigned)__builtin_amdgcn_readlane((int)g, 1));
                        BAR();
                        asm volatile("s_barrier" ::: "memory");
                    }
                    done = n;
                }
                if ((unsigned)v != tag) v = __hip_atomic_load(fl, RLX_AGENT);
            }
        }
        st16_wt((u32x4){POISON, POISON, POISON, POISON}, rsOI, lane * 16, ((qt * 4 + w) * NH + hd) * 2048);
        st16_wt((u32x4){POISON, POISON, POISON, POISON}, rsOI, lane * 16 + 1024, ((qt * 4 + w) * NH + hd) * 2048);
        asm volatile("s_waitcnt vmcnt(0)" ::: "memory");
        if (lane == 0) __hip_atomic_store((GAS u64*)(wsg + WS_F + F_P + ((qt * 4 + w) * 16 + hd) * 8), (u64)tag, RLX_AGENT);
        if (w == 1 && b == 0) {
            const GAS u64* fr = (const GAS u64*)(wsg + WS_F + F_R) + lane;
#pragma nounroll
            for (unsigned spins = 0; spins < (1u << 16); ++spins) {
                const u64 v0 = __hip_atomic_load(fr, RLX_AGENT), v1 = __hip_atomic_load(fr + 64, RLX_AGENT);
                const u64 v2 = __hip_atomic_load(fr + 128, RLX_AGENT), v3 = __hip_atomic_load(fr + 192, RLX_AGENT);
                if (__all((unsigned)v0 == tag && (unsigned)v1 == tag && (unsigned)v2 == tag && (unsigned)v3 == tag)) break;
                __builtin_amdgcn_s_sleep(4);
            }
            if (lane == 0) __hip_atomic_store((GAS unsigned*)(wsg + WS_F + F_G), tag, RLX_AGENT);
        }
        {
            const __amdgpu_buffer_rsrc_t rsO = mkrs(wo, DM * DM * 2);
            const unsigned vow = (4 * w + (lane >> 4)) * (DM * 2) + cg * 256 + ((((lane >> 2) & 3) ^ (lane >> 4)) * 64) + (lane & 3) * 16;
#pragma unroll 1
            for (unsigned j = 0; j < 28; ++j) { dma16(lds0 + L_WO + (4 * j + w) * 1024, vow, rsO, j * 14336); __builtin_amdgcn_s_sleep(1); }
        }
        (void)poll_tags((const GAS u64*)(wsg + WS_F + F_P + q32 * 128) + w * 4 + (lane & 3), tag);
        VBAR(0);
        asm volatile("s_barrier" ::: "memory");
        {
            unsigned pend = 15;
#pragma nounroll
            for (unsigned spins = 0; pend && spins < (1u << 16); ++spins) {
                u32x4 t[8];
                asm volatile("" ::: "memory");
#pragma unroll
                for (int j = 0; j < 4; ++j)
                    if (pend & (1u << j)) {
                        t[2 * j] = ld16_wt(rsOI, lane * 16 + j * 2048, q32 * (NH * 2048) + w * 8192);
                        t[2 * j + 1] = ld16_wt(rsOI, lane * 16 + j * 2048 + 1024, q32 * (NH * 2048) + w * 8192);
                    }
#pragma unroll
                for (int j = 0; j < 4; ++j)
                    if (pend & (1u << j)) {
                        const u32x4 u = t[2 * j], v = t[2 * j + 1];
                        *(LAS u32x4*)(lds + L_CB + (w * 8 + 2 * j) * 1024 + lane * 16) = u;
                        *(LAS u32x4*)(lds + L_CB + (w * 8 + 2 * j + 1) * 1024 + lane * 16) = v;
                        const unsigned m = max(max(max(u[0], u[1]), max(u[2], u[3])), max(max(v[0], v[1]), max(v[2], v[3])));
                        if (__all(m != POISON)) pend &= ~(1u << j);
                    }
            }
        }
        BAR();
        return;
    }

    DECODE(); DECODE2();
    const int r = lane & 31, h = lane >> 5, i16 = lane & 15, q4 = i16 >> 2, p4 = i16 & 3, g16 = (lane >> 4) & 1;
    f32x16 a0 = {}, a1 = {};
    bf16x8 xf[7][4];
    const unsigned kt = qt * 4 + w;
    unsigned tch1;
    {
        unsigned long long pc0;
        unsigned len;
        asm volatile("s_getpc_b64 %0\nMHA_PF%=:\n\ts_mov_b32 %1, MHA_CODE_END - MHA_PF%=" : "=s"(pc0), "=s"(len));
        tch1 = touch((const GAS char*)(pc0 & ~3ull) + min((((b >> 3) & 3) * 4 + w) * 1024 + (lane & 7) * 128, len));
    }
    if (threadIdx.x == 0) *(LAS unsigned*)(lds + L_GB) = 0;
    unsigned vok = WS_K + (g0 + h) * 512 + r * 16, vov = r <= DH ? WS_V + h * VROW + (r < DH ? DH * hd + r : 448) * 16 : 0x70000000u;
    const __amdgpu_buffer_rsrc_t rsKVr = mkrs(ws, 4u << 20);
    unsigned q2a = lds0 + L_Q2 + r * 16, kba = lds0 + L_GB + 16, ordn = lds0 + L_CB + w * 12288 + (w == 2 ? 2048 : 0) + 4, puta = lds0 + L_CB + w * 12288 + lane * 16;
    unsigned qxa[4];
#pragma unroll
    for (int i = 0; i < 4; ++i) qxa[i] = lds0 + L_QX + ((w + 1 + i) & 3) * 2048 + lane * 16;
    LAS const char* qb = lds + L_WQ + (8 * h + q4) * 64 + g16 * 32 + (((p4 & 1) << 1) | (p4 >> 1)) * 8;
    asm volatile("" : "+v"(vok), "+v"(vov), "+v"(q2a), "+v"(kba), "+v"(ordn), "+v"(puta), "+v"(qxa[0]), "+v"(qxa[1]), "+v"(qxa[2]), "+v"(qxa[3]), "+v"(qb));
    const __amdgpu_buffer_rsrc_t rsKV = mkrs(ws, hd < 14 ? 4u << 20 : 0u);
    if (isK) {
        unsigned vo = WS_K + (8 * ch + h) * 512 + r * 16;
        asm volatile("" : "+v"(vo));
        kv_gemm<true>(lds, w, lane, xf, a0, a1);
        st16_wt(pack8(a0, 0), rsKV, vo, kt * TS); st16_wt(pack8(a0, 1), rsKV, vo + 1024, kt * TS);
        st16_wt(pack8(a1, 0), rsKV, vo + 2048, kt * TS); st16_wt(pack8(a1, 1), rsKV, vo + 3072, kt * TS);
        SBAR();
        float s0 = 0.f, s1 = 0.f, s2 = 0.f, s3 = 0.f;
#pragma unroll
        for (int j = 0; j < 8; ++j) { s0 += a0[j] * a0[j]; s1 += a0[8 + j] * a0[8 + j]; s2 += a1[j] * a1[j]; s3 += a1[8 + j] * a1[8 + j]; }
        asm volatile("ds_max_u32 %0, %1" ::"v"(lds0 + L_GB), "v"(max(max(__float_as_uint(s0), __float_as_uint(s1)), max(__float_as_uint(s2), __float_as_uint(s3)))) : "memory");
    } else {
        unsigned v0 = WS_V + h * VROW + (64 * (ch - 7) + r) * 16, v2 = v0 + 2 * VROW;
        if (lane < 32)
            st16_wt((u32x4){0x3f803f80u, 0x3f803f80u, 0x3f803f80u, 0x3f803f80u}, rsKV, WS_V + (lane >> 3) * VROW + (448 + (lane & 7)) * 16, kt * TS);
        asm volatile("" : "+v"(v0), "+v"(v2));
        kv_gemm<false>(lds, w, lane, xf, a0, a1);
        st16_wt(pack8(a0, 0), rsKV, v0, kt * TS); st16_wt(pack8(a0, 1), rsKV, v2, kt * TS);
        st16_wt(pack8(a1, 0), rsKV, v0 + 512, kt * TS); st16_wt(pack8(a1, 1), rsKV, v2 + 512, kt * TS);
        SBAR();
    }
    asm volatile("s_waitcnt vmcnt(0) lgkmcnt(0)" : "+v"(tch1)::"memory");
    asm volatile("s_barrier" ::: "memory");
    asm volatile("s_barrier" ::: "memory");
    f32x16 aq = {};
    {
        bf16x8 wq[3][4];
#define RDQ(i) do { LAS const char* qk = qb + kbi[i] * 4096; _Pragma("unroll") for (int ks = 0; ks < 4; ++ks)                  \
            wq[(i) % 3][ks] = cat(vtr(qk + ks * 1024), vtr(qk + ks * 1024 + 256)); } while (0)
        RDQ(0); RDQ(1); SBAR();
#pragma unroll
        for (int i = 0; i < 7; ++i) {
            if (i + 2 < 7) RDQ(i + 2);
            SBAR();
#pragma unroll
            for (int ks = 0; ks < 4; ++ks) aq = MFMA(wq[i % 3][ks], xf[i][ks], aq);
            SBAR();
        }
#undef RDQ
    }
    {
        const bool zlo = (hd & 1) && !h, zhi = !(hd & 1) && h;
        float qs[4] = {0.f, 0.f, 0.f, 0.f};
#pragma unroll
        for (int i = 0; i < 16; ++i) {
            aq[i] *= QSCALE;
            if (i < 4) aq[i] = zlo ? 0.f : aq[i];
            if (i >= 12) aq[i] = zhi ? 0.f : aq[i];
            qs[i & 3] += aq[i] * aq[i];
        }
        *(LAS u32x4*)(lds + (qxa[3] - lds0)) = pack8(aq, 0);
        *(LAS u32x4*)(lds + (qxa[3] - lds0) + 1024) = pack8(aq, 1);
        float q2 = (qs[0] + qs[1]) + (qs[2] + qs[3]);
        q2 = half_lo(q2) + half_hi(q2);
        *(LAS float*)(lds + (q2a - lds0) + w * 4) = q2;
    }

    f32x16 acc;
    u32x4 qo0, qo1;
    {
        unsigned sb = (w + (qt & 12)) * (4 * TS), cnt = 4;
        asm volatile(
        "s_setprio 2\n\t"
        "v_mov_b64 v[0:1], 0\n\t"
        "v_mov_b64 v[2:3], 0\n\t"
        "v_mov_b64 v[4:5], 0\n\t"
        "v_mov_b64 v[6:7], 0\n\t"
        "v_mov_b64 v[8:9], 0\n\t"
        "v_mov_b64 v[10:11], 0\n\t"
        "v_mov_b64 v[12:13], 0\n\t"
        "v_mov_b64 v[14:15], 0\n\t"
        "v_mov_b64 v[16:17], 0\n\t"
        "v_mov_b64 v[18:19], 0\n\t"
        "v_mov_b64 v[20:21], 0\n\t"
        "v_mov_b64 v[22:23], 0\n\t"
        "v_mov_b64 v[24:25], 0\n\t"
        "v_mov_b64 v[26:27], 0\n\t"
        "v_mov_b64 v[28:29], 0\n\t"
        "v_mov_b64 v[30:31], 0\n\t"
        "v_mov_b64 v[32:33], 0\n\t"
        "v_mov_b64 v[34:35], 0\n\t"
        "v_mov_b64 v[36:37], 0\n\t"
        "v_mov_b64 v[38:39], 0\n\t"
        "v_mov_b64 v[40:41], 0\n\t"
        "v_mov_b64 v[42:43], 0\n\t"
        "v_mov_b64 v[44:45], 0\n\t"
        "v_mov_b64 v[46:47], 0\n\t"
        "v_mov_b64 v[48:49], 0\n\t"
        "v_mov_b64 v[50:51], 0\n\t"
        "v_mov_b64 v[52:53], 0\n\t"
        "v_mov_b64 v[54:55], 0\n\t"
        "v_mov_b64 v[56:57], 0\n\t"
        "v_mov_b64 v[58:59], 0\n\t"
        "v_mov_b64 v[60:61], 0\n\t"
        "v_mov_b64 v[62:63], 0\n\t"
        "v_mov_b64 v[152:153], 0\n\t"
        "v_mov_b64 v[154:155], 0\n\t"
        "v_mov_b64 v[156:157], 0\n\t"
        "v_mov_b64 v[158:159], 0\n\t"
        "v_mov_b64 v[216:217], 0\n\t"
        "v_mov_b64 v[218:219], 0\n\t"
        "v_mov_b64 v[220:221], 0\n\t"
        "v_mov_b64 v[222:223], 0\n\t"
        "s_waitcnt lgkmcnt(0)\n\t"
        "s_barrier\n\t"
        "buffer_load_dwordx4 v[160:163], v224, %[rs], %[sb] offen sc1\n\t"
        "buffer_load_dwordx4 v[164:167], v224, %[rs], %[sb] offen offset:1024 sc1\n\t"
        "buffer_load_dwordx4 v[168:171], v225, %[rs], %[sb] offen sc1\n\t"
        "buffer_load_dwordx4 v[172:175], v226, %[rs], %[sb] offen sc1\n\t"
        "ds_read_b128 v[80:83], v236\n\t"
        "ds_read_b32 v237, v237\n\t"
        "ds_read_b128 v[112:115], v241\n\t"
        "ds_read_b128 v[116:119], v241 offset:1024\n\t"
        "ds_read_b128 v[120:123], v242\n\t"
        "ds_read_b128 v[124:127], v242 offset:1024\n\t"
        "ds_read_b128 v[128:131], v243\n\t"
        "ds_read_b128 v[132:135], v243 offset:1024\n\t"
        "ds_read_b128 v[136:139], v244\n\t"
        "ds_read_b128 v[140:143], v244 offset:1024\n\t"
        "s_waitcnt lgkmcnt(0)\n\t"
        "s_barrier\n\t"
        "v_max3_f32 v236, v80, v81, v82\n\t"
        "v_max_f32 v236, v236, v83\n\t"
        "v_mul_f32 v236, v236, v237\n\t"
        "v_sqrt_f32 v236, v236\n\t"
        "s_nop 0\n\t"
        "v_mul_f32 v96, -0.5, v236\n\t"
        "v_mov_b32 v97, v96\n\t"
        "v_mov_b64 v[98:99], v[96:97]\n\t"
        "v_mov_b64 v[100:101], v[96:97]\n\t"
        "v_mov_b64 v[102:103], v[96:97]\n\t"
        "v_mov_b64 v[104:105], v[96:97]\n\t"
        "v_mov_b64 v[106:107], v[96:97]\n\t"
        "v_mov_b64 v[108:109], v[96:97]\n\t"
        "v_mov_b64 v[110:111], v[96:97]\n\t"
        "buffer_load_dwordx4 v[176:179], v227, %[rs], %[sb] offen sc1\n\t"
        "buffer_load_dwordx4 v[180:183], v227, %[rs], %[sb] offen offset:1024 sc1\n\t"
        "buffer_load_dwordx4 v[184:187], v228, %[rs], %[sb] offen sc1\n\t"
        "buffer_load_dwordx4 v[188:191], v229, %[rs], %[sb] offen sc1\n\t"
        "s_waitcnt vmcnt(4)\n\t"
        "v_mfma_f32_32x32x16_bf16 v[64:79], v[160:163], v[112:115], v[96:111]\n\t"
        "v_mfma_f32_32x32x16_bf16 v[64:79], v[164:167], v[116:119], v[64:79]\n\t"
        "buffer_load_dwordx4 v[192:195], v230, %[rs], %[sb] offen sc1\n\t"
        "buffer_load_dwordx4 v[196:199], v230, %[rs], %[sb] offen offset:1024 sc1\n\t"
        "buffer_load_dwordx4 v[200:203], v231, %[rs], %[sb] offen sc1\n\t"
        "buffer_load_dwordx4 v[204:207], v232, %[rs], %[sb] offen sc1\n\t"
        "s_nop 9\n\t"
        "LOOP%=:\n\t"
        "v_mfma_f32_32x32x16_bf16 v[80:95], v[160:163], v[120:123], v[96:111]\n\t"
        "v_exp_f32 v64, v64\n\t"
        "v_exp_f32 v65, v65\n\t"
        "v_exp_f32 v66, v66\n\t"
        "v_exp_f32 v67, v67\n\t"
        "v_mfma_f32_32x32x16_bf16 v[80:95], v[164:167], v[124:127], v[80:95]\n\t"
        "v_exp_f32 v68, v68\n\t"
        "v_exp_f32 v69, v69\n\t"
        "v_exp_f32 v70, v70\n\t"
        "v_exp_f32 v71, v71\n\t"
        "v_cvt_pk_bf16_f32 v144, v64, v65\n\t"
        "v_cvt_pk_bf16_f32 v145, v66, v67\n\t"
        "v_mfma_f32_32x32x16_bf16 v[48:63], v[216:219], v[152:155], v[48:63]\n\t"
        "v_exp_f32 v72, v72\n\t"
        "v_exp_f32 v73, v73\n\t"
        "v_exp_f32 v74, v74\n\t"
        "v_exp_f32 v75, v75\n\t"
        "v_cvt_pk_bf16_f32 v146, v68, v69\n\t"
        "v_cvt_pk_bf16_f32 v147, v70, v71\n\t"
        "v_mfma_f32_32x32x16_bf16 v[48:63], v[220:223], v[156:159], v[48:63]\n\t"
        "v_exp_f32 v76, v76\n\t"
        "v_exp_f32 v77, v77\n\t"
        "v_exp_f32 v78, v78\n\t"
        "v_exp_f32 v79, v79\n\t"
        "v_cvt_pk_bf16_f32 v148, v72, v73\n\t"
        "v_cvt_pk_bf16_f32 v149, v74, v75\n\t"
        "v_cvt_pk_bf16_f32 v150, v76, v77\n\t"
        "v_cvt_pk_bf16_f32 v151, v78, v79\n\t"
        "v_mfma_f32_32x32x16_bf16 v[64:79], v[160:163], v[128:131], v[96:111]\n\t"
        "buffer_load_dwordx4 v[208:211], v233, %[rs], %[sb] offen sc1\n\t"
        "buffer_load_dwordx4 v[212:215], v233, %[rs], %[sb] offen offset:1024 sc1\n\t"
        "buffer_load_dwordx4 v[216:219], v234, %[rs], %[sb] offen sc1\n\t"
        "buffer_load_dwordx4 v[220:223], v235, %[rs], %[sb] offen sc1\n\t"
        "v_exp_f32 v80, v80\n\t"
        "v_exp_f32 v81, v81\n\t"
        "v_exp_f32 v82, v82\n\t"
        "v_exp_f32 v83, v83\n\t"
        "v_mfma_f32_32x32x16_bf16 v[64:79], v[164:167], v[132:135], v[64:79]\n\t"
        "v_exp_f32 v84, v84\n\t"
        "v_exp_f32 v85, v85\n\t"
        "v_exp_f32 v86, v86\n\t"
        "v_exp_f32 v87, v87\n\t"
        "v_cvt_pk_bf16_f32 v152, v80, v81\n\t"
        "v_cvt_pk_bf16_f32 v153, v82, v83\n\t"
        "v_mfma_f32_32x32x16_bf16 v[0:15], v[168:171], v[144:147], v[0:15]\n\t"
        "v_exp_f32 v88, v88\n\t"
        "v_exp_f32 v89, v89\n\t"
        "v_exp_f32 v90, v90\n\t"
        "v_exp_f32 v91, v91\n\t"
        "v_cvt_pk_bf16_f32 v154, v84, v85\n\t"
        "v_cvt_pk_bf16_f32 v155, v86, v87\n\t"
        "v_mfma_f32_32x32x16_bf16 v[0:15], v[172:175], v[148:151], v[0:15]\n\t"
        "v_exp_f32 v92, v92\n\t"
        "v_exp_f32 v93, v93\n\t"
        "v_exp_f32 v94, v94\n\t"
        "v_exp_f32 v95, v95\n\t"
        "v_cvt_pk_bf16_f32 v156, v88, v89\n\t"
        "v_cvt_pk_bf16_f32 v157, v90, v91\n\t"
        "v_cvt_pk_bf16_f32 v158, v92, v93\n\t"
        "v_cvt_pk_bf16_f32 v159, v94, v95\n\t"
        "v_mfma_f32_32x32x16_bf16 v[80:95], v[160:163], v[136:139], v[96:111]\n\t"
        "ds_read_b32 v239, v238\n\t"
        "v_exp_f32 v64, v64\n\t"
        "v_exp_f32 v65, v65\n\t"
        "v_exp_f32 v66, v66\n\t"
        "v_exp_f32 v67, v67\n\t"
        "v_mfma_f32_32x32x16_bf16 v[80:95], v[164:167], v[140:143], v[80:95]\n\t"
        "v_exp_f32 v68, v68\n\t"
        "v_exp_f32 v69, v69\n\t"
        "v_exp_f32 v70, v70\n\t"
        "v_exp_f32 v71, v71\n\t"
        "v_cvt_pk_bf16_f32 v144, v64, v65\n\t"
        "v_cvt_pk_bf16_f32 v145, v66, v67\n\t"
        "v_mfma_f32_32x32x16_bf16 v[16:31], v[168:171], v[152:155], v[16:31]\n\t"
        "v_exp_f32 v72, v72\n\t"
        "v_exp_f32 v73, v73\n\t"
        "v_exp_f32 v74, v74\n\t"
        "v_exp_f32 v75, v75\n\t"
        "v_cvt_pk_bf16_f32 v146, v68, v69\n\t"
        "v_cvt_pk_bf16_f32 v147, v70, v71\n\t"
        "v_mfma_f32_32x32x16_bf16 v[16:31], v[172:175], v[156:159], v[16:31]\n\t"
        "v_exp_f32 v76, v76\n\t"
        "v_exp_f32 v77, v77\n\t"
        "v_exp_f32 v78, v78\n\t"
        "v_exp_f32 v79, v79\n\t"
        "v_cvt_pk_bf16_f32 v148, v72, v73\n\t"
        "v_cvt_pk_bf16_f32 v149, v74, v75\n\t"
        "v_cvt_pk_bf16_f32 v150, v76, v77\n\t"
        "v_cvt_pk_bf16_f32 v151, v78, v79\n\t"
        "s_waitcnt vmcnt(8)\n\t"
        "v_mfma_f32_32x32x16_bf16 v[64:79], v[176:179], v[112:115], v[96:111]\n\t"
        "v_exp_f32 v80, v80\n\t"
        "v_exp_f32 v81, v81\n\t"
        "v_exp_f32 v82, v82\n\t"
        "v_exp_f32 v83, v83\n\t"
        "v_mfma_f32_32x32x16_bf16 v[64:79], v[180:183], v[116:119], v[64:79]\n\t"
        "v_exp_f32 v84, v84\n\t"
        "v_exp_f32 v85, v85\n\t"
        "v_exp_f32 v86, v86\n\t"
        "v_exp_f32 v87, v87\n\t"
        "v_cvt_pk_bf16_f32 v152, v80, v81\n\t"
        "v_cvt_pk_bf16_f32 v153, v82, v83\n\t"
        "v_mfma_f32_32x32x16_bf16 v[32:47], v[168:171], v[144:147], v[32:47]\n\t"
        "v_exp_f32 v88, v88\n\t"
        "v_exp_f32 v89, v89\n\t"
        "v_exp_f32 v90, v90\n\t"
        "v_exp_f32 v91, v91\n\t"
        "v_cvt_pk_bf16_f32 v154, v84, v85\n\t"
        "v_cvt_pk_bf16_f32 v155, v86, v87\n\t"
        "v_mfma_f32_32x32x16_bf16 v[32:47], v[172:175], v[148:151], v[32:47]\n\t"
        "v_exp_f32 v92, v92\n\t"
        "v_exp_f32 v93, v93\n\t"
        "v_exp_f32 v94, v94\n\t"
        "v_exp_f32 v95, v95\n\t"
        "v_cvt_pk_bf16_f32 v156, v88, v89\n\t"
        "v_cvt_pk_bf16_f32 v157, v90, v91\n\t"
        "v_cvt_pk_bf16_f32 v158, v92, v93\n\t"
        "v_cvt_pk_bf16_f32 v159, v94, v95\n\t"
        "v_mfma_f32_32x32x16_bf16 v[80:95], v[176:179], v[120:123], v[96:111]\n\t"
        "CHK%=:\n\t"
        "s_waitcnt lgkmcnt(0)\n\t"
        "v_readfirstlane_b32 %[sb], v239\n\t"
        "ds_read_b32 v239, v238\n\t"
        "s_cmp_eq_u32 %[sb], -1\n\t"
        "s_cbranch_scc1 CHK%=\n\t"
        "v_add_u32 v238, 4, v238\n\t"
        "v_exp_f32 v64, v64\n\t"
        "v_exp_f32 v65, v65\n\t"
        "v_exp_f32 v66, v66\n\t"
        "v_exp_f32 v67, v67\n\t"
        "v_mfma_f32_32x32x16_bf16 v[80:95], v[180:183], v[124:127], v[80:95]\n\t"
        "v_exp_f32 v68, v68\n\t"
        "v_exp_f32 v69, v69\n\t"
        "v_exp_f32 v70, v70\n\t"
        "v_exp_f32 v71, v71\n\t"
        "v_cvt_pk_bf16_f32 v144, v64, v65\n\t"
        "v_cvt_pk_bf16_f32 v145, v66, v67\n\t"
        "v_mfma_f32_32x32x16_bf16 v[48:63], v[168:171], v[152:155], v[48:63]\n\t"
        "v_exp_f32 v72, v72\n\t"
        "v_exp_f32 v73, v73\n\t"
        "v_exp_f32 v74, v74\n\t"
        "v_exp_f32 v75, v75\n\t"
        "v_cvt_pk_bf16_f32 v146, v68, v69\n\t"
        "v_cvt_pk_bf16_f32 v147, v70, v71\n\t"
        "v_mfma_f32_32x32x16_bf16 v[48:63], v[172:175], v[156:159], v[48:63]\n\t"
        "v_exp_f32 v76, v76\n\t"
        "v_exp_f32 v77, v77\n\t"
        "v_exp_f32 v78, v78\n\t"
        "v_exp_f32 v79, v79\n\t"
        "v_cvt_pk_bf16_f32 v148, v72, v73\n\t"
        "v_cvt_pk_bf16_f32 v149, v74, v75\n\t"
        "v_cvt_pk_bf16_f32 v150, v76, v77\n\t"
        "v_cvt_pk_bf16_f32 v151, v78, v79\n\t"
        "v_mfma_f32_32x32x16_bf16 v[64:79], v[176:179], v[128:131], v[96:111]\n\t"
        "buffer_load_dwordx4 v[160:163], v224, %[rs], %[sb] offen sc1\n\t"
        "buffer_load_dwordx4 v[164:167], v224, %[rs], %[sb] offen offset:1024 sc1\n\t"
        "buffer_load_dwordx4 v[168:171], v225, %[rs], %[sb] offen sc1\n\t"
        "buffer_load_dwordx4 v[172:175], v226, %[rs], %[sb] offen sc1\n\t"
        "v_exp_f32 v80, v80\n\t"
        "v_exp_f32 v81, v81\n\t"
        "v_exp_f32 v82, v82\n\t"
        "v_exp_f32 v83, v83\n\t"
        "v_mfma_f32_32x32x16_bf16 v[64:79], v[180:183], v[132:135], v[64:79]\n\t"
        "v_exp_f32 v84, v84\n\t"
        "v_exp_f32 v85, v85\n\t"
        "v_exp_f32 v86, v86\n\t"
        "v_exp_f32 v87, v87\n\t"
        "v_cvt_pk_bf16_f32 v152, v80, v81\n\t"
        "v_cvt_pk_bf16_f32 v153, v82, v83\n\t"
        "v_mfma_f32_32x32x16_bf16 v[0:15], v[184:187], v[144:147], v[0:15]\n\t"
        "v_exp_f32 v88, v88\n\t"
        "v_exp_f32 v89, v89\n\t"
        "v_exp_f32 v90, v90\n\t"
        "v_exp_f32 v91, v91\n\t"
        "v_cvt_pk_bf16_f32 v154, v84, v85\n\t"
        "v_cvt_pk_bf16_f32 v155, v86, v87\n\t"
        "v_mfma_f32_32x32x16_bf16 v[0:15], v[188:191], v[148:151], v[0:15]\n\t"
        "v_exp_f32 v92, v92\n\t"
        "v_exp_f32 v93, v93\n\t"
        "v_exp_f32 v94, v94\n\t"
        "v_exp_f32 v95, v95\n\t"
        "v_cvt_pk_bf16_f32 v156, v88, v89\n\t"
        "v_cvt_pk_bf16_f32 v157, v90, v91\n\t"
        "v_cvt_pk_bf16_f32 v158, v92, v93\n\t"
        "v_cvt_pk_bf16_f32 v159, v94, v95\n\t"
        "v_mfma_f32_32x32x16_bf16 v[80:95], v[176:179], v[136:139], v[96:111]\n\t"
        "v_exp_f32 v64, v64\n\t"
        "v_exp_f32 v65, v65\n\t"
        "v_exp_f32 v66, v66\n\t"
        "v_exp_f32 v67, v67\n\t"
        "v_mfma_f32_32x32x16_bf16 v[80:95], v[180:183], v[140:143], v[80:95]\n\t"
        "v_exp_f32 v68, v68\n\t"
        "v_exp_f32 v69, v69\n\t"
        "v_exp_f32 v70, v70\n\t"
        "v_exp_f32 v71, v71\n\t"
        "v_cvt_pk_bf16_f32 v144, v64, v65\n\t"
        "v_cvt_pk_bf16_f32 v145, v66, v67\n\t"
        "v_mfma_f32_32x32x16_bf16 v[16:31], v[184:187], v[152:155], v[16:31]\n\t"
        "v_exp_f32 v72, v72\n\t"
        "v_exp_f32 v73, v73\n\t"
        "v_exp_f32 v74, v74\n\t"
        "v_exp_f32 v75, v75\n\t"
        "v_cvt_pk_bf16_f32 v146, v68, v69\n\t"
        "v_cvt_pk_bf16_f32 v147, v70, v71\n\t"
        "v_mfma_f32_32x32x16_bf16 v[16:31], v[188:191], v[156:159], v[16:31]\n\t"
        "v_exp_f32 v76, v76\n\t"
        "v_exp_f32 v77, v77\n\t"
        "v_exp_f32 v78, v78\n\t"
        "v_exp_f32 v79, v79\n\t"
        "v_cvt_pk_bf16_f32 v148, v72, v73\n\t"
        "v_cvt_pk_bf16_f32 v149, v74, v75\n\t"
        "v_cvt_pk_bf16_f32 v150, v76, v77\n\t"
        "v_cvt_pk_bf16_f32 v151, v78, v79\n\t"
        "s_waitcnt vmcnt(8)\n\t"
        "v_mfma_f32_32x32x16_bf16 v[64:79], v[192:195], v[112:115], v[96:111]\n\t"
        "v_exp_f32 v80, v80\n\t"
        "v_exp_f32 v81, v81\n\t"
        "v_exp_f32 v82, v82\n\t"
        "v_exp_f32 v83, v83\n\t"
        "v_mfma_f32_32x32x16_bf16 v[64:79], v[196:199], v[116:119], v[64:79]\n\t"
        "v_exp_f32 v84, v84\n\t"
        "v_exp_f32 v85, v85\n\t"
        "v_exp_f32 v86, v86\n\t"
        "v_exp_f32 v87, v87\n\t"
        "v_cvt_pk_bf16_f32 v152, v80, v81\n\t"
        "v_cvt_pk_bf16_f32 v153, v82, v83\n\t"
        "v_mfma_f32_32x32x16_bf16 v[32:47], v[184:187], v[144:147], v[32:47]\n\t"
        "v_exp_f32 v88, v88\n\t"
        "v_exp_f32 v89, v89\n\t"
        "v_exp_f32 v90, v90\n\t"
        "v_exp_f32 v91, v91\n\t"
        "v_cvt_pk_bf16_f32 v154, v84, v85\n\t"
        "v_cvt_pk_bf16_f32 v155, v86, v87\n\t"
        "v_mfma_f32_32x32x16_bf16 v[32:47], v[188:191], v[148:151], v[32:47]\n\t"
        "v_exp_f32 v92, v92\n\t"
        "v_exp_f32 v93, v93\n\t"
        "v_exp_f32 v94, v94\n\t"
        "v_exp_f32 v95, v95\n\t"
        "v_cvt_pk_bf16_f32 v156, v88, v89\n\t"
        "v_cvt_pk_bf16_f32 v157, v90, v91\n\t"
        "v_cvt_pk_bf16_f32 v158, v92, v93\n\t"
        "v_cvt_pk_bf16_f32 v159, v94, v95\n\t"
        "v_mfma_f32_32x32x16_bf16 v[80:95], v[192:195], v[120:123], v[96:111]\n\t"
        "v_exp_f32 v64, v64\n\t"
        "v_exp_f32 v65, v65\n\t"
        "v_exp_f32 v66, v66\n\t"
        "v_exp_f32 v67, v67\n\t"
        "v_mfma_f32_32x32x16_bf16 v[80:95], v[196:199], v[124:127], v[80:95]\n\t"
        "v_exp_f32 v68, v68\n\t"
        "v_exp_f32 v69, v69\n\t"
        "v_exp_f32 v70, v70\n\t"
        "v_exp_f32 v71, v71\n\t"
        "v_cvt_pk_bf16_f32 v144, v64, v65\n\t"
        "v_cvt_pk_bf16_f32 v145, v66, v67\n\t"
        "v_mfma_f32_32x32x16_bf16 v[48:63], v[184:187], v[152:155], v[48:63]\n\t"
        "v_exp_f32 v72, v72\n\t"
        "v_exp_f32 v73, v73\n\t"
        "v_exp_f32 v74, v74\n\t"
        "v_exp_f32 v75, v75\n\t"
        "v_cvt_pk_bf16_f32 v146, v68, v69\n\t"
        "v_cvt_pk_bf16_f32 v147, v70, v71\n\t"
        "v_mfma_f32_32x32x16_bf16 v[48:63], v[188:191], v[156:159], v[48:63]\n\t"
        "v_exp_f32 v76, v76\n\t"
        "v_exp_f32 v77, v77\n\t"
        "v_exp_f32 v78, v78\n\t"
        "v_exp_f32 v79, v79\n\t"
        "v_cvt_pk_bf16_f32 v148, v72, v73\n\t"
        "v_cvt_pk_bf16_f32 v149, v74, v75\n\t"
        "v_cvt_pk_bf16_f32 v150, v76, v77\n\t"
        "v_cvt_pk_bf16_f32 v151, v78, v79\n\t"
        "v_mfma_f32_32x32x16_bf16 v[64:79], v[192:195], v[128:131], v[96:111]\n\t"
        "buffer_load_dwordx4 v[176:179], v227, %[rs], %[sb] offen sc1\n\t"
        "buffer_load_dwordx4 v[180:183], v227, %[rs], %[sb] offen offset:1024 sc1\n\t"
        "buffer_load_dwordx4 v[184:187], v228, %[rs], %[sb] offen sc1\n\t"
        "buffer_load_dwordx4 v[188:191], v229, %[rs], %[sb] offen sc1\n\t"
        "v_exp_f32 v80, v80\n\t"
        "v_exp_f32 v81, v81\n\t"
        "v_exp_f32 v82, v82\n\t"
        "v_exp_f32 v83, v83\n\t"
        "v_mfma_f32_32x32x16_bf16 v[64:79], v[196:199], v[132:135], v[64:79]\n\t"
        "v_exp_f32 v84, v84\n\t"
        "v_exp_f32 v85, v85\n\t"
        "v_exp_f32 v86, v86\n\t"
        "v_exp_f32 v87, v87\n\t"
        "v_cvt_pk_bf16_f32 v152, v80, v81\n\t"
        "v_cvt_pk_bf16_f32 v153, v82, v83\n\t"
        "v_mfma_f32_32x32x16_bf16 v[0:15], v[200:203], v[144:147], v[0:15]\n\t"
        "v_exp_f32 v88, v88\n\t"
        "v_exp_f32 v89, v89\n\t"
        "v_exp_f32 v90, v90\n\t"
        "v_exp_f32 v91, v91\n\t"
        "v_cvt_pk_bf16_f32 v154, v84, v85\n\t"
        "v_cvt_pk_bf16_f32 v155, v86, v87\n\t"
        "v_mfma_f32_32x32x16_bf16 v[0:15], v[204:207], v[148:151], v[0:15]\n\t"
        "v_exp_f32 v92, v92\n\t"
        "v_exp_f32 v93, v93\n\t"
        "v_exp_f32 v94, v94\n\t"
        "v_exp_f32 v95, v95\n\t"
        "v_cvt_pk_bf16_f32 v156, v88, v89\n\t"
        "v_cvt_pk_bf16_f32 v157, v90, v91\n\t"
        "v_cvt_pk_bf16_f32 v158, v92, v93\n\t"
        "v_cvt_pk_bf16_f32 v159, v94, v95\n\t"
        "v_mfma_f32_32x32x16_bf16 v[80:95], v[192:195], v[136:139], v[96:111]\n\t"
        "v_exp_f32 v64, v64\n\t"
        "v_exp_f32 v65, v65\n\t"
        "v_exp_f32 v66, v66\n\t"
        "v_exp_f32 v67, v67\n\t"
        "v_mfma_f32_32x32x16_bf16 v[80:95], v[196:199], v[140:143], v[80:95]\n\t"
        "v_exp_f32 v68, v68\n\t"
        "v_exp_f32 v69, v69\n\t"
        "v_exp_f32 v70, v70\n\t"
        "v_exp_f32 v71, v71\n\t"
        "v_cvt_pk_bf16_f32 v144, v64, v65\n\t"
        "v_cvt_pk_bf16_f32 v145, v66, v67\n\t"
        "v_mfma_f32_32x32x16_bf16 v[16:31], v[200:203], v[152:155], v[16:31]\n\t"
        "v_exp_f32 v72, v72\n\t"
        "v_exp_f32 v73, v73\n\t"
        "v_exp_f32 v74, v74\n\t"
        "v_exp_f32 v75, v75\n\t"
        "v_cvt_pk_bf16_f32 v146, v68, v69\n\t"
        "v_cvt_pk_bf16_f32 v147, v70, v71\n\t"
        "v_mfma_f32_32x32x16_bf16 v[16:31], v[204:207], v[156:159], v[16:31]\n\t"
        "v_exp_f32 v76, v76\n\t"
        "v_exp_f32 v77, v77\n\t"
        "v_exp_f32 v78, v78\n\t"
        "v_exp_f32 v79, v79\n\t"
        "v_cvt_pk_bf16_f32 v148, v72, v73\n\t"
        "v_cvt_pk_bf16_f32 v149, v74, v75\n\t"
        "v_cvt_pk_bf16_f32 v150, v76, v77\n\t"
        "v_cvt_pk_bf16_f32 v151, v78, v79\n\t"
        "s_waitcnt vmcnt(8)\n\t"
        "v_mfma_f32_32x32x16_bf16 v[64:79], v[208:211], v[112:115], v[96:111]\n\t"
        "v_exp_f32 v80, v80\n\t"
        "v_exp_f32 v81, v81\n\t"
        "v_exp_f32 v82, v82\n\t"
        "v_exp_f32 v83, v83\n\t"
        "v_mfma_f32_32x32x16_bf16 v[64:79], v[212:215], v[116:119], v[64:79]\n\t"
        "v_exp_f32 v84, v84\n\t"
        "v_exp_f32 v85, v85\n\t"
        "v_exp_f32 v86, v86\n\t"
        "v_exp_f32 v87, v87\n\t"
        "v_cvt_pk_bf16_f32 v152, v80, v81\n\t"
        "v_cvt_pk_bf16_f32 v153, v82, v83\n\t"
        "v_mfma_f32_32x32x16_bf16 v[32:47], v[200:203], v[144:147], v[32:47]\n\t"
        "v_exp_f32 v88, v88\n\t"
        "v_exp_f32 v89, v89\n\t"
        "v_exp_f32 v90, v90\n\t"
        "v_exp_f32 v91, v91\n\t"
        "v_cvt_pk_bf16_f32 v154, v84, v85\n\t"
        "v_cvt_pk_bf16_f32 v155, v86, v87\n\t"
        "v_mfma_f32_32x32x16_bf16 v[32:47], v[204:207], v[148:151], v[32:47]\n\t"
        "v_exp_f32 v92, v92\n\t"
        "v_exp_f32 v93, v93\n\t"
        "v_exp_f32 v94, v94\n\t"
        "v_exp_f32 v95, v95\n\t"
        "v_cvt_pk_bf16_f32 v156, v88, v89\n\t"
        "v_cvt_pk_bf16_f32 v157, v90, v91\n\t"
        "v_cvt_pk_bf16_f32 v158, v92, v93\n\t"
        "v_cvt_pk_bf16_f32 v159, v94, v95\n\t"
        "v_mfma_f32_32x32x16_bf16 v[80:95], v[208:211], v[120:123], v[96:111]\n\t"
        "v_exp_f32 v64, v64\n\t"
        "v_exp_f32 v65, v65\n\t"
        "v_exp_f32 v66, v66\n\t"
        "v_exp_f32 v67, v67\n\t"
        "v_mfma_f32_32x32x16_bf16 v[80:95], v[212:215], v[124:127], v[80:95]\n\t"
        "v_exp_f32 v68, v68\n\t"
        "v_exp_f32 v69, v69\n\t"
        "v_exp_f32 v70, v70\n\t"
        "v_exp_f32 v71, v71\n\t"
        "v_cvt_pk_bf16_f32 v144, v64, v65\n\t"
        "v_cvt_pk_bf16_f32 v145, v66, v67\n\t"
        "v_mfma_f32_32x32x16_bf16 v[48:63], v[200:203], v[152:155], v[48:63]\n\t"
        "v_exp_f32 v72, v72\n\t"
        "v_exp_f32 v73, v73\n\t"
        "v_exp_f32 v74, v74\n\t"
        "v_exp_f32 v75, v75\n\t"
        "v_cvt_pk_bf16_f32 v146, v68, v69\n\t"
        "v_cvt_pk_bf16_f32 v147, v70, v71\n\t"
        "v_mfma_f32_32x32x16_bf16 v[48:63], v[204:207], v[156:159], v[48:63]\n\t"
        "v_exp_f32 v76, v76\n\t"
        "v_exp_f32 v77, v77\n\t"
        "v_exp_f32 v78, v78\n\t"
        "v_exp_f32 v79, v79\n\t"
        "v_cvt_pk_bf16_f32 v148, v72, v73\n\t"
        "v_cvt_pk_bf16_f32 v149, v74, v75\n\t"
        "v_cvt_pk_bf16_f32 v150, v76, v77\n\t"
        "v_cvt_pk_bf16_f32 v151, v78, v79\n\t"
        "v_mfma_f32_32x32x16_bf16 v[64:79], v[208:211], v[128:131], v[96:111]\n\t"
        "buffer_load_dwordx4 v[192:195], v230, %[rs], %[sb] offen sc1\n\t"
        "buffer_load_dwordx4 v[196:199], v230, %[rs], %[sb] offen offset:1024 sc1\n\t"
        "buffer_load_dwordx4 v[200:203], v231, %[rs], %[sb] offen sc1\n\t"
        "buffer_load_dwordx4 v[204:207], v232, %[rs], %[sb] offen sc1\n\t"
        "s_sub_u32 %[cnt], %[cnt], 1\n\t"
        "s_cmp_eq_u32 %[cnt], 0\n\t"
        "v_exp_f32 v80, v80\n\t"
        "v_exp_f32 v81, v81\n\t"
        "v_exp_f32 v82, v82\n\t"
        "v_exp_f32 v83, v83\n\t"
        "v_mfma_f32_32x32x16_bf16 v[64:79], v[212:215], v[132:135], v[64:79]\n\t"
        "v_exp_f32 v84, v84\n\t"
        "v_exp_f32 v85, v85\n\t"
        "v_exp_f32 v86, v86\n\t"
        "v_exp_f32 v87, v87\n\t"
        "v_cvt_pk_bf16_f32 v152, v80, v81\n\t"
        "v_cvt_pk_bf16_f32 v153, v82, v83\n\t"
        "v_mfma_f32_32x32x16_bf16 v[0:15], v[216:219], v[144:147], v[0:15]\n\t"
        "v_exp_f32 v88, v88\n\t"
        "v_exp_f32 v89, v89\n\t"
        "v_exp_f32 v90, v90\n\t"
        "v_exp_f32 v91, v91\n\t"
        "v_cvt_pk_bf16_f32 v154, v84, v85\n\t"
        "v_cvt_pk_bf16_f32 v155, v86, v87\n\t"
        "v_mfma_f32_32x32x16_bf16 v[0:15], v[220:223], v[148:151], v[0:15]\n\t"
        "v_exp_f32 v92, v92\n\t"
        "v_exp_f32 v93, v93\n\t"
        "v_exp_f32 v94, v94\n\t"
        "v_exp_f32 v95, v95\n\t"
        "v_cvt_pk_bf16_f32 v156, v88, v89\n\t"
        "v_cvt_pk_bf16_f32 v157, v90, v91\n\t"
        "v_cvt_pk_bf16_f32 v158, v92, v93\n\t"
        "v_cvt_pk_bf16_f32 v159, v94, v95\n\t"
        "s_cbranch_scc1 TAIL%=\n\t"
        "v_mfma_f32_32x32x16_bf16 v[80:95], v[208:211], v[136:139], v[96:111]\n\t"
        "v_exp_f32 v64, v64\n\t"
        "v_exp_f32 v65, v65\n\t"
        "v_exp_f32 v66, v66\n\t"
        "v_exp_f32 v67, v67\n\t"
        "v_mfma_f32_32x32x16_bf16 v[80:95], v[212:215], v[140:143], v[80:95]\n\t"
        "v_exp_f32 v68, v68\n\t"
        "v_exp_f32 v69, v69\n\t"
        "v_exp_f32 v70, v70\n\t"
        "v_exp_f32 v71, v71\n\t"
        "v_cvt_pk_bf16_f32 v144, v64, v65\n\t"
        "v_cvt_pk_bf16_f32 v145, v66, v67\n\t"
        "v_mfma_f32_32x32x16_bf16 v[16:31], v[216:219], v[152:155], v[16:31]\n\t"
        "v_exp_f32 v72, v72\n\t"
        "v_exp_f32 v73, v73\n\t"
        "v_exp_f32 v74, v74\n\t"
        "v_exp_f32 v75, v75\n\t"
        "v_cvt_pk_bf16_f32 v146, v68, v69\n\t"
        "v_cvt_pk_bf16_f32 v147, v70, v71\n\t"
        "v_mfma_f32_32x32x16_bf16 v[16:31], v[220:223], v[156:159], v[16:31]\n\t"
        "v_exp_f32 v76, v76\n\t"
        "v_exp_f32 v77, v77\n\t"
        "v_exp_f32 v78, v78\n\t"
        "v_exp_f32 v79, v79\n\t"
        "v_cvt_pk_bf16_f32 v148, v72, v73\n\t"
        "v_cvt_pk_bf16_f32 v149, v74, v75\n\t"
        "v_cvt_pk_bf16_f32 v150, v76, v77\n\t"
        "v_cvt_pk_bf16_f32 v151, v78, v79\n\t"
        "s_waitcnt vmcnt(8)\n\t"
        "v_mfma_f32_32x32x16_bf16 v[64:79], v[160:163], v[112:115], v[96:111]\n\t"
        "v_exp_f32 v80, v80\n\t"
        "v_exp_f32 v81, v81\n\t"
        "v_exp_f32 v82, v82\n\t"
        "v_exp_f32 v83, v83\n\t"
        "v_mfma_f32_32x32x16_bf16 v[64:79], v[164:167], v[116:119], v[64:79]\n\t"
        "v_exp_f32 v84, v84\n\t"
        "v_exp_f32 v85, v85\n\t"
        "v_exp_f32 v86, v86\n\t"
        "v_exp_f32 v87, v87\n\t"
        "v_cvt_pk_bf16_f32 v152, v80, v81\n\t"
        "v_cvt_pk_bf16_f32 v153, v82, v83\n\t"
        "v_mfma_f32_32x32x16_bf16 v[32:47], v[216:219], v[144:147], v[32:47]\n\t"
        "v_exp_f32 v88, v88\n\t"
        "v_exp_f32 v89, v89\n\t"
        "v_exp_f32 v90, v90\n\t"
        "v_exp_f32 v91, v91\n\t"
        "v_cvt_pk_bf16_f32 v154, v84, v85\n\t"
        "v_cvt_pk_bf16_f32 v155, v86, v87\n\t"
        "v_mfma_f32_32x32x16_bf16 v[32:47], v[220:223], v[148:151], v[32:47]\n\t"
        "v_exp_f32 v92, v92\n\t"
        "v_exp_f32 v93, v93\n\t"
        "v_exp_f32 v94, v94\n\t"
        "v_exp_f32 v95, v95\n\t"
        "v_cvt_pk_bf16_f32 v156, v88, v89\n\t"
        "v_cvt_pk_bf16_f32 v157, v90, v91\n\t"
        "v_cvt_pk_bf16_f32 v158, v92, v93\n\t"
        "v_cvt_pk_bf16_f32 v159, v94, v95\n\t"
        "s_branch LOOP%=\n\t"
        "TAIL%=:\n\t"
        "v_mfma_f32_32x32x16_bf16 v[80:95], v[208:211], v[136:139], v[96:111]\n\t"
        "v_exp_f32 v64, v64\n\t"
        "v_exp_f32 v65, v65\n\t"
        "v_exp_f32 v66, v66\n\t"
        "v_exp_f32 v67, v67\n\t"
        "v_mfma_f32_32x32x16_bf16 v[80:95], v[212:215], v[140:143], v[80:95]\n\t"
        "ds_write_b128 v240, v[0:3] offset:0\n\t"
        "ds_write_b128 v240, v[4:7] offset:1024\n\t"
        "ds_write_b128 v240, v[8:11] offset:2048\n\t"
        "ds_write_b128 v240, v[12:15] offset:3072\n\t"
        "v_exp_f32 v68, v68\n\t"
        "v_exp_f32 v69, v69\n\t"
        "v_exp_f32 v70, v70\n\t"
        "v_exp_f32 v71, v71\n\t"
        "v_cvt_pk_bf16_f32 v144, v64, v65\n\t"
        "v_cvt_pk_bf16_f32 v145, v66, v67\n\t"
        "v_mfma_f32_32x32x16_bf16 v[16:31], v[216:219], v[152:155], v[16:31]\n\t"
        "v_exp_f32 v72, v72\n\t"
        "v_exp_f32 v73, v73\n\t"
        "v_exp_f32 v74, v74\n\t"
        "v_exp_f32 v75, v75\n\t"
        "v_cvt_pk_bf16_f32 v146, v68, v69\n\t"
        "v_cvt_pk_bf16_f32 v147, v70, v71\n\t"
        "v_mfma_f32_32x32x16_bf16 v[16:31], v[220:223], v[156:159], v[16:31]\n\t"
        "v_exp_f32 v76, v76\n\t"
        "v_exp_f32 v77, v77\n\t"
        "v_exp_f32 v78, v78\n\t"
        "v_exp_f32 v79, v79\n\t"
        "v_cvt_pk_bf16_f32 v148, v72, v73\n\t"
        "v_cvt_pk_bf16_f32 v149, v74, v75\n\t"
        "v_cvt_pk_bf16_f32 v150, v76, v77\n\t"
        "v_cvt_pk_bf16_f32 v151, v78, v79\n\t"
        "v_exp_f32 v80, v80\n\t"
        "v_exp_f32 v81, v81\n\t"
        "v_exp_f32 v82, v82\n\t"
        "v_exp_f32 v83, v83\n\t"
        "v_exp_f32 v84, v84\n\t"
        "v_exp_f32 v85, v85\n\t"
        "v_exp_f32 v86, v86\n\t"
        "v_exp_f32 v87, v87\n\t"
        "v_cvt_pk_bf16_f32 v152, v80, v81\n\t"
        "v_cvt_pk_bf16_f32 v153, v82, v83\n\t"
        "v_mfma_f32_32x32x16_bf16 v[32:47], v[216:219], v[144:147], v[32:47]\n\t"
        "ds_write_b128 v240, v[16:19] offset:4096\n\t"
        "ds_write_b128 v240, v[20:23] offset:5120\n\t"
        "ds_write_b128 v240, v[24:27] offset:6144\n\t"
        "ds_write_b128 v240, v[28:31] offset:7168\n\t"
        "v_exp_f32 v88, v88\n\t"
        "v_exp_f32 v89, v89\n\t"
        "v_exp_f32 v90, v90\n\t"
        "v_exp_f32 v91, v91\n\t"
        "v_cvt_pk_bf16_f32 v154, v84, v85\n\t"
        "v_cvt_pk_bf16_f32 v155, v86, v87\n\t"
        "v_mfma_f32_32x32x16_bf16 v[32:47], v[220:223], v[148:151], v[32:47]\n\t"
        "v_exp_f32 v92, v92\n\t"
        "v_exp_f32 v93, v93\n\t"
        "v_exp_f32 v94, v94\n\t"
        "v_exp_f32 v95, v95\n\t"
        "v_cvt_pk_bf16_f32 v156, v88, v89\n\t"
        "v_cvt_pk_bf16_f32 v157, v90, v91\n\t"
        "v_cvt_pk_bf16_f32 v158, v92, v93\n\t"
        "v_cvt_pk_bf16_f32 v159, v94, v95\n\t"
        "s_nop 1\n\t"
        "v_mfma_f32_32x32x16_bf16 v[48:63], v[216:219], v[152:155], v[48:63]\n\t"
        "v_mfma_f32_32x32x16_bf16 v[48:63], v[220:223], v[156:159], v[48:63]\n\t"
        "s_nop 3\n\t"
        "ds_write_b128 v240, v[32:35] offset:8192\n\t"
        "ds_write_b128 v240, v[36:39] offset:9216\n\t"
        "ds_write_b128 v240, v[40:43] offset:10240\n\t"
        "ds_write_b128 v240, v[44:47] offset:11264\n\t"
        "s_waitcnt vmcnt(0) lgkmcnt(0)\n\t"
        "s_barrier\n\t"
        "s_nop 7\n\t"
        : "={v[48:63]}"(acc), [sb] "+s"(sb), [cnt] "+s"(cnt), "+{v236}"(q2a), "+{v237}"(kba), "+{v238}"(ordn), "={v[136:139]}"(qo0), "={v[140:143]}"(qo1)
        : "{v224}"(vok + 0 * TS), "{v225}"(vov + 0 * TS), "{v226}"(vov + 2 * VROW + 0 * TS), "{v227}"(vok + 1 * TS), "{v228}"(vov + 1 * TS), "{v229}"(vov + 2 * VROW + 1 * TS), "{v230}"(vok + 2 * TS), "{v231}"(vov + 2 * TS), "{v232}"(vov + 2 * VROW + 2 * TS), "{v233}"(vok + 3 * TS), "{v234}"(vov + 3 * TS), "{v235}"(vov + 2 * VROW + 3 * TS),
          "{v240}"(puta), "{v241}"(qxa[0]), "{v242}"(qxa[1]), "{v243}"(qxa[2]), "{v244}"(qxa[3]), [rs] "s"(rsKVr)
        : "v0", "v1", "v2", "v3", "v4", "v5", "v6", "v7", "v8", "v9", "v10", "v11", "v12", "v13", "v14", "v15", "v16", "v17", "v18", "v19", "v20", "v21", "v22", "v23", "v24", "v25", "v26", "v27", "v28", "v29", "v30", "v31", "v32", "v33", "v34", "v35", "v36", "v37", "v38", "v39", "v40", "v41", "v42", "v43", "v44", "v45", "v46", "v47", "v64", "v65", "v66", "v67", "v68", "v69", "v70", "v71", "v72", "v73", "v74", "v75", "v76", "v77", "v78", "v79", "v80", "v81", "v82", "v83", "v84", "v85", "v86", "v87", "v88", "v89", "v90", "v91", "v92", "v93", "v94", "v95", "v96", "v97", "v98", "v99", "v100", "v101", "v102", "v103", "v104", "v105", "v106", "v107", "v108", "v109", "v110", "v111", "v112", "v113", "v114", "v115", "v116", "v117", "v118", "v119", "v120", "v121", "v122", "v123", "v124", "v125", "v126", "v127", "v128", "v129", "v130", "v131", "v132", "v133", "v134", "v135", "v144", "v145", "v146", "v147", "v148", "v149", "v150", "v151", "v152", "v153", "v154", "v155", "v156", "v157", "v158", "v159", "v160", "v161", "v162", "v163", "v164", "v165", "v166", "v167", "v168", "v169", "v170", "v171", "v172", "v173", "v174", "v175", "v176", "v177", "v178", "v179", "v180", "v181", "v182", "v183", "v184", "v185", "v186", "v187", "v188", "v189", "v190", "v191", "v192", "v193", "v194", "v195", "v196", "v197", "v198", "v199", "v200", "v201", "v202", "v203", "v204", "v205", "v206", "v207", "v208", "v209", "v210", "v211", "v212", "v213", "v214", "v215", "v216", "v217", "v218", "v219", "v220", "v221", "v222", "v223", "v239", "scc", "memory");
    }
#pragma unroll
    for (int s = 0; s < 3; ++s) {
        LAS const char* sp = lds + L_CB + (((w - 1 - s) & 3) * 3 + s) * 4096 + lane * 16;
#pragma unroll
        for (int g = 0; g < 4; ++g) {
            const f32x4 v = *(LAS const f32x4*)(sp + g * 1024);
            acc[4 * g] += v[0]; acc[4 * g + 1] += v[1]; acc[4 * g + 2] += v[2]; acc[4 * g + 3] += v[3];
        }
    }
    float l = half_hi(acc[12]);
    if (__builtin_expect(__any(!(l >= L_MIN && l <= L_MAX)), 0)) {
        const f32x16 zero = {};
        float m = -__builtin_inff();
#pragma unroll 1
        for (int t = 0; t < 64; ++t) {
            f32x16 s = MFMA(asbf(ld16_wt(rsKVr, vok, t * TS)), asbf(qo0), zero);
            s = MFMA(asbf(ld16_wt(rsKVr, vok + 1024, t * TS)), asbf(qo1), s);
#pragma unroll
            for (int i = 0; i < 16; ++i) m = __builtin_fmaxf(m, s[i]);
        }
        m = __builtin_fmaxf(half_lo(m), half_hi(m));
        acc = zero;
#pragma unroll 1
        for (int t = 0; t < 64; ++t) {
            f32x16 s = MFMA(asbf(ld16_wt(rsKVr, vok, t * TS)), asbf(qo0), zero);
            s = MFMA(asbf(ld16_wt(rsKVr, vok + 1024, t * TS)), asbf(qo1), s);
#pragma unroll
            for (int i = 0; i < 16; ++i) s[i] = __builtin_amdgcn_exp2f(s[i] - m);
            acc = MFMA(asbf(ld16_wt(rsKVr, vov, t * TS)), asbf(pack8(s, 0)), acc);
            acc = MFMA(asbf(ld16_wt(rsKVr, vov + 2 * VROW, t * TS)), asbf(pack8(s, 1)), acc);
        }
        l = half_hi(acc[12]);
    }
    {
        const float rl = __builtin_amdgcn_rcpf(l);
#pragma unroll
        for (int i = 0; i < 16; ++i) acc[i] = (i >= 12 && h) ? 0.f : acc[i] * rl;
        const unsigned so = (kt * NH + hd) * 2048;
        u32x4 p0 = pack8(acc, 0), p1 = pack8(acc, 1);
#pragma unroll
        for (int i = 0; i < 4; ++i) { p0[i] = min(p0[i], POISON - 1); p1[i] = min(p1[i], POISON - 1); }
        st16_wt(p0, rsOI, lane * 16, so);
        st16_wt(p1, rsOI, lane * 16 + 1024, so);
    }
    BAR();

    bf16x8 wf[32];
    LAS const char* ob = lds + L_CB + lane * 16;
    const __amdgpu_buffer_rsrc_t rsOut = mkrs(out, SEQ * DM * 2);
    unsigned oo = (q32 * 32 + w * 8 + (lane >> 4)) * (DM * 2) + cg * 256 + (lane & 15) * 16;
    asm volatile("" : "+v"(ob), "+v"(oo) : "s"(rsOut));
    {
        LAS const char* wb = lds + L_WO + (4 * h + q4) * 256 + ((w ^ q4) * 64) + g16 * 32 + p4 * 8;
#pragma unroll
        for (int k = 0; k < 32; ++k) {
            const int ro = ((k >> 1) * DH + (k & 1) * 16) * 256;
            wf[k] = cat(vtr(wb + ro), vtr(wb + ro + 2048 - (k == 31 ? h * 1024 : 0)));
        }
    }
    asm volatile("s_waitcnt lgkmcnt(0)\n\ts_barrier\nMHA_CODE_END:" ::: "memory");
    LAS char* tb = lds + L_CB + 32768;
    if (cg < 3 || w < 2) {
        f32x16 c = {};
        bf16x8 of[3][4];
#define RDO(i) do { _Pragma("unroll") for (int j = 0; j < 4; ++j) of[(i) % 3][j] = *(LAS const bf16x8*)(ob + ((i) * 4 + j) * 1024); } while (0)
        RDO(0); RDO(1); SBAR();
#pragma unroll
        for (int i = 0; i < 8; ++i) {
            if (i + 2 < 8) RDO(i + 2);
            SBAR();
#pragma unroll
            for (int j = 0; j < 4; ++j) c = MFMA(wf[i * 4 + j], of[i % 3][j], c);
            SBAR();
        }
#undef RDO
#pragma unroll
        for (int g = 0; g < 4; ++g)
            *(LAS u32x2*)(tb + r * 272 + w * 64 + g * 16 + h * 8) = (u32x2){cvtpk(c[4 * g], c[4 * g + 1]), cvtpk(c[4 * g + 2], c[4 * g + 3])};
    }
    BAR();
    {
        const unsigned rr = w * 8 + (lane >> 4), cc = (lane & 15) * 16;
        const u32x4 v0 = *(LAS const u32x4*)(tb + rr * 272 + cc), v1 = *(LAS const u32x4*)(tb + (rr + 4) * 272 + cc);
        if (cg < 3 || cc < 128) {
            st16_wt(v0, rsOut, oo, 0);
            st16_wt(v1, rsOut, oo + 4 * DM * 2, 0);
        }
    }
}

extern "C" void kernel_launch(void* const* d_in, const int* in_sizes, int n_in, void* d_out, int out_size, void* d_ws, size_t ws_size,
                              hipStream_t stream) {
    (void)hipFuncSetAttribute((const void*)mha_kernel, hipFuncAttributeMaxDynamicSharedMemorySize, LDS_BYTES);
    mha_kernel<<<256, 512, LDS_BYTES, stream>>>((const char*)d_in[0], (const char*)d_in[1], (const char*)d_in[2], (char*)d_out, (char*)d_ws);
}
```
